```python
import torch

def _softmax(x, axis=-1):
    return torch.softmax(x, dim=axis)
def _gelu(x):
    return torch.nn.functional.gelu(x, approximate="tanh")
def _silu(x):
    return torch.nn.functional.silu(x)
def _sigmoid(x):
    return torch.sigmoid(x)
def _layernorm(x, axis=-1, eps=1e-5):
    m = x.mean(dim=axis, keepdim=True)
    v = x.var(dim=axis, keepdim=True, unbiased=False)
    return (x - m) / torch.sqrt(v + eps)
def _scatter_add(tgt, idx, src, axis=0):
    return tgt.clone().index_add_(axis, idx, src)
def _segment_sum(src, seg, n):
    out = torch.zeros((n,) + src.shape[1:], dtype=src.dtype)
    return out.index_add_(0, seg, src)
def _pad(x, pw):
    flat = tuple(v for pair in reversed(pw) for v in pair)
    return torch.nn.functional.pad(x, flat)

kernel_name = 'sequential_preLN__gqa_block_cached__swiglu_ffn__decode'

def setup_inputs():

    _w = (lambda a: (lambda t: t.to(torch.bfloat16) if t.is_floating_point() else t)(torch.as_tensor(a)))
    def _A_setup():
        B, Sq, D = 2, 1, 512
        Hq, Hkv, Sk, Dh = 32, 8, 8192, 128
        assert Hq % Hkv == 0, (Hq, Hkv)
        return {
            "x": _w((lambda *s: torch.randn(*s, dtype=torch.bfloat16) * 0.05)(B, Sq, D)),
            "k_cache": _w((lambda *s: torch.randn(*s, dtype=torch.bfloat16) * 3.2)(B, Hkv, Sk, Dh)),
            "v_cache": _w((lambda *s: torch.randn(*s, dtype=torch.bfloat16) * 0.4)(B, Hkv, Sk, Dh)),
            "wq": _w((lambda *s: torch.randn(*s, dtype=torch.bfloat16) * 3.2)(D, Hq * Dh) * (D ** -0.5)),
            "wo": _w((lambda *s: torch.randn(*s, dtype=torch.bfloat16) * 0.4)(Hq * Dh, D) * ((Hq * Dh) ** -0.5)),
        }
    _A_in = _A_setup()

    def _B_setup():
        B, S, D, F = 2, 1, 512, 1792
        return {
            "x": _w((lambda *s: torch.randn(*s, dtype=torch.bfloat16) * 0.05)(B, S, D)),
            "w_gate": _w((lambda *s: torch.randn(*s, dtype=torch.bfloat16) * 0.05)(D, F)),
            "w_up": _w((lambda *s: torch.randn(*s, dtype=torch.bfloat16) * 0.05)(D, F)),
            "w_down": _w((lambda *s: torch.randn(*s, dtype=torch.bfloat16) * 0.05)(F, D)),
        }
    _B_in = _B_setup()

    return {
        "A_x": _A_in["x"],
        "A_k_cache": _A_in["k_cache"],
        "A_v_cache": _A_in["v_cache"],
        "A_wq": _A_in["wq"],
        "A_wo": _A_in["wo"],
        "B_x": _B_in["x"],
        "B_w_gate": _B_in["w_gate"],
        "B_w_up": _B_in["w_up"],
        "B_w_down": _B_in["w_down"],
    }

def reference(A_x, A_k_cache, A_v_cache, A_wq, A_wo, B_x, B_w_gate, B_w_up, B_w_down):
    def _A_ref(x, k_cache, v_cache, wq, wo):
        B, Sq, D = x.shape
        Hq, Dh = 32, 128
        Hkv = k_cache.shape[1]
        rep = Hq // Hkv
        q = (lambda x, *d: x.permute(*d))((x @ wq).reshape(B, Sq, Hq, Dh), 0, 2, 1, 3)
        k = (lambda x, n, axis: torch.repeat_interleave(x, n, dim=axis))(k_cache, rep, axis=1)
        v = (lambda x, n, axis: torch.repeat_interleave(x, n, dim=axis))(v_cache, rep, axis=1)
        scores = torch.einsum("bhqd,bhkd->bhqk", q, k) * (Dh ** -0.5)
        p = _softmax(scores, axis=-1)
        o = torch.einsum("bhqk,bhkd->bhqd", p, v)
        return (lambda x, *d: x.permute(*d))(o, 0, 2, 1, 3).reshape(B, Sq, Hq * Dh) @ wo

    def _B_ref(x, w_gate, w_up, w_down):
        g = _silu(x @ w_gate)
        u = x @ w_up
        return (g * u) @ w_down

    x = A_x
    h = x + _A_ref(_layernorm(x, axis=-1), A_k_cache, A_v_cache, A_wq, A_wo)
    h2 = h + _B_ref(_layernorm(h, axis=-1), B_w_gate, B_w_up, B_w_down)
    return h2
```

```cpp
#include <hip/hip_runtime.h>
#include <hip/hip_bf16.h>
#include <stdint.h>

typedef unsigned u32x4 __attribute__((ext_vector_type(4)));
typedef float f32x4 __attribute__((ext_vector_type(4)));

constexpr int D = 512, HQ = 32, HKV = 8, SK = 8192, DH = 128, F = 1792, NB = 2;
constexpr int NSPLIT = 16, KPS = SK / NSPLIT;

__device__ __forceinline__ float bflo(unsigned u) { return __builtin_bit_cast(float, u << 16); }
__device__ __forceinline__ float bfhi(unsigned u) { return __builtin_bit_cast(float, u & 0xffff0000u); }

__device__ void ln512(const float* src, float* dst, float* red) {
  int t = threadIdx.x;
  float a = src[t], b = src[t + 256];
  float s = a + b;
  for (int o = 32; o; o >>= 1) s += __shfl_xor(s, o);
  if ((t & 63) == 0) red[t >> 6] = s;
  __syncthreads();
  float mean = (red[0] + red[1] + red[2] + red[3]) * (1.f / 512.f);
  __syncthreads();
  float da = a - mean, db = b - mean;
  float v = da * da + db * db;
  for (int o = 32; o; o >>= 1) v += __shfl_xor(v, o);
  if ((t & 63) == 0) red[t >> 6] = v;
  __syncthreads();
  float var = (red[0] + red[1] + red[2] + red[3]) * (1.f / 512.f);
  float r = 1.f / sqrtf(var + 1e-5f);
  dst[t] = da * r;
  dst[t + 256] = db * r;
  __syncthreads();
}

template <int KD>
__device__ void gemv64(const float* act  , const unsigned short* W, int N, int n0, float* scratch  , float* res) {
  int t = threadIdx.x, c = t & 7, rg = t >> 3;
  float acc[2][8];
#pragma unroll
  for (int j = 0; j < 8; ++j) acc[0][j] = acc[1][j] = 0.f;
#pragma unroll 4
  for (int k = rg; k < KD; k += 32) {
    u32x4 w = *reinterpret_cast<const u32x4*>(W + (size_t)k * N + n0 + c * 8);
    float a0 = act[k], a1 = act[KD + k];
#pragma unroll
    for (int j = 0; j < 4; ++j) {
      float lo = bflo(w[j]), hi = bfhi(w[j]);
      acc[0][2 * j] += a0 * lo; acc[0][2 * j + 1] += a0 * hi;
      acc[1][2 * j] += a1 * lo; acc[1][2 * j + 1] += a1 * hi;
    }
  }
#pragma unroll
  for (int j = 0; j < 8; ++j) { scratch[(rg * 2 + 0) * 64 + c * 8 + j] = acc[0][j]; scratch[(rg * 2 + 1) * 64 + c * 8 + j] = acc[1][j]; }
  __syncthreads();
  if (t < 128) {
    float s = 0.f;
    for (int r = 0; r < 32; ++r) s += scratch[r * 128 + t];
    res[t] = s;
  }
  __syncthreads();
}

__global__ void __launch_bounds__(256) k_q(const unsigned short* x, const unsigned short* wq, float* q) {
  __shared__ float xs[2][512], xn[2][512], red[4], scratch[32 * 128], res[128];
  int t = threadIdx.x;
  for (int i = t; i < 1024; i += 256) xs[i >> 9][i & 511] = bflo(x[i]);
  __syncthreads();
  ln512(xs[0], xn[0], red);
  ln512(xs[1], xn[1], red);
  gemv64<512>(&xn[0][0], wq, HQ * DH, blockIdx.x * 64, scratch, res);
  if (t < 128) q[(t >> 6) * 4096 + blockIdx.x * 64 + (t & 63)] = res[t];
}

__global__ void __launch_bounds__(256) k_attn(const float* q, const unsigned short* kc, const unsigned short* vc, float* po, float* pml) {
  __shared__ float qs[4][128], sc[4][KPS], red[4][4], ored[16][4][128];
  int s = blockIdx.x, kvh = blockIdx.y, b = blockIdx.z, t = threadIdx.x, lane = t & 63, wave = t >> 6;
  for (int i = t; i < 512; i += 256) qs[i >> 7][i & 127] = q[b * 4096 + kvh * 512 + i];
  __syncthreads();
  const unsigned short* kb = kc + ((size_t)(b * HKV + kvh) * SK + s * KPS) * DH;
  const unsigned short* vb = vc + ((size_t)(b * HKV + kvh) * SK + s * KPS) * DH;
  int c = t & 15, r = t >> 4;
  float qr[4][8];
#pragma unroll
  for (int g = 0; g < 4; ++g)
#pragma unroll
    for (int j = 0; j < 8; ++j) qr[g][j] = qs[g][c * 8 + j];
#pragma unroll 4
  for (int k = r; k < KPS; k += 16) {
    u32x4 w = *reinterpret_cast<const u32x4*>(kb + (size_t)k * DH + c * 8);
    float d[4];
#pragma unroll
    for (int g = 0; g < 4; ++g) {
      float a = 0.f;
#pragma unroll
      for (int j = 0; j < 4; ++j) a += qr[g][2 * j] * bflo(w[j]) + qr[g][2 * j + 1] * bfhi(w[j]);
      for (int o = 8; o; o >>= 1) a += __shfl_xor(a, o);
      d[g] = a;
    }
    if (c < 4) sc[c][k] = (c == 0 ? d[0] : c == 1 ? d[1] : c == 2 ? d[2] : d[3]) * 0.088388347648318440f;
  }
  __syncthreads();
  {
    int g = wave;
    float m = -INFINITY;
    for (int k = lane; k < KPS; k += 64) m = fmaxf(m, sc[g][k]);
    for (int o = 32; o; o >>= 1) m = fmaxf(m, __shfl_xor(m, o));
    float l = 0.f;
    for (int k = lane; k < KPS; k += 64) { float p = __expf(sc[g][k] - m); sc[g][k] = p; l += p; }
    for (int o = 32; o; o >>= 1) l += __shfl_xor(l, o);
    if (lane == 0) { size_t idx = ((size_t)(b * HQ + kvh * 4 + g) * NSPLIT + s) * 2; pml[idx] = m; pml[idx + 1] = l; }
  }
  __syncthreads();
  float acc[4][8];
#pragma unroll
  for (int g = 0; g < 4; ++g)
#pragma unroll
    for (int j = 0; j < 8; ++j) acc[g][j] = 0.f;
#pragma unroll 4
  for (int k = r; k < KPS; k += 16) {
    u32x4 w = *reinterpret_cast<const u32x4*>(vb + (size_t)k * DH + c * 8);
#pragma unroll
    for (int g = 0; g < 4; ++g) {
      float p = sc[g][k];
#pragma unroll
      for (int j = 0; j < 4; ++j) { acc[g][2 * j] += p * bflo(w[j]); acc[g][2 * j + 1] += p * bfhi(w[j]); }
    }
  }
#pragma unroll
  for (int g = 0; g < 4; ++g)
#pragma unroll
    for (int j = 0; j < 8; ++j) ored[r][g][c * 8 + j] = acc[g][j];
  __syncthreads();
  for (int i = t; i < 512; i += 256) {
    float a = 0.f;
    for (int rr = 0; rr < 16; ++rr) a += ored[rr][i >> 7][i & 127];
    po[((size_t)(b * HQ + kvh * 4 + (i >> 7)) * NSPLIT + s) * DH + (i & 127)] = a;
  }
}

__global__ void __launch_bounds__(128) k_comb(const float* po, const float* pml, float* o) {
  int bh = blockIdx.x, d = threadIdx.x;
  float M = -INFINITY;
  for (int s = 0; s < NSPLIT; ++s) M = fmaxf(M, pml[(bh * NSPLIT + s) * 2]);
  float L = 0.f, a = 0.f;
  for (int s = 0; s < NSPLIT; ++s) {
    float w = __expf(pml[(bh * NSPLIT + s) * 2] - M);
    L += w * pml[(bh * NSPLIT + s) * 2 + 1];
    a += w * po[((size_t)bh * NSPLIT + s) * DH + d];
  }
  o[bh * DH + d] = a / L;
}

__global__ void __launch_bounds__(256) k_wo(const float* o, const unsigned short* wo, float* hp) {
  __shared__ float a[2][512], scratch[32 * 128], res[128];
  int t = threadIdx.x, ks = blockIdx.y;
  for (int i = t; i < 1024; i += 256) a[i >> 9][i & 511] = o[(i >> 9) * 4096 + ks * 512 + (i & 511)];
  __syncthreads();
  gemv64<512>(&a[0][0], wo + (size_t)ks * 512 * D, D, blockIdx.x * 64, scratch, res);
  if (t < 128) hp[(ks * 2 + (t >> 6)) * 512 + blockIdx.x * 64 + (t & 63)] = res[t];
}

__global__ void __launch_bounds__(256) k_gu(const unsigned short* x, const float* hp, const unsigned short* wg, const unsigned short* wu, float* act, float* hout) {
  __shared__ float hs[2][512], hn[2][512], red[4], scratch[32 * 128], resg[128], resu[128];
  int t = threadIdx.x;
  for (int i = t; i < 1024; i += 256) {
    float s = bflo(x[i]);
    for (int ks = 0; ks < 8; ++ks) s += hp[ks * 1024 + i];
    hs[i >> 9][i & 511] = s;
    if (blockIdx.x == 0) hout[i] = s;
  }
  __syncthreads();
  ln512(hs[0], hn[0], red);
  ln512(hs[1], hn[1], red);
  gemv64<512>(&hn[0][0], wg, F, blockIdx.x * 64, scratch, resg);
  gemv64<512>(&hn[0][0], wu, F, blockIdx.x * 64, scratch, resu);
  if (t < 128) {
    float g = resg[t], u = resu[t];
    float sg = g / (1.f + __expf(-g));
    act[(t >> 6) * F + blockIdx.x * 64 + (t & 63)] = sg * u;
  }
}

__global__ void __launch_bounds__(256) k_down(const float* act, const float* h, const unsigned short* wd, __hip_bfloat16* out) {
  __shared__ float a[2][F], scratch[32 * 128], res[128];
  int t = threadIdx.x;
  for (int i = t; i < 2 * F; i += 256) (&a[0][0])[i] = act[i];
  __syncthreads();
  gemv64<F>(&a[0][0], wd, D, blockIdx.x * 64, scratch, res);
  if (t < 128) {
    int idx = (t >> 6) * 512 + blockIdx.x * 64 + (t & 63);
    out[idx] = __float2bfloat16(h[idx] + res[t]);
  }
}

extern "C" void kernel_launch(void* const* d_in, const int* in_sizes, int n_in, void* d_out, int out_size, void* d_ws, size_t ws_size,
                              hipStream_t stream) {
  const unsigned short* x = (const unsigned short*)d_in[0];
  const unsigned short* kc = (const unsigned short*)d_in[1];
  const unsigned short* vc = (const unsigned short*)d_in[2];
  const unsigned short* wq = (const unsigned short*)d_in[3];
  const unsigned short* wo = (const unsigned short*)d_in[4];
  const unsigned short* wg = (const unsigned short*)d_in[6];
  const unsigned short* wu = (const unsigned short*)d_in[7];
  const unsigned short* wd = (const unsigned short*)d_in[8];
  float* ws = (float*)d_ws;
  float* q = ws;
  float* po = q + 8192;
  float* pml = po + 64 * 16 * 128;
  float* o = pml + 64 * 16 * 2;
  float* hp = o + 8192;
  float* act = hp + 8192;
  float* h = act + 4096;
  k_q<<<64, 256, 0, stream>>>(x, wq, q);
  k_attn<<<dim3(NSPLIT, HKV, NB), 256, 0, stream>>>(q, kc, vc, po, pml);
  k_comb<<<64, 128, 0, stream>>>(po, pml, o);
  k_wo<<<dim3(8, 8), 256, 0, stream>>>(o, wo, hp);
  k_gu<<<28, 256, 0, stream>>>(x, hp, wg, wu, act, h);
  k_down<<<8, 256, 0, stream>>>(act, h, wd, (__hip_bfloat16*)d_out);
}
```

```cpp
#include <hip/hip_runtime.h>
#include <hip/hip_bf16.h>
#include <stdint.h>

namespace fk {
typedef unsigned u32x4 __attribute__((ext_vector_type(4)));
typedef unsigned u32x2 __attribute__((ext_vector_type(2)));
typedef float f32x4 __attribute__((ext_vector_type(4)));
typedef float f32x2 __attribute__((ext_vector_type(2)));
typedef short s16x4 __attribute__((ext_vector_type(4)));
typedef __bf16 bf16x8 __attribute__((ext_vector_type(8)));
typedef __bf16 bf16x2 __attribute__((ext_vector_type(2)));
typedef unsigned long long u64;
#define LAS __attribute__((address_space(3)))
#define GAS __attribute__((address_space(1)))
#define RLX_AGENT __ATOMIC_RELAXED, __HIP_MEMORY_SCOPE_AGENT

#define FK_TS 0

constexpr unsigned SENT = 0xFFFFFFFFu;
constexpr int NWG = 256, NTHR = 576;
constexpr int WS_EPOCH = 0;
constexpr int WS_QG = 4096;
constexpr int WS_ST = WS_QG + 65536;
constexpr int WS_OP = WS_ST + 8192;
constexpr int WS_HP = WS_OP + 262144;
constexpr int WS_ACT = WS_HP + 16384;
constexpr int WS_END = WS_ACT + 8192;
constexpr int WS_TS = 1 << 20;
constexpr int L_RB_O = 10240, L_RB_B = L_RB_O + 2048, L_RB_C = L_RB_B + 512, L_RB_CS = L_RB_C + 1024, L_RB_D = L_RB_CS + 512;
constexpr int L_WQ = 131072, L_Q = L_WQ + 16384, L_WM = L_Q + 1024, L_WL = L_WM + 128, L_F = L_WL + 128, L_W = L_F + 128, L_H = L_W + 512,
              L_FLAGS = L_H + 128, L_END = L_FLAGS + 64;
enum { F_Q = 0, F_STAT = 1, F_FAC = 2, F_VISS = 3, F_WTS = 4, F_H = 5, F_S1 = 6, F_S2 = 7, F_S3 = 8 };
constexpr int SPIN_G = 1 << 18;
constexpr int SPIN_L = 1 << 22;
constexpr float QSCALE = 0.088388347648318440f * 1.4426950408889634f;

__device__ __forceinline__ f32x4 mfma(u32x4 a, u32x4 b, f32x4 c) {
  return __builtin_amdgcn_mfma_f32_16x16x32_bf16(__builtin_bit_cast(bf16x8, a), __builtin_bit_cast(bf16x8, b), c, 0, 0, 0);
}
__device__ __forceinline__ unsigned pk(float lo, float hi) { bf16x2 v = {(__bf16)lo, (__bf16)hi}; return __builtin_bit_cast(unsigned, v); }
__device__ __forceinline__ unsigned nosent(unsigned x) { return x < 0xFFFFFFFEu ? x : 0xFFFFFFFEu; }
__device__ __forceinline__ float bflo(unsigned u) { return __builtin_bit_cast(float, u << 16); }
__device__ __forceinline__ float bfhi(unsigned u) { return __builtin_bit_cast(float, u & 0xffff0000u); }
__device__ __forceinline__ float x16_sum(float x) {
  auto s = __builtin_amdgcn_permlane16_swap(__float_as_uint(x), __float_as_uint(x), false, false);
  return __uint_as_float(s[0]) + __uint_as_float(s[1]);
}
__device__ __forceinline__ float x32_sum(float x) {
  auto s = __builtin_amdgcn_permlane32_swap(__float_as_uint(x), __float_as_uint(x), false, false);
  return __uint_as_float(s[0]) + __uint_as_float(s[1]);
}
__device__ __forceinline__ float x16_max(float x) {
  auto s = __builtin_amdgcn_permlane16_swap(__float_as_uint(x), __float_as_uint(x), false, false);
  return fmaxf(__uint_as_float(s[0]), __uint_as_float(s[1]));
}
__device__ __forceinline__ float x32_max(float x) {
  auto s = __builtin_amdgcn_permlane32_swap(__float_as_uint(x), __float_as_uint(x), false, false);
  return fmaxf(__uint_as_float(s[0]), __uint_as_float(s[1]));
}
__device__ __forceinline__ float rdl(float x, int l) { return __builtin_bit_cast(float, __builtin_amdgcn_readlane(__builtin_bit_cast(int, x), l)); }
template <int CTRL> __device__ __forceinline__ float dpp(float x) {
  return __builtin_bit_cast(float, __builtin_amdgcn_mov_dpp(__builtin_bit_cast(int, x), CTRL, 0xf, 0xf, true));
}
constexpr int XOR1 = 0xB1, XOR2 = 0x4E, XOR7 = 0x141;

template <int N> __device__ __forceinline__ void waitvm() { asm volatile("s_waitcnt vmcnt(%0)" ::"i"(N) : "memory"); }
__device__ __forceinline__ void dma_nt(const void* sbase, unsigned voff, unsigned ldst) {
  unsigned keep;
  asm volatile("s_mov_b32 %0, m0\n\ts_mov_b32 m0, %3\n\ts_nop 0\n\tglobal_load_lds_dwordx4 %1, %2 nt\n\ts_mov_b32 m0, %0"
               : "=&s"(keep) : "v"(voff), "s"(sbase), "s"(ldst) : "memory");
}
__device__ __forceinline__ void ldv_nt(u32x4& dst, const void* sbase, unsigned voff) {
  asm volatile("global_load_dwordx4 %0, %1, %2 nt" : "=v"(dst) : "v"(voff), "s"(sbase) : "memory");
}
__device__ __forceinline__ u32x2 trread(unsigned addr) {
  return __builtin_bit_cast(u32x2, __builtin_amdgcn_ds_read_tr16_b64_v4i16((LAS s16x4*)(uintptr_t)addr));
}
__device__ __forceinline__ u32x4 trfrag(unsigned a0, unsigned a1) { u32x2 lo = trread(a0), hi = trread(a1); return u32x4{lo[0], lo[1], hi[0], hi[1]}; }
template <typename T> __device__ __forceinline__ T lds_ld(unsigned addr) { return *(LAS T*)(uintptr_t)addr; }
template <typename T> __device__ __forceinline__ void lds_st(unsigned addr, T v) { *(LAS T*)(uintptr_t)addr = v; }
__device__ __forceinline__ void flag_set(unsigned base, int f, unsigned v, int lane) {
  asm volatile("" ::: "memory");
  if (lane == 0) *(volatile LAS unsigned*)(uintptr_t)(base + L_FLAGS + f * 4) = v;
  asm volatile("" ::: "memory");
}
__device__ __forceinline__ void flag_add(unsigned base, int f, int lane) {
  asm volatile("" ::: "memory");
  if (lane == 0) __hip_atomic_fetch_add((LAS unsigned*)(uintptr_t)(base + L_FLAGS + f * 4), 1u, __ATOMIC_RELAXED, __HIP_MEMORY_SCOPE_WORKGROUP);
  asm volatile("" ::: "memory");
}
template <int SLP> __device__ __forceinline__ void flag_wait(unsigned base, int f, unsigned v) {
  asm volatile("" ::: "memory");
  for (int i = 0; i < SPIN_L; ++i) {
    unsigned x = *(volatile LAS unsigned*)(uintptr_t)(base + L_FLAGS + f * 4);
    if ((unsigned)__builtin_amdgcn_readfirstlane(x) >= v) break;
    if (SLP) __builtin_amdgcn_s_sleep(SLP);
  }
  asm volatile("" ::: "memory");
}
__device__ __forceinline__ bool pend4(u32x4 v) {
  unsigned a = v[0] > v[1] ? v[0] : v[1], b = v[2] > v[3] ? v[2] : v[3];
  return (a > b ? a : b) == SENT;
}
__device__ __forceinline__ void poll_round1(u32x4 rs, u32x4& v0, unsigned o0, u64 m0) {
  u64 keep;
  asm volatile("s_mov_b64 %0, exec\n\t"
               "s_mov_b64 exec, %4\n\tbuffer_load_dwordx4 %1, %2, %3, 0 offen sc1\n\t"
               "s_mov_b64 exec, %0\n\ts_waitcnt vmcnt(0)"
               : "=&s"(keep), "+v"(v0) : "v"(o0), "s"(rs), "s"(m0) : "memory");
}
__device__ __forceinline__ void poll_round2(u32x4 rs, u32x4& v0, u32x4& v1, unsigned o0, unsigned o1, u64 m0, u64 m1) {
  u64 keep;
  asm volatile("s_mov_b64 %0, exec\n\t"
               "s_mov_b64 exec, %6\n\tbuffer_load_dwordx4 %1, %3, %5, 0 offen sc1\n\t"
               "s_mov_b64 exec, %7\n\tbuffer_load_dwordx4 %2, %4, %5, 0 offen sc1\n\t"
               "s_mov_b64 exec, %0\n\ts_waitcnt vmcnt(0)"
               : "=&s"(keep), "+v"(v0), "+v"(v1) : "v"(o0), "v"(o1), "s"(rs), "s"(m0), "s"(m1) : "memory");
}
__device__ __forceinline__ void poll_round4(u32x4 rs, u32x4 (&v)[4], const unsigned (&o)[4], const u64 (&m)[4]) {
  u64 keep;
  asm volatile("s_mov_b64 %0, exec\n\t"
               "s_mov_b64 exec, %10\n\tbuffer_load_dwordx4 %1, %5, %9, 0 offen sc1\n\t"
               "s_mov_b64 exec, %11\n\tbuffer_load_dwordx4 %2, %6, %9, 0 offen sc1\n\t"
               "s_mov_b64 exec, %12\n\tbuffer_load_dwordx4 %3, %7, %9, 0 offen sc1\n\t"
               "s_mov_b64 exec, %13\n\tbuffer_load_dwordx4 %4, %8, %9, 0 offen sc1\n\t"
               "s_mov_b64 exec, %0\n\ts_waitcnt vmcnt(0)"
               : "=&s"(keep), "+v"(v[0]), "+v"(v[1]), "+v"(v[2]), "+v"(v[3])
               : "v"(o[0]), "v"(o[1]), "v"(o[2]), "v"(o[3]), "s"(rs), "s"(m[0]), "s"(m[1]), "s"(m[2]), "s"(m[3]) : "memory");
}
__device__ __forceinline__ void poll_round7(u32x4 rs, u32x4 (&v)[7], unsigned o, const u64 (&m)[7]) {
  u64 keep;
  asm volatile("s_mov_b64 %0, exec\n\t"
               "s_mov_b64 exec, %10\n\tbuffer_load_dwordx4 %1, %8, %9, 0 offen sc1\n\t"
               "s_mov_b64 exec, %11\n\tbuffer_load_dwordx4 %2, %8, %9, 0 offen offset:128 sc1\n\t"
               "s_mov_b64 exec, %12\n\tbuffer_load_dwordx4 %3, %8, %9, 0 offen offset:256 sc1\n\t"
               "s_mov_b64 exec, %13\n\tbuffer_load_dwordx4 %4, %8, %9, 0 offen offset:384 sc1\n\t"
               "s_mov_b64 exec, %14\n\tbuffer_load_dwordx4 %5, %8, %9, 0 offen offset:512 sc1\n\t"
               "s_mov_b64 exec, %15\n\tbuffer_load_dwordx4 %6, %8, %9, 0 offen offset:640 sc1\n\t"
               "s_mov_b64 exec, %16\n\tbuffer_load_dwordx4 %7, %8, %9, 0 offen offset:768 sc1\n\t"
               "s_mov_b64 exec, %0\n\ts_waitcnt vmcnt(0)"
               : "=&s"(keep), "+v"(v[0]), "+v"(v[1]), "+v"(v[2]), "+v"(v[3]), "+v"(v[4]), "+v"(v[5]), "+v"(v[6])
               : "v"(o), "s"(rs), "s"(m[0]), "s"(m[1]), "s"(m[2]), "s"(m[3]), "s"(m[4]), "s"(m[5]), "s"(m[6]) : "memory");
}

#define TS(k) do {} while (0)

__global__ void __launch_bounds__(NTHR, 3)
fused_kernel(const unsigned char* __restrict__ x, const unsigned char* __restrict__ kc, const unsigned char* __restrict__ vc,
             const unsigned char* __restrict__ wq, const unsigned char* __restrict__ wo, const unsigned char* __restrict__ wg,
             const unsigned char* __restrict__ wu, const unsigned char* __restrict__ wd, unsigned* __restrict__ out, unsigned char* __restrict__ ws) {
  extern __shared__ __attribute__((aligned(16))) unsigned char smem[];
  const unsigned L0 = (unsigned)(uintptr_t)(LAS unsigned char*)smem;
  const int tid = threadIdx.x, lane = tid & 63, wave = __builtin_amdgcn_readfirstlane(tid >> 6);
  const int w = blockIdx.x, kvh = w & 7, idx = w >> 3, bat = idx >> 4, spl = idx & 15;
  const int n16 = lane & 15, g4 = lane >> 4;
  const int prod = (bat * 8 + kvh) * 16 + spl;
  const int role = idx < 16 ? 0 : idx < 23 ? 1 : idx < 27 ? 2 : 3;
  const int cb = idx, cc = kvh * 7 + (idx - 16), dd = kvh * 4 + (idx - 23);
  const __amdgpu_buffer_rsrc_t rsb = __builtin_amdgcn_make_buffer_rsrc(ws, 0, WS_END, 0x00020000);
  const u32x4 rs = {(unsigned)(uintptr_t)ws, (unsigned)((uintptr_t)ws >> 32) & 0xffffu, (unsigned)WS_END, 0x00020000u};
  TS(0);

  if (wave == 8) {
    __builtin_amdgcn_s_setprio(3);
    if (lane < 16) lds_st<unsigned>(L0 + L_FLAGS + lane * 4, 0u);
    {
      const unsigned char* base = wq + (kvh * 512 + idx * 16) * 2;
      const unsigned voff = (lane >> 1) * 8192 + (lane & 1) * 16;
#pragma unroll
      for (int i = 0; i < 16; ++i) dma_nt(base + (size_t)i * 32 * 8192, voff, L0 + L_WQ + i * 1024);
    }
    asm volatile("s_waitcnt lgkmcnt(0)\n\ts_barrier" ::: "memory");
    const u64 epoch = __hip_atomic_load((GAS u64*)(ws + WS_EPOCH), RLX_AGENT);
    u32x4 xa[16];
#pragma unroll
    for (int t = 0; t < 16; ++t) xa[t] = *(const u32x4*)(x + (n16 & 1) * 1024 + t * 64 + g4 * 16);
    {
      const u32x4 s4 = {SENT, SENT, SENT, SENT};
      __builtin_amdgcn_raw_buffer_store_b128(s4, rsb, WS_OP + prod * 1024 + lane * 16, 0, 16);
      int off = 0x7ffffff0;
      if (lane < 2) off = WS_ST + (kvh * 32 + idx) * 32 + lane * 16;
      else if (lane >= 8 && lane < 16) {
        if (role == 0) off = WS_HP + (kvh * 16 + cb) * 128 + (lane - 8) * 16;
        else if (role == 1) off = WS_ACT + cc * 128 + (lane - 8) * 16;
      }
      __builtin_amdgcn_raw_buffer_store_b128(s4, rsb, off, 0, 16);
    }
    if (n16 == 2) {
#pragma unroll
      for (int t = 0; t < 16; ++t) xa[t] = u32x4{0x3F803F80u, 0x3F803F80u, 0x3F803F80u, 0x3F803F80u};
    }
    f32x4 gram = {0.f, 0.f, 0.f, 0.f};
#pragma unroll
    for (int t = 0; t < 16; ++t) gram = mfma(xa[t], xa[t], gram);
    const float sx0 = rdl(gram[0], 2), sx1 = rdl(gram[1], 2);
    const float sq0 = rdl(gram[0], 0), sq1 = rdl(gram[1], 1);
    const float mu0 = sx0 * (1.f / 512.f), mu1 = sx1 * (1.f / 512.f);
    const float r0 = 1.f / sqrtf(fmaxf(sq0 * (1.f / 512.f) - mu0 * mu0, 0.f) + 1e-5f);
    const float r1 = 1.f / sqrtf(fmaxf(sq1 * (1.f / 512.f) - mu1 * mu1, 0.f) + 1e-5f);
    waitvm<0>();
    TS(1);
    f32x4 qa = {0.f, 0.f, 0.f, 0.f}, qb = {0.f, 0.f, 0.f, 0.f};
    {
      const unsigned a0 = L0 + L_WQ + (8 * g4 + ((lane >> 2) & 3)) * 32 + (lane & 3) * 8;
#pragma unroll
      for (int t = 0; t < 16; t += 2) {
        qa = mfma(xa[t], trfrag(a0 + t * 1024, a0 + t * 1024 + 128), qa);
        qb = mfma(xa[t + 1], trfrag(a0 + (t + 1) * 1024, a0 + (t + 1) * 1024 + 128), qb);
      }
    }
    const u64 tag = ((epoch + 1) & 0xFFFFFFFFFFFFull) << 16;
    {
      const float d0 = qa[0] + qb[0], d1 = qa[1] + qb[1], cs = qa[2] + qb[2];
      const float q0 = r0 * (d0 - mu0 * cs), q1 = r1 * (d1 - mu1 * cs);
      const unsigned p = pk(q0, q1);
      if (lane < 16) {
        GAS u64* g0 = (GAS u64*)(ws + WS_QG) + (kvh * 2 + 0) * 512 + idx * 16 + lane;
        __hip_atomic_store(g0, tag | (u64)(p & 0xffffu), RLX_AGENT);
        __hip_atomic_store(g0 + 512, tag | (u64)(p >> 16), RLX_AGENT);
      }
    }
    TS(2);
    {
      const unsigned thi = (unsigned)(tag >> 32), tlo = (unsigned)tag;
      u32x4 v[4];
      const unsigned o = WS_QG + (kvh * 2 + bat) * 4096 + lane * 16;
      for (int spin = 0; spin < SPIN_G; ++spin) {
        asm volatile("" ::: "memory");
#pragma unroll
        for (int i = 0; i < 4; ++i) v[i] = __builtin_amdgcn_raw_buffer_load_b128(rsb, o + i * 1024, 0, 16);
        bool ok = true;
#pragma unroll
        for (int i = 0; i < 4; ++i)
          ok = ok && v[i][1] == thi && v[i][3] == thi && (v[i][0] & 0xffff0000u) == tlo && (v[i][2] & 0xffff0000u) == tlo;
        if (__all(ok)) break;
      }
#pragma unroll
      for (int i = 0; i < 4; ++i) lds_st<unsigned>(L0 + L_Q + i * 256 + lane * 4, (v[i][0] & 0xffffu) | (v[i][2] << 16));
      flag_set(L0, F_Q, 1u, lane);
      TS(3);
      for (int spin = 0; spin < SPIN_G; ++spin) {
        asm volatile("" ::: "memory");
        u32x2 f[4];
#pragma unroll
        for (int i = 0; i < 4; ++i) {
          const int ww = i * 64 + lane;
          f[i] = __builtin_amdgcn_raw_buffer_load_b64(rsb, WS_QG + (ww >> 5) * 8192 + (ww & 31) * 128, 0, 16);
        }
        bool ok = true;
#pragma unroll
        for (int i = 0; i < 4; ++i) ok = ok && f[i][1] == thi && (f[i][0] & 0xffff0000u) == tlo;
        if (__all(ok)) break;
      }
      if (w == NWG - 1 && lane == 0) __hip_atomic_store((GAS u64*)(ws + WS_EPOCH), epoch + 1, RLX_AGENT);
    }
    TS(4);
    flag_wait<1>(L0, F_STAT, 8u);
    {
      const int l = lane & 31;
      const float m = lds_ld<float>(L0 + L_WM + l * 4), ls = lds_ld<float>(L0 + L_WL + l * 4);
      float M = m;
      M = fmaxf(M, __shfl_xor(M, 4)); M = fmaxf(M, __shfl_xor(M, 8)); M = fmaxf(M, __shfl_xor(M, 16));
      const float f = __builtin_amdgcn_exp2f(m - M);
      float L = f * ls;
      L += __shfl_xor(L, 4); L += __shfl_xor(L, 8); L += __shfl_xor(L, 16);
      if (lane < 32) lds_st<float>(L0 + L_F + l * 4, f);
      flag_set(L0, F_FAC, 1u, lane);
      if (lane < 4)
        __builtin_amdgcn_raw_buffer_store_b64(u32x2{nosent(__float_as_uint(M)), nosent(__float_as_uint(L))}, rsb, WS_ST + (kvh * 32 + idx) * 32 + lane * 8, 0, 16);
    }
    TS(5);
    if (role == 3) return;
    flag_wait<1>(L0, F_VISS, 8u);
    TS(6);
    if (role == 0) {
      const unsigned char* base = wo + (size_t)kvh * 512 * 1024 + cb * 64;
      const unsigned voff = (lane >> 2) * 1024 + (lane & 3) * 16;
#pragma unroll 4
      for (int j = 0; j < 32; ++j) dma_nt(base + (size_t)j * 16 * 1024, voff, L0 + (j >> 2) * 16384 + (j & 3) * 1024);
      u32x4 st = {SENT, SENT, SENT, SENT};
      const unsigned o = WS_ST + kvh * 1024 + lane * 16;
      for (int spin = 0; spin < SPIN_G; ++spin) {
        const u64 m = __ballot(pend4(st));
        if (m == 0) break;
        poll_round1(rs, st, o, m);
      }
      const float m0 = __uint_as_float(st[0]), l0 = __uint_as_float(st[1]), m1 = __uint_as_float(st[2]), l1 = __uint_as_float(st[3]);
      float M0 = m0, M1 = m1;
#pragma unroll
      for (int o2 = 2; o2 <= 16; o2 <<= 1) { M0 = fmaxf(M0, __shfl_xor(M0, o2)); M1 = fmaxf(M1, __shfl_xor(M1, o2)); }
      const float e0 = __builtin_amdgcn_exp2f(m0 - M0), e1 = __builtin_amdgcn_exp2f(m1 - M1);
      float S0 = e0 * l0, S1 = e1 * l1;
#pragma unroll
      for (int o2 = 2; o2 <= 16; o2 <<= 1) { S0 += __shfl_xor(S0, o2); S1 += __shfl_xor(S1, o2); }
      const int bb = lane >> 5, ss = (lane >> 1) & 15, h0 = (lane & 1) * 2;
      lds_st<float>(L0 + L_W + ((bb * 4 + h0) * 16 + ss) * 4, e0 / S0);
      lds_st<float>(L0 + L_W + ((bb * 4 + h0 + 1) * 16 + ss) * 4, e1 / S1);
      waitvm<0>();
      flag_set(L0, F_WTS, 1u, lane);
    } else if (role == 1) {
      const unsigned voff = (lane >> 2) * 3584 + (lane & 3) * 16;
      const unsigned char* bg = wg + cc * 64;
      const unsigned char* bu = wu + cc * 64;
#pragma unroll 4
      for (int j = 0; j < 32; ++j) {
        dma_nt(bg + (size_t)j * 16 * 3584, voff, L0 + (j >> 2) * 16384 + (j & 3) * 1024);
        dma_nt(bu + (size_t)j * 16 * 3584, voff, L0 + (j >> 2) * 16384 + 4096 + (j & 3) * 1024);
        if (j == 15) waitvm<16>();
      }
      waitvm<0>();
      flag_set(L0, F_WTS, 1u, lane);
    } else {
      const unsigned voff = (lane >> 1) * 1024 + (lane & 1) * 16;
      const unsigned char* base = wd + dd * 32;
      for (int v = 0; v < 8; ++v) {
#pragma unroll
        for (int i = 0; i < 7; ++i) dma_nt(base + (size_t)(v * 7 + i) * 32 * 1024, voff, L0 + v * 16384 + i * 1024);
        if (v == 3) waitvm<16>();
      }
      waitvm<0>();
      flag_set(L0, F_WTS, 1u, lane);
      u32x4 hv = lane < 32 ? u32x4{SENT, SENT, SENT, SENT} : u32x4{0u, 0u, 0u, 0u};
      const unsigned o = WS_HP + (((lane >> 2) & 7) * 16 + (dd >> 1)) * 128 + ((lane >> 1) & 1) * 64 + (dd & 1) * 32 + (lane & 1) * 16;
      for (int spin = 0; spin < SPIN_G; ++spin) {
        const u64 m = __ballot(pend4(hv));
        if (m == 0) break;
        poll_round1(rs, hv, o, m);
      }
      float hf[8];
#pragma unroll
      for (int j = 0; j < 4; ++j) { hf[2 * j] = bflo(hv[j]); hf[2 * j + 1] = bfhi(hv[j]); }
#pragma unroll
      for (int j = 0; j < 8; ++j) { hf[j] += __shfl_xor(hf[j], 4); hf[j] += __shfl_xor(hf[j], 8); hf[j] += __shfl_xor(hf[j], 16); }
      if (lane < 4) {
        lds_st<f32x4>(L0 + L_H + lane * 32, f32x4{hf[0], hf[1], hf[2], hf[3]});
        lds_st<f32x4>(L0 + L_H + lane * 32 + 16, f32x4{hf[4], hf[5], hf[6], hf[7]});
      }
      flag_set(L0, F_H, 1u, lane);
    }
    TS(7);
    return;
  }

  const unsigned LW = L0 + wave * 16384;
  const size_t chunk = ((size_t)(bat * 8 + kvh) * 8192 + spl * 512 + wave * 64) * 256;
  const unsigned char* kb = kc + chunk;
  const unsigned char* vb = vc + chunk;
  {
    unsigned kvo[4];
#pragma unroll
    for (int a = 0; a < 4; ++a) kvo[a] = g4 * 256 + ((n16 ^ (4 * g4 + a)) * 16);
#pragma unroll
    for (int i = 0; i < 4; ++i) dma_nt(kb + i * 1024, kvo[i & 3], LW + i * 1024);
    asm volatile("s_barrier" ::: "memory");
#pragma unroll
    for (int i = 4; i < 16; ++i) dma_nt(kb + i * 1024, kvo[i & 3], LW + i * 1024);
  }
  u32x4 V[16];
  const unsigned vo = lane * 16;
#pragma unroll
  for (int i = 0; i < 8; ++i) ldv_nt(V[i], vb + i * 1024, vo);
  TS(1);
  flag_wait<1>(L0, F_Q, 1u);
  waitvm<8>();
  TS(2);
  unsigned P[2][16];
  f32x4 acc[2] = {{0.f, 0.f, 0.f, 0.f}, {0.f, 0.f, 0.f, 0.f}};
  {
    u32x4 qf[2][4];
#pragma unroll
    for (int u = 0; u < 2; ++u)
#pragma unroll
      for (int t = 0; t < 4; ++t) qf[u][t] = lds_ld<u32x4>(L0 + L_Q + (2 * u + (n16 >> 3)) * 256 + t * 64 + g4 * 16);
    f32x4 S[2][4];
    const unsigned krow = LW + (4 * (n16 & 3) + (n16 >> 2)) * 256;
#pragma unroll
    for (int T = 0; T < 4; ++T) {
      S[0][T] = f32x4{0.f, 0.f, 0.f, 0.f};
      S[1][T] = f32x4{0.f, 0.f, 0.f, 0.f};
#pragma unroll
      for (int t = 0; t < 4; ++t) {
        const u32x4 kf = lds_ld<u32x4>(krow + T * 4096 + (((4 * t + g4) ^ n16) * 16));
        S[0][T] = mfma(kf, qf[0][t], S[0][T]);
        S[1][T] = mfma(kf, qf[1][t], S[1][T]);
      }
    }
#pragma unroll
    for (int u = 0; u < 2; ++u) {
      float m = S[u][0][0];
#pragma unroll
      for (int T = 0; T < 4; ++T)
#pragma unroll
        for (int r = 0; r < 4; ++r) m = fmaxf(m, S[u][T][r]);
      m = x32_max(x16_max(m));
      const float ms = m * QSCALE;
      float l = 0.f;
#pragma unroll
      for (int T = 0; T < 4; ++T)
#pragma unroll
        for (int r = 0; r < 4; ++r) {
          const float p = __builtin_amdgcn_exp2f(fmaf(S[u][T][r], QSCALE, -ms));
          const unsigned pp = pk(p, p);
          P[u][T * 4 + r] = pp;
          l += bfhi(pp);
        }
      l = x32_sum(x16_sum(l));
      if ((lane & 55) == 0) {
        lds_st<float>(L0 + L_WM + (wave * 4 + 2 * u + (n16 >> 3)) * 4, ms);
        lds_st<float>(L0 + L_WL + (wave * 4 + 2 * u + (n16 >> 3)) * 4, l);
      }
    }
    flag_add(L0, F_STAT, lane);
  }
  TS(3);
  u32x4 msk;
  {
    const unsigned hm = (lane & 1) ? 0xffff0000u : 0x0000ffffu;
    const int dsel = (lane & 7) >> 1;
    msk = u32x4{dsel == 0 ? hm : 0u, dsel == 1 ? hm : 0u, dsel == 2 ? hm : 0u, dsel == 3 ? hm : 0u};
  }
#define PV(i)                                                        \
  do {                                                               \
    const u32x4 b0 = msk & P[0][i], b1 = msk & P[1][i];              \
    acc[0] = mfma(V[i], b0, acc[0]);                                 \
    acc[1] = mfma(V[i], b1, acc[1]);                                 \
  } while (0)
#pragma unroll
  for (int j = 0; j < 8; ++j) {
    ldv_nt(V[8 + j], vb + (8 + j) * 1024, vo);
    asm volatile("s_waitcnt vmcnt(8)" : "+v"(V[j])::"memory");
    PV(j);
  }
  flag_add(L0, F_VISS, lane);
  TS(4);
#define PVW(j, N)                                                    \
  do {                                                               \
    asm volatile("s_waitcnt vmcnt(" #N ")" : "+v"(V[j])::"memory");  \
    PV(j);                                                           \
  } while (0)
  PVW(8, 7); PVW(9, 6); PVW(10, 5); PVW(11, 4); PVW(12, 3); PVW(13, 2); PVW(14, 1);
  flag_wait<0>(L0, F_FAC, 1u);
  const float f0 = lds_ld<float>(L0 + L_F + (wave * 4 + (n16 >> 3)) * 4), f1 = lds_ld<float>(L0 + L_F + (wave * 4 + 2 + (n16 >> 3)) * 4);
  PVW(15, 0);
  TS(5);
  lds_st<f32x4>(LW + L_RB_O + lane * 16, acc[0] * f0);
  lds_st<f32x4>(LW + L_RB_O + 1024 + lane * 16, acc[1] * f1);
  flag_add(L0, F_S1, lane);
  if (wave < 2) {
    flag_wait<0>(L0, F_S1, 8u);
    f32x4 s = lds_ld<f32x4>(L0 + L_RB_O + wave * 1024 + lane * 16);
#pragma unroll
    for (int v = 1; v < 8; ++v) s += lds_ld<f32x4>(L0 + v * 16384 + L_RB_O + wave * 1024 + lane * 16);
    __builtin_amdgcn_raw_buffer_store_b64(u32x2{nosent(pk(s[0], s[1])), nosent(pk(s[2], s[3]))}, rsb,
                                          WS_OP + prod * 1024 + (2 * wave + (n16 >> 3)) * 256 + g4 * 64 + (lane & 7) * 8, 0, 16);
  }
  TS(6);
  if (role == 3) return;

  const unsigned trq = (lane >> 2) & 3, trp = lane & 3;
  if (role == 0) {
    const int hd = wave >> 1, half = wave & 1;
    u32x4 A[4];
    unsigned o[4];
#pragma unroll
    for (int i = 0; i < 4; ++i) {
      A[i] = u32x4{SENT, SENT, SENT, SENT};
      o[i] = WS_OP + (((i >> 1) * 8 + kvh) * 16 + n16) * 1024 + hd * 256 + (2 * half + (i & 1)) * 64 + g4 * 16;
    }
    float xres = 0.f;
    if (wave == 0 && kvh == 0) xres = bflo(*(const unsigned short*)(x + ((g4 >> 1) * 512 + cb * 32 + (g4 & 1) * 16 + n16) * 2));
    flag_wait<0>(L0, F_WTS, 1u);
    u32x4 Bw[2][2];
#pragma unroll
    for (int t = 0; t < 2; ++t)
#pragma unroll
      for (int nt = 0; nt < 2; ++nt) {
        const unsigned a = LW + (32 * t + 8 * trq + 2 * g4) * 64 + nt * 32 + trp * 8;
        Bw[t][nt] = trfrag(a, a + 64);
      }
    const f32x4 w0 = lds_ld<f32x4>(L0 + L_W + ((0 * 4 + hd) * 16 + 4 * g4) * 4), w1 = lds_ld<f32x4>(L0 + L_W + ((1 * 4 + hd) * 16 + 4 * g4) * 4);
    TS(7);
    for (int spin = 0; spin < SPIN_G; ++spin) {
      u64 m[4];
#pragma unroll
      for (int i = 0; i < 4; ++i) m[i] = __ballot(pend4(A[i]));
      if ((m[0] | m[1] | m[2] | m[3]) == 0) break;
      poll_round4(rs, A, o, m);
    }
    TS(8);
    f32x4 val;
#pragma unroll
    for (int bb = 0; bb < 2; ++bb)
#pragma unroll
      for (int nt = 0; nt < 2; ++nt) {
        f32x4 d = {0.f, 0.f, 0.f, 0.f};
        d = mfma(A[bb * 2 + 0], Bw[0][nt], d);
        d = mfma(A[bb * 2 + 1], Bw[1][nt], d);
        const f32x4 ww = bb ? w1 : w0;
        val[bb * 2 + nt] = x32_sum(x16_sum(d[0] * ww[0] + d[1] * ww[1] + d[2] * ww[2] + d[3] * ww[3]));
      }
    if (lane < 16) lds_st<f32x4>(LW + L_RB_B + lane * 16, val);
    flag_add(L0, F_S2, lane);
    if (wave == 0) {
      flag_wait<0>(L0, F_S2, 8u);
      float s = xres;
#pragma unroll
      for (int v = 0; v < 8; ++v) s += lds_ld<float>(L0 + v * 16384 + L_RB_B + n16 * 16 + g4 * 4);
      const float nb = dpp<XOR1>(s);
      if ((lane & 1) == 0)
        __builtin_amdgcn_raw_buffer_store_b32(nosent(pk(s, nb)), rsb, WS_HP + (kvh * 16 + cb) * 128 + (g4 >> 1) * 64 + ((g4 & 1) * 16 + n16) * 2, 0, 16);
      TS(9);
    }
  } else if (role == 1) {
    u32x4 A0 = {SENT, SENT, SENT, SENT}, A1 = {SENT, SENT, SENT, SENT};
    const unsigned o0 = WS_HP + ((n16 & 7) * 16 + 2 * wave) * 128 + (n16 >> 3) * 64 + g4 * 16, o1 = o0 + 128;
    const u32x4 ones = {0x3F803F80u, 0x3F803F80u, 0x3F803F80u, 0x3F803F80u};
    flag_wait<0>(L0, F_WTS, 1u);
    u32x4 Bw[2][2][2];
#pragma unroll
    for (int mt = 0; mt < 2; ++mt)
#pragma unroll
      for (int t = 0; t < 2; ++t)
#pragma unroll
        for (int nt = 0; nt < 2; ++nt) {
          const unsigned a = LW + mt * 4096 + (32 * t + 8 * g4 + trq) * 64 + nt * 32 + trp * 8;
          Bw[mt][t][nt] = trfrag(a, a + 256);
        }
    {
      f32x4 cs;
#pragma unroll
      for (int nt = 0; nt < 2; ++nt)
#pragma unroll
        for (int mt = 0; mt < 2; ++mt) {
          f32x4 d = {0.f, 0.f, 0.f, 0.f};
          d = mfma(ones, Bw[mt][0][nt], d);
          d = mfma(ones, Bw[mt][1][nt], d);
          cs[nt * 2 + mt] = d[0];
        }
      if (lane < 16) lds_st<f32x4>(LW + L_RB_CS + lane * 16, cs);
      flag_add(L0, F_S3, lane);
    }
    f32x2 csum = {0.f, 0.f};
    if (wave == 0) {
      flag_wait<0>(L0, F_S3, 8u);
#pragma unroll
      for (int v = 0; v < 8; ++v) csum += lds_ld<f32x2>(L0 + v * 16384 + L_RB_CS + n16 * 16 + (g4 & 1) * 8);
    }
    TS(7);
    for (int spin = 0; spin < SPIN_G; ++spin) {
      const u64 m0 = __ballot(pend4(A0)), m1 = __ballot(pend4(A1));
      if ((m0 | m1) == 0) break;
      poll_round2(rs, A0, A1, o0, o1, m0, m1);
    }
    TS(8);
    f32x4 gr = {0.f, 0.f, 0.f, 0.f}, s1 = {0.f, 0.f, 0.f, 0.f};
    gr = mfma(A0, A0, gr); s1 = mfma(A0, ones, s1);
    gr = mfma(A1, A1, gr); s1 = mfma(A1, ones, s1);
    f32x4 val;
#pragma unroll
    for (int nt = 0; nt < 2; ++nt)
#pragma unroll
      for (int mt = 0; mt < 2; ++mt) {
        f32x4 d = {0.f, 0.f, 0.f, 0.f};
        d = mfma(A0, Bw[mt][0][nt], d);
        d = mfma(A1, Bw[mt][1][nt], d);
        val[nt * 2 + mt] = x16_sum(d[0] + d[1] + d[2] + d[3]);
      }
    float gs = ((n16 >> 3) == (g4 >> 1)) ? (gr[0] + gr[1] + gr[2] + gr[3]) : 0.f;
    gs += dpp<XOR1>(gs); gs += dpp<XOR2>(gs); gs += dpp<XOR7>(gs);
    gs = x16_sum(gs);
    const float ss = x16_sum(s1[0] + s1[1] + s1[2] + s1[3]);
    if ((g4 & 1) == 0) {
      lds_st<f32x4>(LW + L_RB_C + ((g4 >> 1) * 16 + n16) * 16, val);
      if (n16 == (g4 >> 1) * 8) lds_st<f32x2>(LW + L_RB_C + 512 + (g4 >> 1) * 8, f32x2{ss, gs});
    }
    flag_add(L0, F_S2, lane);
    if (wave == 0) {
      flag_wait<0>(L0, F_S2, 8u);
      f32x2 gu = {0.f, 0.f}, st = {0.f, 0.f};
#pragma unroll
      for (int v = 0; v < 8; ++v) {
        gu += lds_ld<f32x2>(L0 + v * 16384 + L_RB_C + ((g4 >> 1) * 16 + n16) * 16 + (g4 & 1) * 8);
        st += lds_ld<f32x2>(L0 + v * 16384 + L_RB_C + 512 + (g4 >> 1) * 8);
      }
      const float mu = st[0] * (1.f / 512.f);
      const float r = 1.f / sqrtf(fmaxf(st[1] * (1.f / 512.f) - mu * mu, 0.f) + 1e-5f);
      const float gg = r * (gu[0] - mu * csum[0]), uu = r * (gu[1] - mu * csum[1]);
      const float a = gg / (1.f + __expf(-gg)) * uu;
      const float nb = dpp<XOR1>(a);
      if ((lane & 1) == 0)
        __builtin_amdgcn_raw_buffer_store_b32(nosent(pk(a, nb)), rsb, WS_ACT + cc * 128 + (g4 >> 1) * 64 + ((g4 & 1) * 16 + n16) * 2, 0, 16);
      TS(9);
    }
  } else {
    u32x4 A[7];
#pragma unroll
    for (int i = 0; i < 7; ++i) A[i] = n16 < 2 ? u32x4{SENT, SENT, SENT, SENT} : u32x4{0u, 0u, 0u, 0u};
    const unsigned o = WS_ACT + wave * 7 * 128 + (n16 & 1) * 64 + g4 * 16;
    flag_wait<0>(L0, F_WTS, 1u);
    u32x4 Bw[7];
#pragma unroll
    for (int i = 0; i < 7; ++i) {
      const unsigned a = LW + (32 * i + 8 * g4 + trq) * 32 + trp * 8;
      Bw[i] = trfrag(a, a + 128);
    }
    TS(7);
    for (int spin = 0; spin < SPIN_G; ++spin) {
      u64 m[7], any = 0;
#pragma unroll
      for (int i = 0; i < 7; ++i) { m[i] = __ballot(pend4(A[i])); any |= m[i]; }
      if (any == 0) break;
      poll_round7(rs, A, o, m);
    }
    TS(8);
    f32x4 d0 = {0.f, 0.f, 0.f, 0.f}, d1 = {0.f, 0.f, 0.f, 0.f};
#pragma unroll
    for (int i = 0; i < 7; ++i) { if (i & 1) d1 = mfma(A[i], Bw[i], d1); else d0 = mfma(A[i], Bw[i], d0); }
    if (lane < 16) lds_st<f32x2>(LW + L_RB_D + lane * 8, f32x2{d0[0] + d1[0], d0[1] + d1[1]});
    flag_add(L0, F_S2, lane);
    if (wave == 0) {
      flag_wait<0>(L0, F_H, 1u);
      flag_wait<0>(L0, F_S2, 8u);
      float s = lds_ld<float>(L0 + L_H + ((g4 & 1) * 16 + n16) * 4);
#pragma unroll
      for (int v = 0; v < 8; ++v) s += lds_ld<float>(L0 + v * 16384 + L_RB_D + n16 * 8 + (g4 & 1) * 4);
      const float nb = dpp<XOR1>(s);
      if ((lane & 1) == 0 && lane < 32) out[((g4 & 1) * 512 + dd * 16 + n16) >> 1] = pk(s, nb);
      TS(9);
    }
  }
}
}

extern "C" void kernel_launch(void* const* d_in, const int* in_sizes, int n_in, void* d_out, int out_size, void* d_ws, size_t ws_size,
                              hipStream_t stream) {
  (void)in_sizes; (void)n_in; (void)out_size; (void)ws_size;
  (void)hipFuncSetAttribute((const void*)fk::fused_kernel, hipFuncAttributeMaxDynamicSharedMemorySize, fk::L_END);
  fk::fused_kernel<<<fk::NWG, fk::NTHR, fk::L_END, stream>>>(
      (const unsigned char*)d_in[0], (const unsigned char*)d_in[1], (const unsigned char*)d_in[2], (const unsigned char*)d_in[3],
      (const unsigned char*)d_in[4], (const unsigned char*)d_in[6], (const unsigned char*)d_in[7], (const unsigned char*)d_in[8],
      (unsigned*)d_out, (unsigned char*)d_ws);
}
```

```cpp
#define FK_TS 1
#include <hip/hip_runtime.h>
#include <hip/hip_bf16.h>
#include <stdint.h>

namespace fk {
typedef unsigned u32x4 __attribute__((ext_vector_type(4)));
typedef unsigned u32x2 __attribute__((ext_vector_type(2)));
typedef float f32x4 __attribute__((ext_vector_type(4)));
typedef float f32x2 __attribute__((ext_vector_type(2)));
typedef short s16x4 __attribute__((ext_vector_type(4)));
typedef __bf16 bf16x8 __attribute__((ext_vector_type(8)));
typedef __bf16 bf16x2 __attribute__((ext_vector_type(2)));
typedef unsigned long long u64;
#define LAS __attribute__((address_space(3)))
#define GAS __attribute__((address_space(1)))
#define RLX_AGENT __ATOMIC_RELAXED, __HIP_MEMORY_SCOPE_AGENT

constexpr unsigned SENT = 0xFFFFFFFFu;
constexpr int NWG = 256, NTHR = 576;
constexpr int WS_EPOCH = 0;
constexpr int WS_QG = 4096;
constexpr int WS_ST = WS_QG + 65536;
constexpr int WS_OP = WS_ST + 8192;
constexpr int WS_HP = WS_OP + 262144;
constexpr int WS_ACT = WS_HP + 16384;
constexpr int WS_END = WS_ACT + 8192;
constexpr int WS_TS = 1 << 20;
constexpr int L_RB_O = 10240, L_RB_B = L_RB_O + 2048, L_RB_C = L_RB_B + 512, L_RB_CS = L_RB_C + 1024, L_RB_D = L_RB_CS + 512;
constexpr int L_WQ = 131072, L_Q = L_WQ + 16384, L_WM = L_Q + 1024, L_WL = L_WM + 128, L_F = L_WL + 128, L_W = L_F + 128, L_H = L_W + 512,
              L_FLAGS = L_H + 128, L_END = L_FLAGS + 64;
enum { F_Q = 0, F_STAT = 1, F_FAC = 2, F_VISS = 3, F_WTS = 4, F_H = 5, F_S1 = 6, F_S2 = 7, F_S3 = 8 };
constexpr int SPIN_G = 1 << 18;
constexpr int SPIN_L = 1 << 22;
constexpr float QSCALE = 0.088388347648318440f * 1.4426950408889634f;

__device__ __forceinline__ f32x4 mfma(u32x4 a, u32x4 b, f32x4 c) {
  return __builtin_amdgcn_mfma_f32_16x16x32_bf16(__builtin_bit_cast(bf16x8, a), __builtin_bit_cast(bf16x8, b), c, 0, 0, 0);
}
__device__ __forceinline__ unsigned pk(float lo, float hi) { bf16x2 v = {(__bf16)lo, (__bf16)hi}; return __builtin_bit_cast(unsigned, v); }
__device__ __forceinline__ unsigned nosent(unsigned x) { return x < 0xFFFFFFFEu ? x : 0xFFFFFFFEu; }
__device__ __forceinline__ float bflo(unsigned u) { return __builtin_bit_cast(float, u << 16); }
__device__ __forceinline__ float bfhi(unsigned u) { return __builtin_bit_cast(float, u & 0xffff0000u); }
__device__ __forceinline__ float x16_sum(float x) {
  auto s = __builtin_amdgcn_permlane16_swap(__float_as_uint(x), __float_as_uint(x), false, false);
  return __uint_as_float(s[0]) + __uint_as_float(s[1]);
}
__device__ __forceinline__ float x32_sum(float x) {
  auto s = __builtin_amdgcn_permlane32_swap(__float_as_uint(x), __float_as_uint(x), false, false);
  return __uint_as_float(s[0]) + __uint_as_float(s[1]);
}
__device__ __forceinline__ float x16_max(float x) {
  auto s = __builtin_amdgcn_permlane16_swap(__float_as_uint(x), __float_as_uint(x), false, false);
  return fmaxf(__uint_as_float(s[0]), __uint_as_float(s[1]));
}
__device__ __forceinline__ float x32_max(float x) {
  auto s = __builtin_amdgcn_permlane32_swap(__float_as_uint(x), __float_as_uint(x), false, false);
  return fmaxf(__uint_as_float(s[0]), __uint_as_float(s[1]));
}
__device__ __forceinline__ float rdl(float x, int l) { return __builtin_bit_cast(float, __builtin_amdgcn_readlane(__builtin_bit_cast(int, x), l)); }
template <int CTRL> __device__ __forceinline__ float dpp(float x) {
  return __builtin_bit_cast(float, __builtin_amdgcn_mov_dpp(__builtin_bit_cast(int, x), CTRL, 0xf, 0xf, true));
}
constexpr int XOR1 = 0xB1, XOR2 = 0x4E, XOR7 = 0x141;

template <int N> __device__ __forceinline__ void waitvm() { asm volatile("s_waitcnt vmcnt(%0)" ::"i"(N) : "memory"); }
__device__ __forceinline__ void dma_nt(const void* sbase, unsigned voff, unsigned ldst) {
  unsigned keep;
  asm volatile("s_mov_b32 %0, m0\n\ts_mov_b32 m0, %3\n\ts_nop 0\n\tglobal_load_lds_dwordx4 %1, %2 nt\n\ts_mov_b32 m0, %0"
               : "=&s"(keep) : "v"(voff), "s"(sbase), "s"(ldst) : "memory");
}
__device__ __forceinline__ void ldv_nt(u32x4& dst, const void* sbase, unsigned voff) {
  asm volatile("global_load_dwordx4 %0, %1, %2 nt" : "=v"(dst) : "v"(voff), "s"(sbase) : "memory");
}
__device__ __forceinline__ u32x2 trread(unsigned addr) {
  return __builtin_bit_cast(u32x2, __builtin_amdgcn_ds_read_tr16_b64_v4i16((LAS s16x4*)(uintptr_t)addr));
}
__device__ __forceinline__ u32x4 trfrag(unsigned a0, unsigned a1) { u32x2 lo = trread(a0), hi = trread(a1); return u32x4{lo[0], lo[1], hi[0], hi[1]}; }
template <typename T> __device__ __forceinline__ T lds_ld(unsigned addr) { return *(LAS T*)(uintptr_t)addr; }
template <typename T> __device__ __forceinline__ void lds_st(unsigned addr, T v) { *(LAS T*)(uintptr_t)addr = v; }
__device__ __forceinline__ void flag_set(unsigned base, int f, unsigned v, int lane) {
  asm volatile("" ::: "memory");
  if (lane == 0) *(volatile LAS unsigned*)(uintptr_t)(base + L_FLAGS + f * 4) = v;
  asm volatile("" ::: "memory");
}
__device__ __forceinline__ void flag_add(unsigned base, int f, int lane) {
  asm volatile("" ::: "memory");
  if (lane == 0) __hip_atomic_fetch_add((LAS unsigned*)(uintptr_t)(base + L_FLAGS + f * 4), 1u, __ATOMIC_RELAXED, __HIP_MEMORY_SCOPE_WORKGROUP);
  asm volatile("" ::: "memory");
}
template <int SLP> __device__ __forceinline__ void flag_wait(unsigned base, int f, unsigned v) {
  asm volatile("" ::: "memory");
  for (int i = 0; i < SPIN_L; ++i) {
    unsigned x = *(volatile LAS unsigned*)(uintptr_t)(base + L_FLAGS + f * 4);
    if ((unsigned)__builtin_amdgcn_readfirstlane(x) >= v) break;
    if (SLP) __builtin_amdgcn_s_sleep(SLP);
  }
  asm volatile("" ::: "memory");
}
__device__ __forceinline__ bool pend4(u32x4 v) {
  unsigned a = v[0] > v[1] ? v[0] : v[1], b = v[2] > v[3] ? v[2] : v[3];
  return (a > b ? a : b) == SENT;
}
__device__ __forceinline__ void poll_round1(u32x4 rs, u32x4& v0, unsigned o0, u64 m0) {
  u64 keep;
  asm volatile("s_mov_b64 %0, exec\n\t"
               "s_mov_b64 exec, %4\n\tbuffer_load_dwordx4 %1, %2, %3, 0 offen sc1\n\t"
               "s_mov_b64 exec, %0\n\ts_waitcnt vmcnt(0)"
               : "=&s"(keep), "+v"(v0) : "v"(o0), "s"(rs), "s"(m0) : "memory");
}
__device__ __forceinline__ void poll_round2(u32x4 rs, u32x4& v0, u32x4& v1, unsigned o0, unsigned o1, u64 m0, u64 m1) {
  u64 keep;
  asm volatile("s_mov_b64 %0, exec\n\t"
               "s_mov_b64 exec, %6\n\tbuffer_load_dwordx4 %1, %3, %5, 0 offen sc1\n\t"
               "s_mov_b64 exec, %7\n\tbuffer_load_dwordx4 %2, %4, %5, 0 offen sc1\n\t"
               "s_mov_b64 exec, %0\n\ts_waitcnt vmcnt(0)"
               : "=&s"(keep), "+v"(v0), "+v"(v1) : "v"(o0), "v"(o1), "s"(rs), "s"(m0), "s"(m1) : "memory");
}
__device__ __forceinline__ void poll_round4(u32x4 rs, u32x4 (&v)[4], const unsigned (&o)[4], const u64 (&m)[4]) {
  u64 keep;
  asm volatile("s_mov_b64 %0, exec\n\t"
               "s_mov_b64 exec, %10\n\tbuffer_load_dwordx4 %1, %5, %9, 0 offen sc1\n\t"
               "s_mov_b64 exec, %11\n\tbuffer_load_dwordx4 %2, %6, %9, 0 offen sc1\n\t"
               "s_mov_b64 exec, %12\n\tbuffer_load_dwordx4 %3, %7, %9, 0 offen sc1\n\t"
               "s_mov_b64 exec, %13\n\tbuffer_load_dwordx4 %4, %8, %9, 0 offen sc1\n\t"
               "s_mov_b64 exec, %0\n\ts_waitcnt vmcnt(0)"
               : "=&s"(keep), "+v"(v[0]), "+v"(v[1]), "+v"(v[2]), "+v"(v[3])
               : "v"(o[0]), "v"(o[1]), "v"(o[2]), "v"(o[3]), "s"(rs), "s"(m[0]), "s"(m[1]), "s"(m[2]), "s"(m[3]) : "memory");
}
__device__ __forceinline__ void poll_round7(u32x4 rs, u32x4 (&v)[7], unsigned o, const u64 (&m)[7]) {
  u64 keep;
  asm volatile("s_mov_b64 %0, exec\n\t"
               "s_mov_b64 exec, %10\n\tbuffer_load_dwordx4 %1, %8, %9, 0 offen sc1\n\t"
               "s_mov_b64 exec, %11\n\tbuffer_load_dwordx4 %2, %8, %9, 0 offen offset:128 sc1\n\t"
               "s_mov_b64 exec, %12\n\tbuffer_load_dwordx4 %3, %8, %9, 0 offen offset:256 sc1\n\t"
               "s_mov_b64 exec, %13\n\tbuffer_load_dwordx4 %4, %8, %9, 0 offen offset:384 sc1\n\t"
               "s_mov_b64 exec, %14\n\tbuffer_load_dwordx4 %5, %8, %9, 0 offen offset:512 sc1\n\t"
               "s_mov_b64 exec, %15\n\tbuffer_load_dwordx4 %6, %8, %9, 0 offen offset:640 sc1\n\t"
               "s_mov_b64 exec, %16\n\tbuffer_load_dwordx4 %7, %8, %9, 0 offen offset:768 sc1\n\t"
               "s_mov_b64 exec, %0\n\ts_waitcnt vmcnt(0)"
               : "=&s"(keep), "+v"(v[0]), "+v"(v[1]), "+v"(v[2]), "+v"(v[3]), "+v"(v[4]), "+v"(v[5]), "+v"(v[6])
               : "v"(o), "s"(rs), "s"(m[0]), "s"(m[1]), "s"(m[2]), "s"(m[3]), "s"(m[4]), "s"(m[5]), "s"(m[6]) : "memory");
}

#define TS(k) do { if (lane == 0) ((u64*)(ws + WS_TS))[(w * 9 + wave) * 16 + (k)] = __builtin_amdgcn_s_memrealtime(); } while (0)

__global__ void __launch_bounds__(NTHR, 3)
fused_kernel(const unsigned char* __restrict__ x, const unsigned char* __restrict__ kc, const unsigned char* __restrict__ vc,
             const unsigned char* __restrict__ wq, const unsigned char* __restrict__ wo, const unsigned char* __restrict__ wg,
             const unsigned char* __restrict__ wu, const unsigned char* __restrict__ wd, unsigned* __restrict__ out, unsigned char* __restrict__ ws) {
  extern __shared__ __attribute__((aligned(16))) unsigned char smem[];
  const unsigned L0 = (unsigned)(uintptr_t)(LAS unsigned char*)smem;
  const int tid = threadIdx.x, lane = tid & 63, wave = __builtin_amdgcn_readfirstlane(tid >> 6);
  const int w = blockIdx.x, kvh = w & 7, idx = w >> 3, bat = idx >> 4, spl = idx & 15;
  const int n16 = lane & 15, g4 = lane >> 4;
  const int prod = (bat * 8 + kvh) * 16 + spl;
  const int role = idx < 16 ? 0 : idx < 23 ? 1 : idx < 27 ? 2 : 3;
  const int cb = idx, cc = kvh * 7 + (idx - 16), dd = kvh * 4 + (idx - 23);
  const __amdgpu_buffer_rsrc_t rsb = __builtin_amdgcn_make_buffer_rsrc(ws, 0, WS_END, 0x00020000);
  const u32x4 rs = {(unsigned)(uintptr_t)ws, (unsigned)((uintptr_t)ws >> 32) & 0xffffu, (unsigned)WS_END, 0x00020000u};
  TS(0);

  if (wave == 8) {
    __builtin_amdgcn_s_setprio(3);
    if (lane < 16) lds_st<unsigned>(L0 + L_FLAGS + lane * 4, 0u);
    {
      const unsigned char* base = wq + (kvh * 512 + idx * 16) * 2;
      const unsigned voff = (lane >> 1) * 8192 + (lane & 1) * 16;
#pragma unroll
      for (int i = 0; i < 16; ++i) dma_nt(base + (size_t)i * 32 * 8192, voff, L0 + L_WQ + i * 1024);
    }
    asm volatile("s_waitcnt lgkmcnt(0)\n\ts_barrier" ::: "memory");
    const u64 epoch = __hip_atomic_load((GAS u64*)(ws + WS_EPOCH), RLX_AGENT);
    u32x4 xa[16];
#pragma unroll
    for (int t = 0; t < 16; ++t) xa[t] = *(const u32x4*)(x + (n16 & 1) * 1024 + t * 64 + g4 * 16);
    {
      const u32x4 s4 = {SENT, SENT, SENT, SENT};
      __builtin_amdgcn_raw_buffer_store_b128(s4, rsb, WS_OP + prod * 1024 + lane * 16, 0, 16);
      int off = 0x7ffffff0;
      if (lane < 2) off = WS_ST + (kvh * 32 + idx) * 32 + lane * 16;
      else if (lane >= 8 && lane < 16) {
        if (role == 0) off = WS_HP + (kvh * 16 + cb) * 128 + (lane - 8) * 16;
        else if (role == 1) off = WS_ACT + cc * 128 + (lane - 8) * 16;
      }
      __builtin_amdgcn_raw_buffer_store_b128(s4, rsb, off, 0, 16);
    }
    if (n16 == 2) {
#pragma unroll
      for (int t = 0; t < 16; ++t) xa[t] = u32x4{0x3F803F80u, 0x3F803F80u, 0x3F803F80u, 0x3F803F80u};
    }
    f32x4 gram = {0.f, 0.f, 0.f, 0.f};
#pragma unroll
    for (int t = 0; t < 16; ++t) gram = mfma(xa[t], xa[t], gram);
    const float sx0 = rdl(gram[0], 2), sx1 = rdl(gram[1], 2);
    const float sq0 = rdl(gram[0], 0), sq1 = rdl(gram[1], 1);
    const float mu0 = sx0 * (1.f / 512.f), mu1 = sx1 * (1.f / 512.f);
    const float r0 = 1.f / sqrtf(fmaxf(sq0 * (1.f / 512.f) - mu0 * mu0, 0.f) + 1e-5f);
    const float r1 = 1.f / sqrtf(fmaxf(sq1 * (1.f / 512.f) - mu1 * mu1, 0.f) + 1e-5f);
    waitvm<0>();
    TS(1);
    f32x4 qa = {0.f, 0.f, 0.f, 0.f}, qb = {0.f, 0.f, 0.f, 0.f};
    {
      const unsigned a0 = L0 + L_WQ + (8 * g4 + ((lane >> 2) & 3)) * 32 + (lane & 3) * 8;
#pragma unroll
      for (int t = 0; t < 16; t += 2) {
        qa = mfma(xa[t], trfrag(a0 + t * 1024, a0 + t * 1024 + 128), qa);
        qb = mfma(xa[t + 1], trfrag(a0 + (t + 1) * 1024, a0 + (t + 1) * 1024 + 128), qb);
      }
    }
    const u64 tag = ((epoch + 1) & 0xFFFFFFFFFFFFull) << 16;
    {
      const float d0 = qa[0] + qb[0], d1 = qa[1] + qb[1], cs = qa[2] + qb[2];
      const float q0 = r0 * (d0 - mu0 * cs), q1 = r1 * (d1 - mu1 * cs);
      const unsigned p = pk(q0, q1);
      if (lane < 16) {
        GAS u64* g0 = (GAS u64*)(ws + WS_QG) + (kvh * 2 + 0) * 512 + idx * 16 + lane;
        __hip_atomic_store(g0, tag | (u64)(p & 0xffffu), RLX_AGENT);
        __hip_atomic_store(g0 + 512, tag | (u64)(p >> 16), RLX_AGENT);
      }
    }
    TS(2);
    {
      const unsigned thi = (unsigned)(tag >> 32), tlo = (unsigned)tag;
      u32x4 v[4];
      const unsigned o = WS_QG + (kvh * 2 + bat) * 4096 + lane * 16;
      for (int spin = 0; spin < SPIN_G; ++spin) {
        asm volatile("" ::: "memory");
#pragma unroll
        for (int i = 0; i < 4; ++i) v[i] = __builtin_amdgcn_raw_buffer_load_b128(rsb, o + i * 1024, 0, 16);
        bool ok = true;
#pragma unroll
        for (int i = 0; i < 4; ++i)
          ok = ok && v[i][1] == thi && v[i][3] == thi && (v[i][0] & 0xffff0000u) == tlo && (v[i][2] & 0xffff0000u) == tlo;
        if (__all(ok)) break;
      }
#pragma unroll
      for (int i = 0; i < 4; ++i) lds_st<unsigned>(L0 + L_Q + i * 256 + lane * 4, (v[i][0] & 0xffffu) | (v[i][2] << 16));
      flag_set(L0, F_Q, 1u, lane);
      TS(3);
      for (int spin = 0; spin < SPIN_G; ++spin) {
        asm volatile("" ::: "memory");
        u32x2 f[4];
#pragma unroll
        for (int i = 0; i < 4; ++i) {
          const int ww = i * 64 + lane;
          f[i] = __builtin_amdgcn_raw_buffer_load_b64(rsb, WS_QG + (ww >> 5) * 8192 + (ww & 31) * 128, 0, 16);
        }
        bool ok = true;
#pragma unroll
        for (int i = 0; i < 4; ++i) ok = ok && f[i][1] == thi && (f[i][0] & 0xffff0000u) == tlo;
        if (__all(ok)) break;
      }
      if (w == NWG - 1 && lane == 0) __hip_atomic_store((GAS u64*)(ws + WS_EPOCH), epoch + 1, RLX_AGENT);
    }
    TS(4);
    flag_wait<1>(L0, F_STAT, 8u);
    {
      const int l = lane & 31;
      const float m = lds_ld<float>(L0 + L_WM + l * 4), ls = lds_ld<float>(L0 + L_WL + l * 4);
      float M = m;
      M = fmaxf(M, __shfl_xor(M, 4)); M = fmaxf(M, __shfl_xor(M, 8)); M = fmaxf(M, __shfl_xor(M, 16));
      const float f = __builtin_amdgcn_exp2f(m - M);
      float L = f * ls;
      L += __shfl_xor(L, 4); L += __shfl_xor(L, 8); L += __shfl_xor(L, 16);
      if (lane < 32) lds_st<float>(L0 + L_F + l * 4, f);
      flag_set(L0, F_FAC, 1u, lane);
      if (lane < 4)
        __builtin_amdgcn_raw_buffer_store_b64(u32x2{nosent(__float_as_uint(M)), nosent(__float_as_uint(L))}, rsb, WS_ST + (kvh * 32 + idx) * 32 + lane * 8, 0, 16);
    }
    TS(5);
    if (role == 3) return;
    flag_wait<1>(L0, F_VISS, 8u);
    TS(6);
    if (role == 0) {
      const unsigned char* base = wo + (size_t)kvh * 512 * 1024 + cb * 64;
      const unsigned voff = (lane >> 2) * 1024 + (lane & 3) * 16;
#pragma unroll 4
      for (int j = 0; j < 32; ++j) dma_nt(base + (size_t)j * 16 * 1024, voff, L0 + (j >> 2) * 16384 + (j & 3) * 1024);
      u32x4 st = {SENT, SENT, SENT, SENT};
      const unsigned o = WS_ST + kvh * 1024 + lane * 16;
      for (int spin = 0; spin < SPIN_G; ++spin) {
        const u64 m = __ballot(pend4(st));
        if (m == 0) break;
        poll_round1(rs, st, o, m);
      }
      const float m0 = __uint_as_float(st[0]), l0 = __uint_as_float(st[1]), m1 = __uint_as_float(st[2]), l1 = __uint_as_float(st[3]);
      float M0 = m0, M1 = m1;
#pragma unroll
      for (int o2 = 2; o2 <= 16; o2 <<= 1) { M0 = fmaxf(M0, __shfl_xor(M0, o2)); M1 = fmaxf(M1, __shfl_xor(M1, o2)); }
      const float e0 = __builtin_amdgcn_exp2f(m0 - M0), e1 = __builtin_amdgcn_exp2f(m1 - M1);
      float S0 = e0 * l0, S1 = e1 * l1;
#pragma unroll
      for (int o2 = 2; o2 <= 16; o2 <<= 1) { S0 += __shfl_xor(S0, o2); S1 += __shfl_xor(S1, o2); }
      const int bb = lane >> 5, ss = (lane >> 1) & 15, h0 = (lane & 1) * 2;
      lds_st<float>(L0 + L_W + ((bb * 4 + h0) * 16 + ss) * 4, e0 / S0);
      lds_st<float>(L0 + L_W + ((bb * 4 + h0 + 1) * 16 + ss) * 4, e1 / S1);
      waitvm<0>();
      flag_set(L0, F_WTS, 1u, lane);
    } else if (role == 1) {
      const unsigned voff = (lane >> 2) * 3584 + (lane & 3) * 16;
      const unsigned char* bg = wg + cc * 64;
      const unsigned char* bu = wu + cc * 64;
#pragma unroll 4
      for (int j = 0; j < 32; ++j) {
        dma_nt(bg + (size_t)j * 16 * 3584, voff, L0 + (j >> 2) * 16384 + (j & 3) * 1024);
        dma_nt(bu + (size_t)j * 16 * 3584, voff, L0 + (j >> 2) * 16384 + 4096 + (j & 3) * 1024);
        if (j == 15) waitvm<16>();
      }
      waitvm<0>();
      flag_set(L0, F_WTS, 1u, lane);
    } else {
      const unsigned voff = (lane >> 1) * 1024 + (lane & 1) * 16;
      const unsigned char* base = wd + dd * 32;
      for (int v = 0; v < 8; ++v) {
#pragma unroll
        for (int i = 0; i < 7; ++i) dma_nt(base + (size_t)(v * 7 + i) * 32 * 1024, voff, L0 + v * 16384 + i * 1024);
        if (v == 3) waitvm<16>();
      }
      waitvm<0>();
      flag_set(L0, F_WTS, 1u, lane);
      u32x4 hv = lane < 32 ? u32x4{SENT, SENT, SENT, SENT} : u32x4{0u, 0u, 0u, 0u};
      const unsigned o = WS_HP + (((lane >> 2) & 7) * 16 + (dd >> 1)) * 128 + ((lane >> 1) & 1) * 64 + (dd & 1) * 32 + (lane & 1) * 16;
      for (int spin = 0; spin < SPIN_G; ++spin) {
        const u64 m = __ballot(pend4(hv));
        if (m == 0) break;
        poll_round1(rs, hv, o, m);
      }
      float hf[8];
#pragma unroll
      for (int j = 0; j < 4; ++j) { hf[2 * j] = bflo(hv[j]); hf[2 * j + 1] = bfhi(hv[j]); }
#pragma unroll
      for (int j = 0; j < 8; ++j) { hf[j] += __shfl_xor(hf[j], 4); hf[j] += __shfl_xor(hf[j], 8); hf[j] += __shfl_xor(hf[j], 16); }
      if (lane < 4) {
        lds_st<f32x4>(L0 + L_H + lane * 32, f32x4{hf[0], hf[1], hf[2], hf[3]});
        lds_st<f32x4>(L0 + L_H + lane * 32 + 16, f32x4{hf[4], hf[5], hf[6], hf[7]});
      }
      flag_set(L0, F_H, 1u, lane);
    }
    TS(7);
    return;
  }

  const unsigned LW = L0 + wave * 16384;
  const size_t chunk = ((size_t)(bat * 8 + kvh) * 8192 + spl * 512 + wave * 64) * 256;
  const unsigned char* kb = kc + chunk;
  const unsigned char* vb = vc + chunk;
  {
    unsigned kvo[4];
#pragma unroll
    for (int a = 0; a < 4; ++a) kvo[a] = g4 * 256 + ((n16 ^ (4 * g4 + a)) * 16);
#pragma unroll
    for (int i = 0; i < 4; ++i) dma_nt(kb + i * 1024, kvo[i & 3], LW + i * 1024);
    asm volatile("s_barrier" ::: "memory");
#pragma unroll
    for (int i = 4; i < 16; ++i) dma_nt(kb + i * 1024, kvo[i & 3], LW + i * 1024);
  }
  u32x4 V[16];
  const unsigned vo = lane * 16;
#pragma unroll
  for (int i = 0; i < 8; ++i) ldv_nt(V[i], vb + i * 1024, vo);
  TS(1);
  flag_wait<1>(L0, F_Q, 1u);
  waitvm<8>();
  TS(2);
  unsigned P[2][16];
  f32x4 acc[2] = {{0.f, 0.f, 0.f, 0.f}, {0.f, 0.f, 0.f, 0.f}};
  {
    u32x4 qf[2][4];
#pragma unroll
    for (int u = 0; u < 2; ++u)
#pragma unroll
      for (int t = 0; t < 4; ++t) qf[u][t] = lds_ld<u32x4>(L0 + L_Q + (2 * u + (n16 >> 3)) * 256 + t * 64 + g4 * 16);
    f32x4 S[2][4];
    const unsigned krow = LW + (4 * (n16 & 3) + (n16 >> 2)) * 256;
#pragma unroll
    for (int T = 0; T < 4; ++T) {
      S[0][T] = f32x4{0.f, 0.f, 0.f, 0.f};
      S[1][T] = f32x4{0.f, 0.f, 0.f, 0.f};
#pragma unroll
      for (int t = 0; t < 4; ++t) {
        const u32x4 kf = lds_ld<u32x4>(krow + T * 4096 + (((4 * t + g4) ^ n16) * 16));
        S[0][T] = mfma(kf, qf[0][t], S[0][T]);
        S[1][T] = mfma(kf, qf[1][t], S[1][T]);
      }
    }
#pragma unroll
    for (int u = 0; u < 2; ++u) {
      float m = S[u][0][0];
#pragma unroll
      for (int T = 0; T < 4; ++T)
#pragma unroll
        for (int r = 0; r < 4; ++r) m = fmaxf(m, S[u][T][r]);
      m = x32_max(x16_max(m));
      const float ms = m * QSCALE;
      float l = 0.f;
#pragma unroll
      for (int T = 0; T < 4; ++T)
#pragma unroll
        for (int r = 0; r < 4; ++r) {
          const float p = __builtin_amdgcn_exp2f(fmaf(S[u][T][r], QSCALE, -ms));
          const unsigned pp = pk(p, p);
          P[u][T * 4 + r] = pp;
          l += bfhi(pp);
        }
      l = x32_sum(x16_sum(l));
      if ((lane & 55) == 0) {
        lds_st<float>(L0 + L_WM + (wave * 4 + 2 * u + (n16 >> 3)) * 4, ms);
        lds_st<float>(L0 + L_WL + (wave * 4 + 2 * u + (n16 >> 3)) * 4, l);
      }
    }
    flag_add(L0, F_STAT, lane);
  }
  TS(3);
  u32x4 msk;
  {
    const unsigned hm = (lane & 1) ? 0xffff0000u : 0x0000ffffu;
    const int dsel = (lane & 7) >> 1;
    msk = u32x4{dsel == 0 ? hm : 0u, dsel == 1 ? hm : 0u, dsel == 2 ? hm : 0u, dsel == 3 ? hm : 0u};
  }
#define PV(i)                                                        \
  do {                                                               \
    const u32x4 b0 = msk & P[0][i], b1 = msk & P[1][i];              \
    acc[0] = mfma(V[i], b0, acc[0]);                                 \
    acc[1] = mfma(V[i], b1, acc[1]);                                 \
  } while (0)
#pragma unroll
  for (int j = 0; j < 8; ++j) {
    ldv_nt(V[8 + j], vb + (8 + j) * 1024, vo);
    asm volatile("s_waitcnt vmcnt(8)" : "+v"(V[j])::"memory");
    PV(j);
  }
  flag_add(L0, F_VISS, lane);
  TS(4);
#define PVW(j, N)                                                    \
  do {                                                               \
    asm volatile("s_waitcnt vmcnt(" #N ")" : "+v"(V[j])::"memory");  \
    PV(j);                                                           \
  } while (0)
  PVW(8, 7); PVW(9, 6); PVW(10, 5); PVW(11, 4); PVW(12, 3); PVW(13, 2); PVW(14, 1);
  flag_wait<0>(L0, F_FAC, 1u);
  const float f0 = lds_ld<float>(L0 + L_F + (wave * 4 + (n16 >> 3)) * 4), f1 = lds_ld<float>(L0 + L_F + (wave * 4 + 2 + (n16 >> 3)) * 4);
  PVW(15, 0);
  TS(5);
  lds_st<f32x4>(LW + L_RB_O + lane * 16, acc[0] * f0);
  lds_st<f32x4>(LW + L_RB_O + 1024 + lane * 16, acc[1] * f1);
  flag_add(L0, F_S1, lane);
  if (wave < 2) {
    flag_wait<0>(L0, F_S1, 8u);
    f32x4 s = lds_ld<f32x4>(L0 + L_RB_O + wave * 1024 + lane * 16);
#pragma unroll
    for (int v = 1; v < 8; ++v) s += lds_ld<f32x4>(L0 + v * 16384 + L_RB_O + wave * 1024 + lane * 16);
    __builtin_amdgcn_raw_buffer_store_b64(u32x2{nosent(pk(s[0], s[1])), nosent(pk(s[2], s[3]))}, rsb,
                                          WS_OP + prod * 1024 + (2 * wave + (n16 >> 3)) * 256 + g4 * 64 + (lane & 7) * 8, 0, 16);
  }
  TS(6);
  if (role == 3) return;

  const unsigned trq = (lane >> 2) & 3, trp = lane & 3;
  if (role == 0) {
    const int hd = wave >> 1, half = wave & 1;
    u32x4 A[4];
    unsigned o[4];
#pragma unroll
    for (int i = 0; i < 4; ++i) {
      A[i] = u32x4{SENT, SENT, SENT, SENT};
      o[i] = WS_OP + (((i >> 1) * 8 + kvh) * 16 + n16) * 1024 + hd * 256 + (2 * half + (i & 1)) * 64 + g4 * 16;
    }
    float xres = 0.f;
    if (wave == 0 && kvh == 0) xres = bflo(*(const unsigned short*)(x + ((g4 >> 1) * 512 + cb * 32 + (g4 & 1) * 16 + n16) * 2));
    flag_wait<0>(L0, F_WTS, 1u);
    u32x4 Bw[2][2];
#pragma unroll
    for (int t = 0; t < 2; ++t)
#pragma unroll
      for (int nt = 0; nt < 2; ++nt) {
        const unsigned a = LW + (32 * t + 8 * trq + 2 * g4) * 64 + nt * 32 + trp * 8;
        Bw[t][nt] = trfrag(a, a + 64);
      }
    const f32x4 w0 = lds_ld<f32x4>(L0 + L_W + ((0 * 4 + hd) * 16 + 4 * g4) * 4), w1 = lds_ld<f32x4>(L0 + L_W + ((1 * 4 + hd) * 16 + 4 * g4) * 4);
    TS(7);
    for (int spin = 0; spin < SPIN_G; ++spin) {
      u64 m[4];
#pragma unroll
      for (int i = 0; i < 4; ++i) m[i] = __ballot(pend4(A[i]));
      if ((m[0] | m[1] | m[2] | m[3]) == 0) break;
      poll_round4(rs, A, o, m);
    }
    TS(8);
    f32x4 val;
#pragma unroll
    for (int bb = 0; bb < 2; ++bb)
#pragma unroll
      for (int nt = 0; nt < 2; ++nt) {
        f32x4 d = {0.f, 0.f, 0.f, 0.f};
        d = mfma(A[bb * 2 + 0], Bw[0][nt], d);
        d = mfma(A[bb * 2 + 1], Bw[1][nt], d);
        const f32x4 ww = bb ? w1 : w0;
        val[bb * 2 + nt] = x32_sum(x16_sum(d[0] * ww[0] + d[1] * ww[1] + d[2] * ww[2] + d[3] * ww[3]));
      }
    if (lane < 16) lds_st<f32x4>(LW + L_RB_B + lane * 16, val);
    flag_add(L0, F_S2, lane);
    if (wave == 0) {
      flag_wait<0>(L0, F_S2, 8u);
      float s = xres;
#pragma unroll
      for (int v = 0; v < 8; ++v) s += lds_ld<float>(L0 + v * 16384 + L_RB_B + n16 * 16 + g4 * 4);
      const float nb = dpp<XOR1>(s);
      if ((lane & 1) == 0)
        __builtin_amdgcn_raw_buffer_store_b32(nosent(pk(s, nb)), rsb, WS_HP + (kvh * 16 + cb) * 128 + (g4 >> 1) * 64 + ((g4 & 1) * 16 + n16) * 2, 0, 16);
      TS(9);
    }
  } else if (role == 1) {
    u32x4 A0 = {SENT, SENT, SENT, SENT}, A1 = {SENT, SENT, SENT, SENT};
    const unsigned o0 = WS_HP + ((n16 & 7) * 16 + 2 * wave) * 128 + (n16 >> 3) * 64 + g4 * 16, o1 = o0 + 128;
    const u32x4 ones = {0x3F803F80u, 0x3F803F80u, 0x3F803F80u, 0x3F803F80u};
    flag_wait<0>(L0, F_WTS, 1u);
    u32x4 Bw[2][2][2];
#pragma unroll
    for (int mt = 0; mt < 2; ++mt)
#pragma unroll
      for (int t = 0; t < 2; ++t)
#pragma unroll
        for (int nt = 0; nt < 2; ++nt) {
          const unsigned a = LW + mt * 4096 + (32 * t + 8 * g4 + trq) * 64 + nt * 32 + trp * 8;
          Bw[mt][t][nt] = trfrag(a, a + 256);
        }
    {
      f32x4 cs;
#pragma unroll
      for (int nt = 0; nt < 2; ++nt)
#pragma unroll
        for (int mt = 0; mt < 2; ++mt) {
          f32x4 d = {0.f, 0.f, 0.f, 0.f};
          d = mfma(ones, Bw[mt][0][nt], d);
          d = mfma(ones, Bw[mt][1][nt], d);
          cs[nt * 2 + mt] = d[0];
        }
      if (lane < 16) lds_st<f32x4>(LW + L_RB_CS + lane * 16, cs);
      flag_add(L0, F_S3, lane);
    }
    f32x2 csum = {0.f, 0.f};
    if (wave == 0) {
      flag_wait<0>(L0, F_S3, 8u);
#pragma unroll
      for (int v = 0; v < 8; ++v) csum += lds_ld<f32x2>(L0 + v * 16384 + L_RB_CS + n16 * 16 + (g4 & 1) * 8);
    }
    TS(7);
    for (int spin = 0; spin < SPIN_G; ++spin) {
      const u64 m0 = __ballot(pend4(A0)), m1 = __ballot(pend4(A1));
      if ((m0 | m1) == 0) break;
      poll_round2(rs, A0, A1, o0, o1, m0, m1);
    }
    TS(8);
    f32x4 gr = {0.f, 0.f, 0.f, 0.f}, s1 = {0.f, 0.f, 0.f, 0.f};
    gr = mfma(A0, A0, gr); s1 = mfma(A0, ones, s1);
    gr = mfma(A1, A1, gr); s1 = mfma(A1, ones, s1);
    f32x4 val;
#pragma unroll
    for (int nt = 0; nt < 2; ++nt)
#pragma unroll
      for (int mt = 0; mt < 2; ++mt) {
        f32x4 d = {0.f, 0.f, 0.f, 0.f};
        d = mfma(A0, Bw[mt][0][nt], d);
        d = mfma(A1, Bw[mt][1][nt], d);
        val[nt * 2 + mt] = x16_sum(d[0] + d[1] + d[2] + d[3]);
      }
    float gs = ((n16 >> 3) == (g4 >> 1)) ? (gr[0] + gr[1] + gr[2] + gr[3]) : 0.f;
    gs += dpp<XOR1>(gs); gs += dpp<XOR2>(gs); gs += dpp<XOR7>(gs);
    gs = x16_sum(gs);
    const float ss = x16_sum(s1[0] + s1[1] + s1[2] + s1[3]);
    if ((g4 & 1) == 0) {
      lds_st<f32x4>(LW + L_RB_C + ((g4 >> 1) * 16 + n16) * 16, val);
      if (n16 == (g4 >> 1) * 8) lds_st<f32x2>(LW + L_RB_C + 512 + (g4 >> 1) * 8, f32x2{ss, gs});
    }
    flag_add(L0, F_S2, lane);
    if (wave == 0) {
      flag_wait<0>(L0, F_S2, 8u);
      f32x2 gu = {0.f, 0.f}, st = {0.f, 0.f};
#pragma unroll
      for (int v = 0; v < 8; ++v) {
        gu += lds_ld<f32x2>(L0 + v * 16384 + L_RB_C + ((g4 >> 1) * 16 + n16) * 16 + (g4 & 1) * 8);
        st += lds_ld<f32x2>(L0 + v * 16384 + L_RB_C + 512 + (g4 >> 1) * 8);
      }
      const float mu = st[0] * (1.f / 512.f);
      const float r = 1.f / sqrtf(fmaxf(st[1] * (1.f / 512.f) - mu * mu, 0.f) + 1e-5f);
      const float gg = r * (gu[0] - mu * csum[0]), uu = r * (gu[1] - mu * csum[1]);
      const float a = gg / (1.f + __expf(-gg)) * uu;
      const float nb = dpp<XOR1>(a);
      if ((lane & 1) == 0)
        __builtin_amdgcn_raw_buffer_store_b32(nosent(pk(a, nb)), rsb, WS_ACT + cc * 128 + (g4 >> 1) * 64 + ((g4 & 1) * 16 + n16) * 2, 0, 16);
      TS(9);
    }
  } else {
    u32x4 A[7];
#pragma unroll
    for (int i = 0; i < 7; ++i) A[i] = n16 < 2 ? u32x4{SENT, SENT, SENT, SENT} : u32x4{0u, 0u, 0u, 0u};
    const unsigned o = WS_ACT + wave * 7 * 128 + (n16 & 1) * 64 + g4 * 16;
    flag_wait<0>(L0, F_WTS, 1u);
    u32x4 Bw[7];
#pragma unroll
    for (int i = 0; i < 7; ++i) {
      const unsigned a = LW + (32 * i + 8 * g4 + trq) * 32 + trp * 8;
      Bw[i] = trfrag(a, a + 128);
    }
    TS(7);
    for (int spin = 0; spin < SPIN_G; ++spin) {
      u64 m[7], any = 0;
#pragma unroll
      for (int i = 0; i < 7; ++i) { m[i] = __ballot(pend4(A[i])); any |= m[i]; }
      if (any == 0) break;
      poll_round7(rs, A, o, m);
    }
    TS(8);
    f32x4 d0 = {0.f, 0.f, 0.f, 0.f}, d1 = {0.f, 0.f, 0.f, 0.f};
#pragma unroll
    for (int i = 0; i < 7; ++i) { if (i & 1) d1 = mfma(A[i], Bw[i], d1); else d0 = mfma(A[i], Bw[i], d0); }
    if (lane < 16) lds_st<f32x2>(LW + L_RB_D + lane * 8, f32x2{d0[0] + d1[0], d0[1] + d1[1]});
    flag_add(L0, F_S2, lane);
    if (wave == 0) {
      flag_wait<0>(L0, F_H, 1u);
      flag_wait<0>(L0, F_S2, 8u);
      float s = lds_ld<float>(L0 + L_H + ((g4 & 1) * 16 + n16) * 4);
#pragma unroll
      for (int v = 0; v < 8; ++v) s += lds_ld<float>(L0 + v * 16384 + L_RB_D + n16 * 8 + (g4 & 1) * 4);
      const float nb = dpp<XOR1>(s);
      if ((lane & 1) == 0 && lane < 32) out[((g4 & 1) * 512 + dd * 16 + n16) >> 1] = pk(s, nb);
      TS(9);
    }
  }
}
__global__ void delay_kernel(unsigned char* ws) {
  __shared__ u64 mn[256], mx[256];
  const int w = threadIdx.x;
  const u64* ts = (const u64*)(ws + WS_TS);
  u64 a = ~0ull, b = 0;
  for (int v = 0; v < 9; ++v) a = min(a, ts[(w * 9 + v) * 16]);
  const int idx = w >> 3; const int r = idx < 16 ? 0 : idx < 23 ? 1 : idx < 27 ? 2 : 3;
  if (-1 < 0 || r == -1) for (int v = 0; v <= 8; ++v) b = max(b, ts[(w * 9 + v) * 16 + 0]);
  mn[w] = a; mx[w] = b;
  __syncthreads();
  if (w == 0) {
    for (int i = 1; i < 256; ++i) { a = min(a, mn[i]); b = max(b, mx[i]); }
    u64 d = b - a; if (d > 20000) d = 20000;
    u64 t0 = __builtin_amdgcn_s_memrealtime();
    while (__builtin_amdgcn_s_memrealtime() - t0 < d * 10) {}
  }
}
}

extern "C" void kernel_launch(void* const* d_in, const int* in_sizes, int n_in, void* d_out, int out_size, void* d_ws, size_t ws_size,
                              hipStream_t stream) {
  (void)in_sizes; (void)n_in; (void)out_size; (void)ws_size;
  (void)hipFuncSetAttribute((const void*)fk::fused_kernel, hipFuncAttributeMaxDynamicSharedMemorySize, fk::L_END);
  fk::fused_kernel<<<fk::NWG, fk::NTHR, fk::L_END, stream>>>(
      (const unsigned char*)d_in[0], (const unsigned char*)d_in[1], (const unsigned char*)d_in[2], (const unsigned char*)d_in[3],
      (const unsigned char*)d_in[4], (const unsigned char*)d_in[6], (const unsigned char*)d_in[7], (const unsigned char*)d_in[8],
      (unsigned*)d_out, (unsigned char*)d_ws);
  fk::delay_kernel<<<1, 256, 0, stream>>>((unsigned char*)d_ws);
}
```

```cpp
#include <hip/hip_runtime.h>
#include <hip/hip_bf16.h>
#include <stdint.h>

namespace fk {
typedef unsigned u32x4 __attribute__((ext_vector_type(4)));
typedef unsigned u32x2 __attribute__((ext_vector_type(2)));
typedef float f32x4 __attribute__((ext_vector_type(4)));
typedef float f32x2 __attribute__((ext_vector_type(2)));
typedef short s16x4 __attribute__((ext_vector_type(4)));
typedef __bf16 bf16x8 __attribute__((ext_vector_type(8)));
typedef __bf16 bf16x2 __attribute__((ext_vector_type(2)));
typedef unsigned long long u64;
#define LAS __attribute__((address_space(3)))
#define GAS __attribute__((address_space(1)))
#define RLX_AGENT __ATOMIC_RELAXED, __HIP_MEMORY_SCOPE_AGENT

#define FK_TS 0
#define KWIN 8
#define VN1 12

#define KW_B 6
#define KW_N 6
#define KISS_AT 31
#define DMA_B 8u
#define DMA_C 8u
#define ROT 1
#define EVENC 1
#define ST_AT 4u
#define QDELAY 40
constexpr unsigned SENT = 0xFFFFFFFFu;
constexpr int NWG = 256, NWAVES = 13, NTHR = NWAVES * 64;
constexpr int WS_EPOCH = 0;
constexpr int WS_FL = 4096;
constexpr int WS_QP = 8192;
constexpr int WS_ST = WS_QP + 131072;
constexpr int WS_OP = WS_ST + 8192;
constexpr int WS_YP = WS_OP + 262144;
constexpr int WS_END = WS_YP + 72 * 2048;
constexpr int WS_TS = 1 << 20;
constexpr int L_RB_O = 12288;
constexpr int L_WQ = 131072, L_P = L_WQ  , L_X = L_WQ + 8192  , L_Q = L_WQ + 16384, L_WM = L_Q + 1024, L_WL = L_WM + 64, L_F = L_WL + 64,
              L_W = L_F + 64, L_ACT = L_W + 512, L_FLAGS = L_ACT + 128, L_END = L_FLAGS + 64;
enum { F_Q = 0, F_STAT = 1, F_FAC = 2, F_VISS = 3, F_WQ = 4, F_KISS = 5, F_S1 = 6, F_S2 = 7, F_S3 = 8, F_ALL = 9, F_W = 10, F_P0 = 12   };
constexpr int SPIN_G = 1 << 18;
constexpr int SPIN_L = 1 << 22;
constexpr float QSCALE = 0.088388347648318440f * 1.4426950408889634f;
constexpr unsigned ONE2 = 0x3F803F80u;

__device__ __forceinline__ f32x4 mfma(u32x4 a, u32x4 b, f32x4 c) {
  return __builtin_amdgcn_mfma_f32_16x16x32_bf16(__builtin_bit_cast(bf16x8, a), __builtin_bit_cast(bf16x8, b), c, 0, 0, 0);
}
__device__ __forceinline__ unsigned pk(float lo, float hi) { bf16x2 v = {(__bf16)lo, (__bf16)hi}; return __builtin_bit_cast(unsigned, v); }
__device__ __forceinline__ unsigned nosent(unsigned x) { return x < 0xFFFFFFFEu ? x : 0xFFFFFFFEu; }
__device__ __forceinline__ float bflo(unsigned u) { return __builtin_bit_cast(float, u << 16); }
__device__ __forceinline__ float bfhi(unsigned u) { return __builtin_bit_cast(float, u & 0xffff0000u); }
__device__ __forceinline__ float x16_sum(float x) {
  auto s = __builtin_amdgcn_permlane16_swap(__float_as_uint(x), __float_as_uint(x), false, false);
  return __uint_as_float(s[0]) + __uint_as_float(s[1]);
}
__device__ __forceinline__ float x32_sum(float x) {
  auto s = __builtin_amdgcn_permlane32_swap(__float_as_uint(x), __float_as_uint(x), false, false);
  return __uint_as_float(s[0]) + __uint_as_float(s[1]);
}
__device__ __forceinline__ float x16_max(float x) {
  auto s = __builtin_amdgcn_permlane16_swap(__float_as_uint(x), __float_as_uint(x), false, false);
  return fmaxf(__uint_as_float(s[0]), __uint_as_float(s[1]));
}
__device__ __forceinline__ float x32_max(float x) {
  auto s = __builtin_amdgcn_permlane32_swap(__float_as_uint(x), __float_as_uint(x), false, false);
  return fmaxf(__uint_as_float(s[0]), __uint_as_float(s[1]));
}
template <int CTRL> __device__ __forceinline__ float dpp(float x) {
  return __builtin_bit_cast(float, __builtin_amdgcn_mov_dpp(__builtin_bit_cast(int, x), CTRL, 0xf, 0xf, true));
}
constexpr int XOR1 = 0xB1, XOR2 = 0x4E, XOR7 = 0x141, ROR4 = 0x124, ROR8 = 0x128;
__device__ __forceinline__ float row_sum16(float x) { x += dpp<XOR1>(x); x += dpp<XOR2>(x); x += dpp<XOR7>(x); x += dpp<ROR8>(x); return x; }
__device__ __forceinline__ float wave_sum(float x) { return x32_sum(x16_sum(row_sum16(x))); }

template <int N> __device__ __forceinline__ void waitvm() { asm volatile("s_waitcnt vmcnt(%0)" ::"i"(N) : "memory"); }
__device__ __forceinline__ void dma_nt(const void* sbase, unsigned voff, unsigned ldst) {
  unsigned keep;
  asm volatile("s_mov_b32 %0, m0\n\ts_mov_b32 m0, %3\n\ts_nop 0\n\tglobal_load_lds_dwordx4 %1, %2 nt\n\ts_mov_b32 m0, %0"
               : "=&s"(keep) : "v"(voff), "s"(sbase), "s"(ldst) : "memory");
}
__device__ __forceinline__ void ldv_nt(u32x4& dst, const void* sbase, unsigned voff) {
  asm volatile("global_load_dwordx4 %0, %1, %2 nt" : "=v"(dst) : "v"(voff), "s"(sbase) : "memory");
}
__device__ __forceinline__ void ldv(u32x4& dst, const void* sbase, unsigned voff) {
  asm volatile("global_load_dwordx4 %0, %1, %2" : "=v"(dst) : "v"(voff), "s"(sbase) : "memory");
}
__device__ __forceinline__ u32x2 trread(unsigned addr) {
  return __builtin_bit_cast(u32x2, __builtin_amdgcn_ds_read_tr16_b64_v4i16((LAS s16x4*)(uintptr_t)addr));
}
__device__ __forceinline__ u32x4 trfrag(unsigned a0, unsigned a1) { u32x2 lo = trread(a0), hi = trread(a1); return u32x4{lo[0], lo[1], hi[0], hi[1]}; }
template <typename T> __device__ __forceinline__ T lds_ld(unsigned addr) { return *(LAS T*)(uintptr_t)addr; }
template <typename T> __device__ __forceinline__ void lds_st(unsigned addr, T v) { *(LAS T*)(uintptr_t)addr = v; }
__device__ __forceinline__ void flag_set(unsigned base, int f, unsigned v, int) {
  asm volatile("s_mov_b64 exec, 1\n\tds_write_b32 %0, %1\n\ts_mov_b64 exec, -1" ::"v"(base + L_FLAGS + f * 4), "v"(v) : "memory");
}
__device__ __forceinline__ void flag_add(unsigned base, int f, int) {
  asm volatile("s_mov_b64 exec, 1\n\tds_add_u32 %0, %1\n\ts_mov_b64 exec, -1" ::"v"(base + L_FLAGS + f * 4), "v"(1u) : "memory");
}
__device__ __forceinline__ void wg_barrier() { asm volatile("s_waitcnt lgkmcnt(0)\n\ts_barrier" ::: "memory"); }
template <int ST> __device__ __forceinline__ void lds_ld8_b64(f32x2 (&r)[8], unsigned a) {
  asm volatile("ds_read_b64 %0, %8\n\tds_read_b64 %1, %8 offset:%9\n\tds_read_b64 %2, %8 offset:%10\n\tds_read_b64 %3, %8 offset:%11\n\t"
               "ds_read_b64 %4, %8 offset:%12\n\tds_read_b64 %5, %8 offset:%13\n\tds_read_b64 %6, %8 offset:%14\n\tds_read_b64 %7, %8 offset:%15\n\t"
               "s_waitcnt lgkmcnt(0)"
               : "=&v"(r[0]), "=&v"(r[1]), "=&v"(r[2]), "=&v"(r[3]), "=&v"(r[4]), "=&v"(r[5]), "=&v"(r[6]), "=&v"(r[7])
               : "v"(a), "i"(ST), "i"(2 * ST), "i"(3 * ST), "i"(4 * ST), "i"(5 * ST), "i"(6 * ST), "i"(7 * ST) : "memory");
}
__device__ __forceinline__ void lds_ld16_b64(f32x2 (&r)[8], f32x2 (&c)[8], unsigned a) {
  asm volatile("ds_read_b64 %0, %16 offset:8192\n\tds_read_b64 %1, %16 offset:8704\n\tds_read_b64 %2, %16 offset:9216\n\tds_read_b64 %3, %16 offset:9728\n\t"
               "ds_read_b64 %4, %16 offset:10240\n\tds_read_b64 %5, %16 offset:10752\n\tds_read_b64 %6, %16 offset:11264\n\tds_read_b64 %7, %16 offset:11776\n\t"
               "ds_read_b64 %8, %16\n\tds_read_b64 %9, %16 offset:1024\n\tds_read_b64 %10, %16 offset:2048\n\tds_read_b64 %11, %16 offset:3072\n\t"
               "ds_read_b64 %12, %16 offset:4096\n\tds_read_b64 %13, %16 offset:5120\n\tds_read_b64 %14, %16 offset:6144\n\tds_read_b64 %15, %16 offset:7168\n\t"
               "s_waitcnt lgkmcnt(0)"
               : "=&v"(r[0]), "=&v"(r[1]), "=&v"(r[2]), "=&v"(r[3]), "=&v"(r[4]), "=&v"(r[5]), "=&v"(r[6]), "=&v"(r[7]),
                 "=&v"(c[0]), "=&v"(c[1]), "=&v"(c[2]), "=&v"(c[3]), "=&v"(c[4]), "=&v"(c[5]), "=&v"(c[6]), "=&v"(c[7])
               : "v"(a) : "memory");
}
template <int ST> __device__ __forceinline__ void lds_ld8_b128(f32x4 (&r)[8], unsigned a, unsigned b) {
  asm volatile("ds_read_b128 %0, %8\n\tds_read_b128 %1, %8 offset:%10\n\tds_read_b128 %2, %8 offset:%11\n\tds_read_b128 %3, %8 offset:%12\n\t"
               "ds_read_b128 %4, %9\n\tds_read_b128 %5, %9 offset:%10\n\tds_read_b128 %6, %9 offset:%11\n\tds_read_b128 %7, %9 offset:%12\n\t"
               "s_waitcnt lgkmcnt(0)"
               : "=&v"(r[0]), "=&v"(r[1]), "=&v"(r[2]), "=&v"(r[3]), "=&v"(r[4]), "=&v"(r[5]), "=&v"(r[6]), "=&v"(r[7])
               : "v"(a), "v"(b), "i"(ST), "i"(2 * ST), "i"(3 * ST) : "memory");
}
template <int SLP> __device__ __forceinline__ void flag_wait(unsigned base, int f, unsigned v) {
  asm volatile("" ::: "memory");
#pragma nounroll
  for (int i = 0; i < SPIN_L; ++i) {
    unsigned x = *(volatile LAS unsigned*)(uintptr_t)(base + L_FLAGS + f * 4);
    if ((unsigned)__builtin_amdgcn_readfirstlane(x) >= v) break;
    if (SLP) __builtin_amdgcn_s_sleep(SLP);
  }
  asm volatile("" ::: "memory");
}
template <int SLP> __device__ __forceinline__ unsigned flag_get(unsigned base, int f) {
  asm volatile("" ::: "memory");
  unsigned x = 0;
#pragma nounroll
  for (int i = 0; i < SPIN_L; ++i) {
    x = __builtin_amdgcn_readfirstlane(*(volatile LAS unsigned*)(uintptr_t)(base + L_FLAGS + f * 4));
    if (x) break;
    if (SLP) __builtin_amdgcn_s_sleep(SLP);
  }
  asm volatile("" ::: "memory");
  return x;
}
__device__ __forceinline__ bool pend4(u32x4 v) {
  unsigned a = v[0] > v[1] ? v[0] : v[1], b = v[2] > v[3] ? v[2] : v[3];
  return (a > b ? a : b) == SENT;
}
__device__ __forceinline__ void poll_round1d(u32x4 rs, unsigned& v0, unsigned o0, u64 m0) {
  u64 keep;
  asm volatile("s_mov_b64 %0, exec\n\t"
               "s_mov_b64 exec, %4\n\tbuffer_load_dword %1, %2, %3, 0 offen sc1\n\t"
               "s_mov_b64 exec, %0\n\ts_waitcnt vmcnt(0)"
               : "=&s"(keep), "+v"(v0) : "v"(o0), "s"(rs), "s"(m0) : "memory");
}
__device__ __forceinline__ void poll_round1(u32x4 rs, u32x4& v0, unsigned o0, u64 m0) {
  u64 keep;
  asm volatile("s_mov_b64 %0, exec\n\t"
               "s_mov_b64 exec, %4\n\tbuffer_load_dwordx4 %1, %2, %3, 0 offen sc1\n\t"
               "s_mov_b64 exec, %0\n\ts_waitcnt vmcnt(0)"
               : "=&s"(keep), "+v"(v0) : "v"(o0), "s"(rs), "s"(m0) : "memory");
}
__device__ __forceinline__ void poll_round2(u32x4 rs, u32x4& v0, u32x4& v1, unsigned o0, unsigned o1, u64 m0, u64 m1) {
  u64 keep;
  asm volatile("s_mov_b64 %0, exec\n\t"
               "s_mov_b64 exec, %6\n\tbuffer_load_dwordx4 %1, %3, %5, 0 offen sc1\n\t"
               "s_mov_b64 exec, %7\n\tbuffer_load_dwordx4 %2, %4, %5, 0 offen sc1\n\t"
               "s_mov_b64 exec, %0\n\ts_waitcnt vmcnt(0)"
               : "=&s"(keep), "+v"(v0), "+v"(v1) : "v"(o0), "v"(o1), "s"(rs), "s"(m0), "s"(m1) : "memory");
}
__device__ __forceinline__ void poll_round4(u32x4 rs, u32x4 (&v)[4], const unsigned (&o)[4], const u64 (&m)[4]) {
  u64 keep;
  asm volatile("s_mov_b64 %0, exec\n\t"
               "s_mov_b64 exec, %10\n\tbuffer_load_dwordx4 %1, %5, %9, 0 offen sc1\n\t"
               "s_mov_b64 exec, %11\n\tbuffer_load_dwordx4 %2, %6, %9, 0 offen sc1\n\t"
               "s_mov_b64 exec, %12\n\tbuffer_load_dwordx4 %3, %7, %9, 0 offen sc1\n\t"
               "s_mov_b64 exec, %13\n\tbuffer_load_dwordx4 %4, %8, %9, 0 offen sc1\n\t"
               "s_mov_b64 exec, %0\n\ts_waitcnt vmcnt(0)"
               : "=&s"(keep), "+v"(v[0]), "+v"(v[1]), "+v"(v[2]), "+v"(v[3])
               : "v"(o[0]), "v"(o[1]), "v"(o[2]), "v"(o[3]), "s"(rs), "s"(m[0]), "s"(m[1]), "s"(m[2]), "s"(m[3]) : "memory");
}
__device__ __forceinline__ void poll_round8(u32x4 rs, u32x4 (&v)[8], unsigned o, const u64 (&m)[8]) {
  u64 keep;
  asm volatile("s_mov_b64 %0, exec\n\t"
               "s_mov_b64 exec, %11\n\tbuffer_load_dwordx4 %1, %9, %10, 0 offen sc1\n\t"
               "s_mov_b64 exec, %12\n\tbuffer_load_dwordx4 %2, %9, %10, 0 offen offset:1024 sc1\n\t"
               "s_mov_b64 exec, %13\n\tbuffer_load_dwordx4 %3, %9, %10, 0 offen offset:2048 sc1\n\t"
               "s_mov_b64 exec, %14\n\tbuffer_load_dwordx4 %4, %9, %10, 0 offen offset:3072 sc1\n\t"
               "s_mov_b64 exec, %15\n\tbuffer_load_dwordx4 %5, %9, %10, %19 offen sc1\n\t"
               "s_mov_b64 exec, %16\n\tbuffer_load_dwordx4 %6, %9, %10, %19 offen offset:1024 sc1\n\t"
               "s_mov_b64 exec, %17\n\tbuffer_load_dwordx4 %7, %9, %10, %19 offen offset:2048 sc1\n\t"
               "s_mov_b64 exec, %18\n\tbuffer_load_dwordx4 %8, %9, %10, %19 offen offset:3072 sc1\n\t"
               "s_mov_b64 exec, %0\n\ts_waitcnt vmcnt(0)"
               : "=&s"(keep), "+v"(v[0]), "+v"(v[1]), "+v"(v[2]), "+v"(v[3]), "+v"(v[4]), "+v"(v[5]), "+v"(v[6]), "+v"(v[7])
               : "v"(o), "s"(rs), "s"(m[0]), "s"(m[1]), "s"(m[2]), "s"(m[3]), "s"(m[4]), "s"(m[5]), "s"(m[6]), "s"(m[7]), "s"(4096u) : "memory");
}
__device__ __forceinline__ void issue4(u32x4 rs, u32x4 (&v)[4], const unsigned (&o)[4]) {
  asm volatile("buffer_load_dwordx4 %0, %4, %8, 0 offen sc1\n\tbuffer_load_dwordx4 %1, %5, %8, 0 offen sc1\n\t"
               "buffer_load_dwordx4 %2, %6, %8, 0 offen sc1\n\tbuffer_load_dwordx4 %3, %7, %8, 0 offen sc1"
               : "=&v"(v[0]), "=&v"(v[1]), "=&v"(v[2]), "=&v"(v[3]) : "v"(o[0]), "v"(o[1]), "v"(o[2]), "v"(o[3]), "s"(rs) : "memory");
}
__device__ __forceinline__ void land4(u32x4 (&v)[4]) {
  asm volatile("s_waitcnt vmcnt(0)" : "+v"(v[0]), "+v"(v[1]), "+v"(v[2]), "+v"(v[3])::"memory");
}
__device__ __forceinline__ void poll2(u32x4 rs, u32x4& A0, u32x4& A1, unsigned o0, unsigned o1) {
#pragma nounroll
  for (int spin = 0; spin < SPIN_G; ++spin) {
    const u64 m0 = __ballot(pend4(A0)), m1 = __ballot(pend4(A1));
    if ((m0 | m1) == 0) break;
    poll_round2(rs, A0, A1, o0, o1, m0, m1);
  }
}
__device__ __forceinline__ void poll4(u32x4 rs, u32x4 (&A)[4], const unsigned (&o)[4]) {
#pragma nounroll
  for (int spin = 0; spin < SPIN_G; ++spin) {
    u64 m[4];
#pragma unroll
    for (int i = 0; i < 4; ++i) m[i] = __ballot(pend4(A[i]));
    if ((m[0] | m[1] | m[2] | m[3]) == 0) break;
    poll_round4(rs, A, o, m);
  }
}
__device__ __forceinline__ void poll1(u32x4 rs, u32x4& A, unsigned o) {
#pragma nounroll
  for (int spin = 0; spin < SPIN_G; ++spin) {
    const u64 m = __ballot(pend4(A));
    if (m == 0) break;
    poll_round1(rs, A, o, m);
  }
}

#define TS(k) do {} while (0)
#define TSEND() do {} while (0)

__global__ void __launch_bounds__(NTHR, 4)
fused_kernel(unsigned char* __restrict__ ws, const unsigned char* __restrict__ x, const unsigned char* __restrict__ wq, const unsigned char* __restrict__ kc,
             const unsigned char* __restrict__ vc, const unsigned char* __restrict__ wo, const unsigned char* __restrict__ wg,
             const unsigned char* __restrict__ wu, const unsigned char* __restrict__ wd, unsigned short* __restrict__ out) {
  extern __shared__ __attribute__((aligned(16))) unsigned char smem[];
  const unsigned L0 = (unsigned)(uintptr_t)(LAS unsigned char*)smem;
  const int tid = threadIdx.x, lane = tid & 63, wave0 = __builtin_amdgcn_readfirstlane(tid >> 6);
  const int w = blockIdx.x, kvh = w & 7, idx = w >> 3, bat = idx >> 4, spl = idx & 15;
  const int n16 = lane & 15, g4 = lane >> 4;
  const int prod = (bat * 8 + kvh) * 16 + spl;
  const int tk = (kvh & 1) == 0 ? (idx < 14 ? 7 * kvh + idx : idx < 30 ? 56 + 16 * kvh + (idx - 14) : 255)
                                : (idx < 16 ? 56 + 16 * kvh + idx : idx < 24 ? 184 + 4 * (kvh - 1) + (idx - 16) : 255);
  const int role = tk < 56 ? 1 : tk < 184 ? 0 : tk < 216 ? 2 : 3;
  const int rq = idx & 3, cbk = idx >> 2;
  const __amdgpu_buffer_rsrc_t rsb = __builtin_amdgcn_make_buffer_rsrc(ws, 0, WS_END, 0x00020000);
  const u32x4 rs = {(unsigned)(uintptr_t)ws, (unsigned)((uintptr_t)ws >> 32) & 0xffffu, (unsigned)WS_END, 0x00020000u};
  const unsigned trq = (lane >> 2) & 3, trp = lane & 3;
  const u32x4 ones = {ONE2, ONE2, ONE2, ONE2};
  const size_t chunk = ((size_t)(bat * 8 + kvh) * 8192 + spl * 512) * 256;

  if (wave0 == 0) {
    __builtin_amdgcn_s_setprio(3);
    if (lane < 16) lds_st<unsigned>(L0 + L_FLAGS + lane * 4, 0u);
    u32x2 ep;
    asm volatile("global_load_dwordx2 %0, %1, %2 sc1" : "=v"(ep) : "v"(0u), "s"(ws) : "memory");
    u32x4 xs0, xs1, xa[4];
    ldv(xs0, x, lane * 16);
    ldv(xs1, x + 1024, lane * 16);
    asm volatile("s_waitcnt lgkmcnt(0)\n\ts_barrier" ::: "memory");
    {
      const u32x4 s4 = {SENT, SENT, SENT, SENT};
      const int OOB = 0x7ffffff0;
      int o1 = OOB;
      if (lane < 2) o1 = WS_ST + (kvh * 32 + idx) * 32 + lane * 16;
      const int o2 = (idx >= 16 && idx < 25) ? WS_YP + (kvh * 9 + idx - 16) * 2048 + lane * 16 : OOB;
      asm volatile("buffer_store_dwordx4 %0, %1, %4, 0 offen sc1\n\t"
                   "buffer_store_dwordx4 %0, %2, %4, 0 offen sc1\n\t"
                   "buffer_store_dwordx4 %0, %3, %4, 0 offen sc1\n\t"
                   "buffer_store_dwordx4 %0, %3, %4, 0 offen offset:1024 sc1\n\ts_nop 1"
                   :: "v"(s4), "v"(WS_OP + prod * 1024 + lane * 16), "v"(o1), "v"(o2), "s"(rs) : "memory");
    }
    asm volatile("s_waitcnt vmcnt(4)" : "+v"(ep), "+v"(xs0), "+v"(xs1)::"memory");
    if (g4 == rq) {
      lds_st<u32x4>(L0 + L_Q + n16 * 16, xs0);
      lds_st<u32x4>(L0 + L_Q + 256 + n16 * 16, xs1);
    }
#pragma unroll
    for (int t = 0; t < 4; ++t) xa[t] = lds_ld<u32x4>(L0 + L_Q + (n16 & 1) * 256 + t * 64 + g4 * 16);
    const u64 tag = (((u64)ep[1] << 32) | ep[0]) + 1;
    const unsigned tlo = __builtin_amdgcn_readfirstlane((unsigned)tag), thi = __builtin_amdgcn_readfirstlane((unsigned)(tag >> 32));
    float mu, rr;
    {
      float s0 = 0.f, q0 = 0.f, s1 = 0.f, q1 = 0.f;
#pragma unroll
      for (int j = 0; j < 4; ++j) {
        const float a = bflo(xs0[j]), b = bfhi(xs0[j]), c = bflo(xs1[j]), d = bfhi(xs1[j]);
        s0 += a + b; q0 += a * a + b * b; s1 += c + d; q1 += c * c + d * d;
      }
      s0 = wave_sum(s0); q0 = wave_sum(q0); s1 = wave_sum(s1); q1 = wave_sum(q1);
      const float s = (lane & 1) ? s1 : s0, q = (lane & 1) ? q1 : q0;
      mu = s * (1.f / 512.f);
      rr = 1.f / sqrtf(fmaxf(q * (1.f / 512.f) - mu * mu, 0.f) + 1e-5f);
    }
#pragma unroll
    for (int t = 0; t < 4; ++t) xa[t] = (n16 & 3) == 2 ? ones : xa[t];
    flag_wait<0>(L0, F_WQ, 4u);
    TS(1);
    {
      f32x4 d[4];
      const unsigned a0 = L0 + L_WQ + (8 * g4 + trq) * 128 + trp * 8, sw = (g4 & 1) * 32;
#pragma unroll
      for (int nt = 0; nt < 4; ++nt) {
        d[nt] = f32x4{0.f, 0.f, 0.f, 0.f};
#pragma unroll
        for (int t = 0; t < 4; ++t) d[nt] = mfma(trfrag(a0 + t * 4096 + ((nt * 32) ^ sw), a0 + t * 4096 + ((nt * 32) ^ sw) + 512), xa[t], d[nt]);
      }
      const f32x4 lo = (n16 & 4) ? d[1] : d[0], hi = (n16 & 4) ? d[3] : d[2];
      const f32x4 dd = (n16 & 8) ? hi : lo;
      float qv[4];
#pragma unroll
      for (int r = 0; r < 4; ++r) qv[r] = rr * (dd[r] - mu * dpp<0xAA>(dd[r]));
      if ((n16 & 3) < 2)
        __builtin_amdgcn_raw_buffer_store_b128(u32x4{tlo, thi, pk(qv[0], qv[1]), pk(qv[2], qv[3])}, rsb,
                                               WS_QP + (((kvh * 2 + (n16 & 1)) * 4 + rq) * 128 + cbk * 16 + (n16 >> 2) * 4 + g4) * 16, 0, 16);
    }
    waitvm<1>();
    if (lane == 0) __builtin_amdgcn_raw_buffer_store_b64(u32x2{tlo, thi}, rsb, WS_FL + w * 8, 0, 16);
    TS(2);
    {
      u32x4 v[8];
      const unsigned ob = WS_QP + (kvh * 2 + bat) * 8192 + lane * 16;
#pragma unroll
      for (int i = 0; i < 8; ++i) v[i] = u32x4{~tlo, 0u, 0u, 0u};
      if (QDELAY) __builtin_amdgcn_s_sleep(QDELAY);
#pragma nounroll
      for (int spin = 0; spin < SPIN_G; ++spin) {
        u64 m[8];
#pragma unroll
        for (int i = 0; i < 8; ++i) m[i] = __ballot(v[i][0] != tlo || v[i][1] != thi);
        if ((m[0] | m[1] | m[2] | m[3] | m[4] | m[5] | m[6] | m[7]) == 0) break;
        poll_round8(rs, v, ob, m);
      }
#pragma unroll
      for (int h = 0; h < 2; ++h) {
        u32x2 r;
#pragma unroll
        for (int j = 0; j < 2; ++j) {
          const float lo = (bflo(v[h][2 + j]) + bflo(v[2 + h][2 + j])) + (bflo(v[4 + h][2 + j]) + bflo(v[6 + h][2 + j]));
          const float hi = (bfhi(v[h][2 + j]) + bfhi(v[2 + h][2 + j])) + (bfhi(v[4 + h][2 + j]) + bfhi(v[6 + h][2 + j]));
          r[j] = pk(lo, hi);
        }
        lds_st<u32x2>(L0 + L_Q + (64 * h + lane) * 8, r);
      }
      flag_set(L0, F_Q, 1u, lane);
    }
    TS(3);
#pragma nounroll
    for (int spin = 0; spin < SPIN_G; ++spin) {
      asm volatile("" ::: "memory");
      const u32x4 f0 = __builtin_amdgcn_raw_buffer_load_b128(rsb, WS_FL + lane * 16, 0, 16);
      const u32x4 f1 = __builtin_amdgcn_raw_buffer_load_b128(rsb, WS_FL + 1024 + lane * 16, 0, 16);
      const bool ok = f0[0] == tlo && f0[1] == thi && f0[2] == tlo && f0[3] == thi && f1[0] == tlo && f1[1] == thi && f1[2] == tlo && f1[3] == thi;
      if (__all(ok)) break;
    }
    flag_set(L0, F_ALL, 1u, lane);
    if (w == NWG - 1 && lane == 0) __hip_atomic_store((GAS u64*)(ws + WS_EPOCH), tag, RLX_AGENT);
    TS(4);
    flag_wait<1>(L0, F_STAT, 4u);
    {
      const float m = lds_ld<float>(L0 + L_WM + n16 * 4), ls = lds_ld<float>(L0 + L_WL + n16 * 4);
      float M = fmaxf(m, dpp<ROR4>(m)); M = fmaxf(M, dpp<ROR8>(M));
      const float f = __builtin_amdgcn_exp2f(m - M);
      float L = f * ls;
      L += dpp<ROR4>(L); L += dpp<ROR8>(L);
      if (lane < 16) lds_st<float>(L0 + L_F + lane * 4, f);
      flag_set(L0, F_FAC, 1u, lane);
      if (lane < 4)
        __builtin_amdgcn_raw_buffer_store_b64(u32x2{nosent(__float_as_uint(M)), nosent(__float_as_uint(L))}, rsb, WS_ST + (kvh * 32 + idx) * 32 + lane * 8, 0, 16);
    }
    TS(5);
    if (role == 0) {
      u32x4 st = {SENT, SENT, SENT, SENT};
      flag_wait<1>(L0, F_VISS, ST_AT);
      poll1(rs, st, WS_ST + ((tk - 56) >> 4) * 1024 + lane * 16);
      const float m0 = __uint_as_float(st[0]), l0 = __uint_as_float(st[1]), m1 = __uint_as_float(st[2]), l1 = __uint_as_float(st[3]);
      float M0 = m0, M1 = m1;
#pragma unroll
      for (int o2 = 2; o2 <= 16; o2 <<= 1) { M0 = fmaxf(M0, __shfl_xor(M0, o2)); M1 = fmaxf(M1, __shfl_xor(M1, o2)); }
      const float e0 = __builtin_amdgcn_exp2f(m0 - M0), e1 = __builtin_amdgcn_exp2f(m1 - M1);
      float S0 = e0 * l0, S1 = e1 * l1;
#pragma unroll
      for (int o2 = 2; o2 <= 16; o2 <<= 1) { S0 += __shfl_xor(S0, o2); S1 += __shfl_xor(S1, o2); }
      const int bb = lane >> 5, ss = (lane >> 1) & 15, h0 = (lane & 1) * 2;
      lds_st<float>(L0 + L_W + ((bb * 4 + h0) * 16 + ss) * 4, e0 / S0);
      lds_st<float>(L0 + L_W + ((bb * 4 + h0 + 1) * 16 + ss) * 4, e1 / S1);
      flag_set(L0, F_W, 1u, lane);
      TS(6);
    }
    TSEND();
    return;
  }

  if (wave0 <= 4) {
    const int kw = wave0 - 1;
    __builtin_amdgcn_s_setprio(2);
    const unsigned LK = L0 + kw * 32768;
    const unsigned char* kb = kc + chunk + kw * 32768;
    const int rk8 = ROT ? (idx & 3) * 2 + (ROT == 2 ? (kw & 1) : 0) : 0;
    {
      unsigned kvo[4];
#pragma unroll
      for (int a = 0; a < 4; ++a) kvo[a] = g4 * 256 + ((n16 ^ (4 * g4 + a)) * 16);
      {
        const unsigned char* base = wq + (size_t)(rq * 128 + kw * 32) * 8192 + (kvh * 512 + cbk * 64) * 2;
        const unsigned voff = (lane >> 3) * 8192 + (lane & 7) * 16;
#pragma unroll
        for (int i = 0; i < 4; ++i) dma_nt(base + (size_t)i * 8 * 8192, voff ^ ((i & 1) * 32), L0 + L_WQ + (kw * 4 + i) * 1024);
      }
#pragma unroll
      for (int i = 0; i < 32; ++i) {
        const unsigned ko = ((i + 4 * rk8) & 31) * 1024;
        dma_nt(kb + ko, kvo[i & 3], LK + ko);
        if (i == 3) {
          asm volatile("s_barrier" ::: "memory");
          waitvm<4>();
          flag_add(L0, F_WQ, lane);
        }
        if (i == KISS_AT) flag_add(L0, F_KISS, lane);
        if (i > 3 && i < 31) {
          if (i >= KWIN - 1) waitvm<KWIN - 1>();
          if (KW_B < KWIN && i >= KW_B - 1 && role == 0) waitvm<KW_B - 1>();
          if (KW_N < KWIN && i >= KW_N - 1 && role >= 2) waitvm<KW_N - 1>();
        }
      }
    }
    TS(1);
    flag_wait<1>(L0, F_Q, 1u);
    TS(2);
    u32x4 qf[4];
#pragma unroll
    for (int t = 0; t < 4; ++t) qf[t] = lds_ld<u32x4>(L0 + L_Q + (n16 & 3) * 256 + t * 64 + g4 * 16);
    f32x4 sel[2];
    const unsigned krow = LK + (4 * (n16 & 3) + (n16 >> 2)) * 256;
#pragma unroll
    for (int hf = 0; hf < 2; ++hf) {
      if (hf == 1) waitvm<0>();
      f32x4 S[4];
#pragma unroll
      for (int T = 0; T < 4; ++T) {
        S[T] = f32x4{0.f, 0.f, 0.f, 0.f};
#pragma unroll
        for (int t = 0; t < 4; ++t) S[T] = mfma(lds_ld<u32x4>(krow + ((4 * hf + T + rk8) & 7) * 4096 + (((4 * t + g4) ^ n16) * 16)), qf[t], S[T]);
      }
      const f32x4 lo = (n16 & 4) ? S[1] : S[0], hi = (n16 & 4) ? S[3] : S[2];
      sel[hf] = (n16 & 8) ? hi : lo;
    }
    float m = fmaxf(fmaxf(fmaxf(sel[0][0], sel[0][1]), fmaxf(sel[0][2], sel[0][3])), fmaxf(fmaxf(sel[1][0], sel[1][1]), fmaxf(sel[1][2], sel[1][3])));
    m = fmaxf(m, dpp<ROR4>(m)); m = fmaxf(m, dpp<ROR8>(m));
    m = x32_max(x16_max(m));
    const float ms = m * QSCALE;
    float l = 0.f;
#pragma unroll
    for (int hf = 0; hf < 2; ++hf) {
      u32x4 pp;
#pragma unroll
      for (int r = 0; r < 4; ++r) {
        const float p = __builtin_amdgcn_exp2f(fmaf(sel[hf][r], QSCALE, -ms));
        pp[r] = pk(p, p);
        l += bfhi(pp[r]);
      }
      const int ta = (4 * hf + (n16 >> 2) + rk8) & 7;
      lds_st<u32x4>(L0 + L_P + (2 * kw + (ta >> 2)) * 1024 + (((n16 & 3) * 4 + g4) * 16 + 4 * (ta & 3)) * 4, pp);
    }
    flag_set(L0, F_P0 + kw, 1u, lane);
    l += dpp<ROR4>(l); l += dpp<ROR8>(l);
    l = x32_sum(x16_sum(l));
    if (lane < 4) {
      lds_st<float>(L0 + L_WM + (kw * 4 + lane) * 4, ms);
      lds_st<float>(L0 + L_WL + (kw * 4 + lane) * 4, l);
    }
    flag_add(L0, F_STAT, lane);
    TS(3);
    __builtin_amdgcn_s_setprio(0);
    const int cc = tk;
    if (role <= 1) flag_wait<1>(L0, F_VISS, role == 0 ? DMA_B : DMA_C);
    if (role == 0) {
      const int rk = (tk - 56) >> 3, cq = (tk - 56) & 7;
      const unsigned char* base = wo + (size_t)(rk * 256 + kw * 64) * 1024 + cq * 128;
      const unsigned voff = (lane >> 3) * 1024 + (((lane & 7) ^ (g4 << 1)) * 16);
#pragma unroll
      for (int j = 0; j < 8; ++j) dma_nt(base + (size_t)j * 8 * 1024, voff, LK + j * 1024);
      TS(4);
      waitvm<0>();
      flag_set(L0, F_P0 + kw, 2u, lane);
      TS(5);
    } else if (role == 1) {
      const unsigned vf[2] = {(lane >> 2) * 3584u + (lane & 3) * 16u, ((lane >> 2) ^ 1) * 3584u + (lane & 3) * 16u};
      const unsigned char* bg = wg + (size_t)kw * 128 * 3584 + cc * 64;
      const unsigned char* bu = wu + (size_t)kw * 128 * 3584 + cc * 64;
#pragma unroll
      for (int j = 0; j < 8; ++j) {
        const int jj = (j + 4 * (cc & 1)) & 7;
        dma_nt(bg + (size_t)jj * 16 * 3584, vf[j & 1], LK + (jj >> 2) * 16384 + (jj & 3) * 1024);
        dma_nt(bu + (size_t)jj * 16 * 3584, vf[j & 1], LK + (jj >> 2) * 16384 + 4096 + (jj & 3) * 1024);
      }
      const unsigned char* bd = wd + (size_t)cc * 32 * 1024 + kw * 256;
      const unsigned voff2 = (lane >> 3) * 1024 + (lane & 7) * 16;
#pragma unroll
      for (int j = 0; j < 8; ++j) dma_nt(bd + (size_t)(j & 3) * 8 * 1024 + (j >> 2) * 128, voff2 ^ ((j & 1) * 32), LK + (j >> 2) * 16384 + 8192 + (j & 3) * 1024);
      TS(4);
      waitvm<8>();
      flag_set(L0, F_P0 + kw, 2u, lane);
      TS(5);
      waitvm<0>();
      flag_set(L0, F_P0 + kw, 3u, lane);
      TS(6);
    }
    waitvm<0>();
    TSEND();
    return;
  }

  const int wave = wave0 - 5;
  const unsigned LW = L0 + wave * 16384;
  const unsigned char* vb = vc + chunk + wave * 16384;
  __builtin_amdgcn_s_setprio(2);
  asm volatile("s_barrier" ::: "memory");
  u32x4 msk;
  {
    const unsigned hm = (lane & 1) ? 0xffff0000u : 0x0000ffffu;
    const int dsel = (lane & 7) >> 1;
    msk = u32x4{dsel == 0 ? hm : 0u, dsel == 1 ? hm : 0u, dsel == 2 ? hm : 0u, dsel == 3 ? hm : 0u};
  }
  u32x4 V[16];
  const unsigned vo = lane * 16;
  const int rv = ROT ? (idx & 3) : 0;
  flag_wait<1>(L0, F_KISS, 4u);
  TS(1);
#pragma unroll
  for (int i = 0; i < VN1; ++i) {
    ldv_nt(V[i], vb + ((i + 4 * rv) & 15) * 1024, vo);
  }
  TS(2);
  flag_wait<1>(L0, F_P0 + (wave >> 1), 1u);
  u32x4 P[2][4];
#pragma unroll
  for (int u = 0; u < 2; ++u)
#pragma unroll
    for (int k = 0; k < 4; ++k) P[u][k] = lds_ld<u32x4>(L0 + L_P + wave * 1024 + ((2 * u + (n16 >> 3)) * 4 + g4) * 64 + ((k + rv) & 3) * 16);
  f32x4 acc[2] = {{0.f, 0.f, 0.f, 0.f}, {0.f, 0.f, 0.f, 0.f}};
  TS(3);
#define PV(j, N)                                                                        \
  do {                                                                                  \
    asm volatile("s_waitcnt vmcnt(%1)" : "+v"(V[j]) : "i"(N) : "memory");               \
    const u32x4 b0 = msk & P[0][(j) >> 2][(j) & 3], b1 = msk & P[1][(j) >> 2][(j) & 3]; \
    acc[0] = mfma(V[j], b0, acc[0]);                                                    \
    acc[1] = mfma(V[j], b1, acc[1]);                                                    \
    __builtin_amdgcn_sched_barrier(0);                                                  \
  } while (0)
#pragma unroll
  for (int j = 0; j < 16 - VN1; ++j) {
    ldv_nt(V[VN1 + j], vb + ((VN1 + j + 4 * rv) & 15) * 1024, vo);
    if (j == 15 - VN1) flag_add(L0, F_VISS, lane);
    PV(2 * j, VN1 - j);
    PV(2 * j + 1, VN1 - j - 1);
  }
  TS(4);
  flag_wait<0>(L0, F_FAC, 1u);
  unsigned oa = L0 + L_RB_O + wave * 1024 + lane * 16, og = WS_OP + prod * 1024 + (2 * wave + (n16 >> 3)) * 256 + g4 * 64 + (lane & 7) * 8;
  asm volatile("" : "+v"(oa), "+v"(og));
  const float f0 = lds_ld<float>(L0 + L_F + ((wave >> 1) * 4 + (n16 >> 3)) * 4), f1 = lds_ld<float>(L0 + L_F + ((wave >> 1) * 4 + 2 + (n16 >> 3)) * 4);
#pragma unroll
  for (int j = 2 * (16 - VN1); j < 16; ++j) PV(j, 15 - j);
  TS(5);
  lds_st<f32x4>(LW + L_RB_O + lane * 16, acc[0] * f0);
  lds_st<f32x4>(LW + L_RB_O + 1024 + lane * 16, acc[1] * f1);
  flag_add(L0, F_S1, lane);
  if (wave < 2) {
    flag_wait<0>(L0, F_S1, 8u);
    f32x4 r[8];
    lds_ld8_b128<16384>(r, oa, oa + 65536);
    const f32x4 s = ((r[0] + r[1]) + (r[2] + r[3])) + ((r[4] + r[5]) + (r[6] + r[7]));
    __builtin_amdgcn_raw_buffer_store_b64(u32x2{nosent(pk(s[0], s[1])), nosent(pk(s[2], s[3]))}, rsb, og, 0, 16);
  }
  TS(6);
  if (role == 3) { TSEND(); return; }
  const u32x4 sel = msk & ones;

  if (role == 0) {
    const int rk = (tk - 56) >> 3, cq = (tk - 56) & 7, kq = wave >> 1, bb = wave & 1;
    const int hd = 2 * (rk & 1) + (kq >> 1);
    const unsigned LK = L0 + kq * 32768;
    u32x4 A0, A1;
    const unsigned o0 = WS_OP + ((bb * 8 + (rk >> 1)) * 16 + (lane >> 3)) * 1024 + hd * 256 + (kq & 1) * 128 + (lane & 7) * 16, o1 = o0 + 8192;
    f32x4 xr0 = {0.f, 0.f, 0.f, 0.f}, xr1 = {0.f, 0.f, 0.f, 0.f};
    if (wave == 0 && rk == 0) {
#pragma unroll
      for (int j = 0; j < 4; ++j) {
        xr0[j] = bflo(*(const unsigned short*)(x + (cq * 64 + j * 16 + n16) * 2));
        xr1[j] = bflo(*(const unsigned short*)(x + (512 + cq * 64 + j * 16 + n16) * 2));
      }
    }
    flag_wait<0>(L0, F_ALL, 1u);
    flag_wait<0>(L0, F_P0 + kq, 2u);
    asm volatile("buffer_load_dwordx4 %0, %2, %4, 0 offen sc1\n\tbuffer_load_dwordx4 %1, %3, %4, 0 offen sc1"
                 : "=&v"(A0), "=&v"(A1) : "v"(o0), "v"(o1), "s"(rs) : "memory");
    u32x4 Bw[2][4];
#pragma unroll
    for (int t = 0; t < 2; ++t)
#pragma unroll
      for (int nt = 0; nt < 4; ++nt) {
        const unsigned a = LK + (32 * t + 8 * trq + 2 * g4) * 128 + ((nt ^ g4) * 32) + trp * 8;
        Bw[t][nt] = trfrag(a, a + 128);
      }
    flag_wait<0>(L0, F_W, 1u);
    const float wa = lds_ld<float>(L0 + L_W + ((bb * 4 + hd) * 16 + 2 * g4 + (n16 >> 3)) * 4), wb = lds_ld<float>(L0 + L_W + ((bb * 4 + hd) * 16 + 8 + 2 * g4 + (n16 >> 3)) * 4);
    const u32x4 sa = msk & pk(wa, wa), sb = msk & pk(wb, wb);
    unsigned sw = LW + 8192 + n16 * 16 + (g4 & 1) * 8, sr = LW + 8192 + (n16 & 1) * 128 + g4 * 16, xw = L0 + L_X + wave * 256 + n16 * 16, xa = L0 + L_X + n16 * 16,
             og2 = WS_YP + rk * 2048 + cq * 256 + n16 * 16;
    const u64 vmask = __ballot((g4 >> 1) == (n16 >> 3));
    asm volatile("" : "+v"(sw), "+v"(sr), "+v"(xw), "+v"(xa), "+v"(og2), "+v"(xr0), "+v"(xr1));
    TS(7);
    asm volatile("s_waitcnt vmcnt(0)" : "+v"(A0), "+v"(A1)::"memory");
    poll2(rs, A0, A1, o0, o1);
    TS(8);
    f32x4 d = mfma(sa, A0, f32x4{0.f, 0.f, 0.f, 0.f});
    d = mfma(sb, A1, d);
    asm volatile("s_mov_b64 exec, %2\n\tds_write_b64 %0, %1\n\ts_mov_b64 exec, -1" ::"v"(sw), "v"(u32x2{pk(d[0], d[1]), pk(d[2], d[3])}), "s"(vmask) : "memory");
    A0 = lds_ld<u32x4>(sr);
    A1 = lds_ld<u32x4>(sr + 64);
    f32x4 val;
#pragma unroll
    for (int nt = 0; nt < 4; ++nt) {
      f32x4 e = mfma(A0, Bw[0][nt], f32x4{0.f, 0.f, 0.f, 0.f});
      e = mfma(A1, Bw[1][nt], e);
      val[nt] = e[0] + e[1];
    }
    asm volatile("s_mov_b64 exec, 0xffff\n\tds_write_b128 %0, %1\n\ts_mov_b64 exec, -1" ::"v"(xw), "v"(val) : "memory");
    wg_barrier();
    if (wave == 0) {
      f32x4 r[8];
      lds_ld8_b128<256>(r, xa, xa + 1024);
      xr0 += (r[0] + r[2]) + (r[4] + r[6]);
      xr1 += (r[1] + r[3]) + (r[5] + r[7]);
      if (lane < 16)
        __builtin_amdgcn_raw_buffer_store_b128(u32x4{nosent(pk(xr0[0], xr0[1])), nosent(pk(xr0[2], xr0[3])), nosent(pk(xr1[0], xr1[1])), nosent(pk(xr1[2], xr1[3]))},
                                               rsb, og2, 0, 16);
      TS(9);
    }
  } else if (role == 1) {
    u32x4 H[4];
    unsigned o[4];
#pragma unroll
    for (int i = 0; i < 4; ++i) o[i] = WS_YP + (4 * i + g4) * 2048 + wave * 256 + n16 * 16;
    flag_wait<1>(L0, F_ALL, 1u);
    flag_wait<1>(L0, F_P0 + (wave >> 1), 2u);
    issue4(rs, H, o);
    u32x4 Bw[2][2][2];
#pragma unroll
    for (int mt = 0; mt < 2; ++mt)
#pragma unroll
      for (int t = 0; t < 2; ++t)
#pragma unroll
        for (int nt = 0; nt < 2; ++nt) {
          const unsigned a = LW + mt * 4096 + (16 * trq + 8 * t + 2 * g4 + (trq & 1)) * 64 + nt * 32 + trp * 8;
          Bw[mt][t][nt] = trfrag(a, a ^ 64);
        }
    unsigned xw = L0 + L_X + wave * 512 + lane * 8, xa = L0 + L_P + lane * 8;
    {
      f32x4 c[2][2];
#pragma unroll
      for (int nt = 0; nt < 2; ++nt)
#pragma unroll
        for (int mt = 0; mt < 2; ++mt) {
          c[nt][mt] = mfma(ones, Bw[mt][0][nt], f32x4{0.f, 0.f, 0.f, 0.f});
          c[nt][mt] = mfma(ones, Bw[mt][1][nt], c[nt][mt]);
        }
      lds_st<f32x2>(L0 + L_P + wave * 1024 + lane * 8, lane < 32 ? f32x2{c[0][0][0], c[0][1][0]} : f32x2{c[1][0][0], c[1][1][0]});
    }
    unsigned hw = LW + lane * 8, hr = LW + ((n16 >> 2) & 1) * 128 + g4 * 16, stw = L0 + L_X + 4096 + wave * 16 + g4 * 8, str = L0 + L_X + 4096 + (n16 & 7) * 16 + (g4 & 1) * 8,
             aw = LW + 256 + (g4 & 1) * 64 + (g4 >> 1) * 32 + n16 * 2, ar = LW + 256 + (n16 & 1) * 64 + g4 * 16, og2 = WS_YP + (16 + tk) * 2048 + wave * 256 + n16 * 16;
    asm volatile("" : "+v"(hw), "+v"(hr), "+v"(stw), "+v"(str), "+v"(aw), "+v"(ar), "+v"(og2), "+v"(xw), "+v"(xa));
    unsigned da = LW + 8192 + (8 * g4 + trq) * 128 + (g4 & 1) * 32 + trp * 8;
    asm volatile("" : "+v"(da));
    TS(7);
    land4(H);
    u32x4 Bd[4];
    const bool early = flag_get<0>(L0, F_P0 + (wave >> 1)) >= 3u;
    if (early) {
#pragma unroll
      for (int j = 0; j < 4; ++j) Bd[j] = trfrag(da ^ (j * 32), (da ^ (j * 32)) + 512);
    }
    poll4(rs, H, o);
    TS(8);
    f32x4 h = mfma(sel, H[0], f32x4{0.f, 0.f, 0.f, 0.f}), h2 = mfma(sel, H[1], f32x4{0.f, 0.f, 0.f, 0.f});
    h = mfma(sel, H[2], h);
    h2 = mfma(sel, H[3], h2);
    h += h2;
    asm volatile("s_mov_b32 exec_hi, 0\n\tds_write_b64 %0, %1\n\ts_mov_b32 exec_hi, -1" ::"v"(hw), "v"(u32x2{pk(h[0], h[1]), pk(h[2], h[3])}) : "memory");
    const u32x4 A0 = lds_ld<u32x4>(hr), A1 = lds_ld<u32x4>(hr + 64);
    const float ss = row_sum16((h[0] + h[1]) + (h[2] + h[3])), gs = row_sum16((h[0] * h[0] + h[1] * h[1]) + (h[2] * h[2] + h[3] * h[3]));
    asm volatile("s_mov_b64 exec, 0x10001\n\tds_write_b64 %0, %1\n\ts_mov_b64 exec, -1" ::"v"(stw), "v"(f32x2{ss, gs}) : "memory");
    f32x4 e[2][2];
#pragma unroll
    for (int nt = 0; nt < 2; ++nt)
#pragma unroll
      for (int mt = 0; mt < 2; ++mt) {
        e[nt][mt] = mfma(A0, Bw[mt][0][nt], f32x4{0.f, 0.f, 0.f, 0.f});
        e[nt][mt] = mfma(A1, Bw[mt][1][nt], e[nt][mt]);
      }
    lds_st<f32x2>(xw, lane < 32 ? f32x2{e[0][0][0], e[0][1][0]} : f32x2{e[1][0][0], e[1][1][0]});
    wg_barrier();
    f32x2 st = lds_ld<f32x2>(str);
    f32x2 r[8];
    f32x2 cs[8];
    lds_ld16_b64(r, cs, xa);
    const f32x2 csum = ((cs[0] + cs[1]) + (cs[2] + cs[3])) + ((cs[4] + cs[5]) + (cs[6] + cs[7]));
    if (!early) {
#pragma unroll
      for (int j = 0; j < 4; ++j) Bd[j] = trfrag(da ^ (j * 32), (da ^ (j * 32)) + 512);
    }
    st[0] += dpp<XOR1>(st[0]); st[1] += dpp<XOR1>(st[1]);
    st[0] += dpp<XOR2>(st[0]); st[1] += dpp<XOR2>(st[1]);
    st[0] += dpp<XOR7>(st[0]); st[1] += dpp<XOR7>(st[1]);
    const f32x2 gu = ((r[0] + r[1]) + (r[2] + r[3])) + ((r[4] + r[5]) + (r[6] + r[7]));
    const float mu = st[0] * (1.f / 512.f);
    const float rr = __builtin_amdgcn_rsqf(fmaxf(st[1] * (1.f / 512.f) - mu * mu, 0.f) + 1e-5f);
    const float gg = rr * (gu[0] - mu * csum[0]), uu = rr * (gu[1] - mu * csum[1]);
    const float a = gg * uu * __builtin_amdgcn_rcpf(1.f + __builtin_amdgcn_exp2f(-1.4426950408889634f * gg));
    lds_st<__bf16>(aw, (__bf16)a);
    const u32x4 Aa = lds_ld<u32x4>(ar);
    f32x4 y[4];
#pragma unroll
    for (int j = 0; j < 4; ++j) y[j] = mfma(Aa, Bd[j], f32x4{0.f, 0.f, 0.f, 0.f});
    if (lane < 16)
      __builtin_amdgcn_raw_buffer_store_b128(
          u32x4{nosent(pk(y[0][0], y[1][0])), nosent(pk(y[2][0], y[3][0])), nosent(pk(y[0][1], y[1][1])), nosent(pk(y[2][1], y[3][1]))}, rsb, og2, 0, 16);
    TS(9);
  } else if (wave == 2) {
    const int ee = tk - 184;
    u32x4 Y0 = {SENT, SENT, SENT, SENT}, Y[4];
    unsigned o[4];
    const unsigned ob = WS_YP + (lane >> 2) * 2048 + ee * 64 + (lane & 3) * 16;
#pragma unroll
    for (int i = 0; i < 4; ++i) {
      Y[i] = (i < 3 || lane < 32) ? u32x4{SENT, SENT, SENT, SENT} : u32x4{0u, 0u, 0u, 0u};
      o[i] = ob + (i + 1) * 32768;
    }
    flag_wait<1>(L0, F_ALL, 1u);
#pragma nounroll
    for (int spin = 0; spin < SPIN_G; ++spin) {
      const u64 m = __ballot(pend4(Y0));
      if (m == 0) break;
      __builtin_amdgcn_s_sleep(4);
      poll_round1(rs, Y0, ob, m);
    }
    f32x4 d0 = mfma(sel, Y0, f32x4{0.f, 0.f, 0.f, 0.f});
    TS(7);
    poll4(rs, Y, o);
    TS(8);
    f32x4 d1 = mfma(sel, Y[1], f32x4{0.f, 0.f, 0.f, 0.f});
    d0 = mfma(sel, Y[0], d0);
    d1 = mfma(sel, Y[3], d1);
    d0 = mfma(sel, Y[2], d0);
    f32x4 d = d0 + d1;
#pragma unroll
    for (int r = 0; r < 4; ++r) { d[r] += dpp<ROR4>(d[r]); d[r] += dpp<ROR8>(d[r]); }
    const float lo = (n16 & 4) ? d[1] : d[0], hi = (n16 & 4) ? d[3] : d[2];
    const float res = (n16 & 8) ? hi : lo;
    const float nb = dpp<XOR1>(res);
    if (lane < 32 && (lane & 1) == 0)
      *(unsigned*)(out + g4 * 512 + 64 * (ee >> 2) + 16 * (n16 >> 2) + 4 * (ee & 3) + (n16 & 3)) = pk(res, nb);
    TS(9);
  }
  TSEND();
}
}

extern "C" void kernel_launch(void* const* d_in, const int* in_sizes, int n_in, void* d_out, int out_size, void* d_ws, size_t ws_size,
                              hipStream_t stream) {
  (void)in_sizes; (void)n_in; (void)out_size; (void)ws_size;
  (void)hipFuncSetAttribute((const void*)fk::fused_kernel, hipFuncAttributeMaxDynamicSharedMemorySize, fk::L_END);
  fk::fused_kernel<<<fk::NWG, fk::NTHR, fk::L_END, stream>>>(
      (unsigned char*)d_ws, (const unsigned char*)d_in[0], (const unsigned char*)d_in[3], (const unsigned char*)d_in[1],
      (const unsigned char*)d_in[2], (const unsigned char*)d_in[4], (const unsigned char*)d_in[6], (const unsigned char*)d_in[7],
      (const unsigned char*)d_in[8], (unsigned short*)d_out);
}
```

```cpp
#include <hip/hip_runtime.h>
#include <hip/hip_bf16.h>
#include <stdint.h>

namespace fk {
typedef unsigned u32x4 __attribute__((ext_vector_type(4)));
typedef unsigned u32x2 __attribute__((ext_vector_type(2)));
typedef float f32x4 __attribute__((ext_vector_type(4)));
typedef float f32x2 __attribute__((ext_vector_type(2)));
typedef short s16x4 __attribute__((ext_vector_type(4)));
typedef __bf16 bf16x8 __attribute__((ext_vector_type(8)));
typedef __bf16 bf16x2 __attribute__((ext_vector_type(2)));
typedef unsigned long long u64;
#define LAS __attribute__((address_space(3)))
#define GAS __attribute__((address_space(1)))
#define RLX_AGENT __ATOMIC_RELAXED, __HIP_MEMORY_SCOPE_AGENT

#define FK_TS 0

constexpr unsigned SENT = 0xFFFFFFFFu;
constexpr int NWG = 256, NTHR = 576;
constexpr int WS_EPOCH = 0;
constexpr int WS_QG = 4096;
constexpr int WS_ST = WS_QG + 65536;
constexpr int WS_OP = WS_ST + 8192;
constexpr int WS_HP = WS_OP + 262144;
constexpr int WS_ACT = WS_HP + 16384;
constexpr int WS_END = WS_ACT + 8192;
constexpr int WS_TS = 1 << 20;
constexpr int L_RB_O = 10240, L_RB_B = L_RB_O + 2048, L_RB_C = L_RB_B + 512, L_RB_CS = L_RB_C + 1024, L_RB_D = L_RB_CS + 512;
constexpr int L_WQ = 131072, L_Q = L_WQ + 16384, L_WM = L_Q + 1024, L_WL = L_WM + 128, L_F = L_WL + 128, L_W = L_F + 128, L_H = L_W + 512,
              L_FLAGS = L_H + 128, L_END = L_FLAGS + 64;
enum { F_Q = 0, F_STAT = 1, F_FAC = 2, F_VISS = 3, F_WTS = 4, F_H = 5, F_S1 = 6, F_S2 = 7, F_S3 = 8 };
constexpr int SPIN_G = 1 << 18;
constexpr int SPIN_L = 1 << 22;
constexpr float QSCALE = 0.088388347648318440f * 1.4426950408889634f;

__device__ __forceinline__ f32x4 mfma(u32x4 a, u32x4 b, f32x4 c) {
  return __builtin_amdgcn_mfma_f32_16x16x32_bf16(__builtin_bit_cast(bf16x8, a), __builtin_bit_cast(bf16x8, b), c, 0, 0, 0);
}
__device__ __forceinline__ unsigned pk(float lo, float hi) { bf16x2 v = {(__bf16)lo, (__bf16)hi}; return __builtin_bit_cast(unsigned, v); }
__device__ __forceinline__ unsigned nosent(unsigned x) { return x < 0xFFFFFFFEu ? x : 0xFFFFFFFEu; }
__device__ __forceinline__ float bflo(unsigned u) { return __builtin_bit_cast(float, u << 16); }
__device__ __forceinline__ float bfhi(unsigned u) { return __builtin_bit_cast(float, u & 0xffff0000u); }
__device__ __forceinline__ float x16_sum(float x) {
  auto s = __builtin_amdgcn_permlane16_swap(__float_as_uint(x), __float_as_uint(x), false, false);
  return __uint_as_float(s[0]) + __uint_as_float(s[1]);
}
__device__ __forceinline__ float x32_sum(float x) {
  auto s = __builtin_amdgcn_permlane32_swap(__float_as_uint(x), __float_as_uint(x), false, false);
  return __uint_as_float(s[0]) + __uint_as_float(s[1]);
}
__device__ __forceinline__ float x16_max(float x) {
  auto s = __builtin_amdgcn_permlane16_swap(__float_as_uint(x), __float_as_uint(x), false, false);
  return fmaxf(__uint_as_float(s[0]), __uint_as_float(s[1]));
}
__device__ __forceinline__ float x32_max(float x) {
  auto s = __builtin_amdgcn_permlane32_swap(__float_as_uint(x), __float_as_uint(x), false, false);
  return fmaxf(__uint_as_float(s[0]), __uint_as_float(s[1]));
}
__device__ __forceinline__ float rdl(float x, int l) { return __builtin_bit_cast(float, __builtin_amdgcn_readlane(__builtin_bit_cast(int, x), l)); }
template <int CTRL> __device__ __forceinline__ float dpp(float x) {
  return __builtin_bit_cast(float, __builtin_amdgcn_mov_dpp(__builtin_bit_cast(int, x), CTRL, 0xf, 0xf, true));
}
constexpr int XOR1 = 0xB1, XOR2 = 0x4E, XOR7 = 0x141;

template <int N> __device__ __forceinline__ void waitvm() { asm volatile("s_waitcnt vmcnt(%0)" ::"i"(N) : "memory"); }
__device__ __forceinline__ void dma_nt(const void* sbase, unsigned voff, unsigned ldst) {
  unsigned keep;
  asm volatile("s_mov_b32 %0, m0\n\ts_mov_b32 m0, %3\n\ts_nop 0\n\tglobal_load_lds_dwordx4 %1, %2 nt\n\ts_mov_b32 m0, %0"
               : "=&s"(keep) : "v"(voff), "s"(sbase), "s"(ldst) : "memory");
}
__device__ __forceinline__ void ldv_nt(u32x4& dst, const void* sbase, unsigned voff) {
  asm volatile("global_load_dwordx4 %0, %1, %2 nt" : "=v"(dst) : "v"(voff), "s"(sbase) : "memory");
}
__device__ __forceinline__ u32x2 trread(unsigned addr) {
  return __builtin_bit_cast(u32x2, __builtin_amdgcn_ds_read_tr16_b64_v4i16((LAS s16x4*)(uintptr_t)addr));
}
__device__ __forceinline__ u32x4 trfrag(unsigned a0, unsigned a1) { u32x2 lo = trread(a0), hi = trread(a1); return u32x4{lo[0], lo[1], hi[0], hi[1]}; }
template <typename T> __device__ __forceinline__ T lds_ld(unsigned addr) { return *(LAS T*)(uintptr_t)addr; }
template <typename T> __device__ __forceinline__ void lds_st(unsigned addr, T v) { *(LAS T*)(uintptr_t)addr = v; }
__device__ __forceinline__ void flag_set(unsigned base, int f, unsigned v, int lane) {
  asm volatile("" ::: "memory");
  if (lane == 0) *(volatile LAS unsigned*)(uintptr_t)(base + L_FLAGS + f * 4) = v;
  asm volatile("" ::: "memory");
}
__device__ __forceinline__ void flag_add(unsigned base, int f, int lane) {
  asm volatile("" ::: "memory");
  if (lane == 0) __hip_atomic_fetch_add((LAS unsigned*)(uintptr_t)(base + L_FLAGS + f * 4), 1u, __ATOMIC_RELAXED, __HIP_MEMORY_SCOPE_WORKGROUP);
  asm volatile("" ::: "memory");
}
template <int SLP> __device__ __forceinline__ void flag_wait(unsigned base, int f, unsigned v) {
  asm volatile("" ::: "memory");
  for (int i = 0; i < SPIN_L; ++i) {
    unsigned x = *(volatile LAS unsigned*)(uintptr_t)(base + L_FLAGS + f * 4);
    if ((unsigned)__builtin_amdgcn_readfirstlane(x) >= v) break;
    if (SLP) __builtin_amdgcn_s_sleep(SLP);
  }
  asm volatile("" ::: "memory");
}
__device__ __forceinline__ bool pend4(u32x4 v) {
  unsigned a = v[0] > v[1] ? v[0] : v[1], b = v[2] > v[3] ? v[2] : v[3];
  return (a > b ? a : b) == SENT;
}
__device__ __forceinline__ void poll_round1(u32x4 rs, u32x4& v0, unsigned o0, u64 m0) {
  u64 keep;
  asm volatile("s_mov_b64 %0, exec\n\t"
               "s_mov_b64 exec, %4\n\tbuffer_load_dwordx4 %1, %2, %3, 0 offen sc1\n\t"
               "s_mov_b64 exec, %0\n\ts_waitcnt vmcnt(0)"
               : "=&s"(keep), "+v"(v0) : "v"(o0), "s"(rs), "s"(m0) : "memory");
}
__device__ __forceinline__ void poll_round2(u32x4 rs, u32x4& v0, u32x4& v1, unsigned o0, unsigned o1, u64 m0, u64 m1) {
  u64 keep;
  asm volatile("s_mov_b64 %0, exec\n\t"
               "s_mov_b64 exec, %6\n\tbuffer_load_dwordx4 %1, %3, %5, 0 offen sc1\n\t"
               "s_mov_b64 exec, %7\n\tbuffer_load_dwordx4 %2, %4, %5, 0 offen sc1\n\t"
               "s_mov_b64 exec, %0\n\ts_waitcnt vmcnt(0)"
               : "=&s"(keep), "+v"(v0), "+v"(v1) : "v"(o0), "v"(o1), "s"(rs), "s"(m0), "s"(m1) : "memory");
}
__device__ __forceinline__ void poll_round4(u32x4 rs, u32x4 (&v)[4], const unsigned (&o)[4], const u64 (&m)[4]) {
  u64 keep;
  asm volatile("s_mov_b64 %0, exec\n\t"
               "s_mov_b64 exec, %10\n\tbuffer_load_dwordx4 %1, %5, %9, 0 offen sc1\n\t"
               "s_mov_b64 exec, %11\n\tbuffer_load_dwordx4 %2, %6, %9, 0 offen sc1\n\t"
               "s_mov_b64 exec, %12\n\tbuffer_load_dwordx4 %3, %7, %9, 0 offen sc1\n\t"
               "s_mov_b64 exec, %13\n\tbuffer_load_dwordx4 %4, %8, %9, 0 offen sc1\n\t"
               "s_mov_b64 exec, %0\n\ts_waitcnt vmcnt(0)"
               : "=&s"(keep), "+v"(v[0]), "+v"(v[1]), "+v"(v[2]), "+v"(v[3])
               : "v"(o[0]), "v"(o[1]), "v"(o[2]), "v"(o[3]), "s"(rs), "s"(m[0]), "s"(m[1]), "s"(m[2]), "s"(m[3]) : "memory");
}
__device__ __forceinline__ void poll_round7(u32x4 rs, u32x4 (&v)[7], unsigned o, const u64 (&m)[7]) {
  u64 keep;
  asm volatile("s_mov_b64 %0, exec\n\t"
               "s_mov_b64 exec, %10\n\tbuffer_load_dwordx4 %1, %8, %9, 0 offen sc1\n\t"
               "s_mov_b64 exec, %11\n\tbuffer_load_dwordx4 %2, %8, %9, 0 offen offset:128 sc1\n\t"
               "s_mov_b64 exec, %12\n\tbuffer_load_dwordx4 %3, %8, %9, 0 offen offset:256 sc1\n\t"
               "s_mov_b64 exec, %13\n\tbuffer_load_dwordx4 %4, %8, %9, 0 offen offset:384 sc1\n\t"
               "s_mov_b64 exec, %14\n\tbuffer_load_dwordx4 %5, %8, %9, 0 offen offset:512 sc1\n\t"
               "s_mov_b64 exec, %15\n\tbuffer_load_dwordx4 %6, %8, %9, 0 offen offset:640 sc1\n\t"
               "s_mov_b64 exec, %16\n\tbuffer_load_dwordx4 %7, %8, %9, 0 offen offset:768 sc1\n\t"
               "s_mov_b64 exec, %0\n\ts_waitcnt vmcnt(0)"
               : "=&s"(keep), "+v"(v[0]), "+v"(v[1]), "+v"(v[2]), "+v"(v[3]), "+v"(v[4]), "+v"(v[5]), "+v"(v[6])
               : "v"(o), "s"(rs), "s"(m[0]), "s"(m[1]), "s"(m[2]), "s"(m[3]), "s"(m[4]), "s"(m[5]), "s"(m[6]) : "memory");
}

#define TS(k) do {} while (0)

__global__ void __launch_bounds__(NTHR, 3)
fused_kernel(const unsigned char* __restrict__ x, const unsigned char* __restrict__ kc, const unsigned char* __restrict__ vc,
             const unsigned char* __restrict__ wq, const unsigned char* __restrict__ wo, const unsigned char* __restrict__ wg,
             const unsigned char* __restrict__ wu, const unsigned char* __restrict__ wd, unsigned* __restrict__ out, unsigned char* __restrict__ ws) {
  extern __shared__ __attribute__((aligned(16))) unsigned char smem[];
  const unsigned L0 = (unsigned)(uintptr_t)(LAS unsigned char*)smem;
  const int tid = threadIdx.x, lane = tid & 63, wave = __builtin_amdgcn_readfirstlane(tid >> 6);
  const int w = blockIdx.x, kvh = w & 7, idx = w >> 3, bat = idx >> 4, spl = idx & 15;
  const int n16 = lane & 15, g4 = lane >> 4;
  const int prod = (bat * 8 + kvh) * 16 + spl;
  const int role = idx < 16 ? 0 : idx < 23 ? 1 : idx < 27 ? 2 : 3;
  const int cb = idx, cc = kvh * 7 + (idx - 16), dd = kvh * 4 + (idx - 23);
  const __amdgpu_buffer_rsrc_t rsb = __builtin_amdgcn_make_buffer_rsrc(ws, 0, WS_END, 0x00020000);
  const u32x4 rs = {(unsigned)(uintptr_t)ws, (unsigned)((uintptr_t)ws >> 32) & 0xffffu, (unsigned)WS_END, 0x00020000u};
  TS(0);

  if (wave == 8) {
    __builtin_amdgcn_s_setprio(3);
    if (lane < 16) lds_st<unsigned>(L0 + L_FLAGS + lane * 4, 0u);
    {
      const unsigned char* base = wq + (kvh * 512 + idx * 16) * 2;
      const unsigned voff = (lane >> 1) * 8192 + (lane & 1) * 16;
#pragma unroll
      for (int i = 0; i < 16; ++i) dma_nt(base + (size_t)i * 32 * 8192, voff, L0 + L_WQ + i * 1024);
    }
    asm volatile("s_waitcnt lgkmcnt(0)\n\ts_barrier" ::: "memory");
    const u64 epoch = __hip_atomic_load((GAS u64*)(ws + WS_EPOCH), RLX_AGENT);
    u32x4 xa[16];
#pragma unroll
    for (int t = 0; t < 16; ++t) xa[t] = *(const u32x4*)(x + (n16 & 1) * 1024 + t * 64 + g4 * 16);
    {
      const u32x4 s4 = {SENT, SENT, SENT, SENT};
      __builtin_amdgcn_raw_buffer_store_b128(s4, rsb, WS_OP + prod * 1024 + lane * 16, 0, 16);
      int off = 0x7ffffff0;
      if (lane < 2) off = WS_ST + (kvh * 32 + idx) * 32 + lane * 16;
      else if (lane >= 8 && lane < 16) {
        if (role == 0) off = WS_HP + (kvh * 16 + cb) * 128 + (lane - 8) * 16;
        else if (role == 1) off = WS_ACT + cc * 128 + (lane - 8) * 16;
      }
      __builtin_amdgcn_raw_buffer_store_b128(s4, rsb, off, 0, 16);
    }
    if (n16 == 2) {
#pragma unroll
      for (int t = 0; t < 16; ++t) xa[t] = u32x4{0x3F803F80u, 0x3F803F80u, 0x3F803F80u, 0x3F803F80u};
    }
    f32x4 gram = {0.f, 0.f, 0.f, 0.f};
#pragma unroll
    for (int t = 0; t < 16; ++t) gram = mfma(xa[t], xa[t], gram);
    const float sx0 = rdl(gram[0], 2), sx1 = rdl(gram[1], 2);
    const float sq0 = rdl(gram[0], 0), sq1 = rdl(gram[1], 1);
    const float mu0 = sx0 * (1.f / 512.f), mu1 = sx1 * (1.f / 512.f);
    const float r0 = 1.f / sqrtf(fmaxf(sq0 * (1.f / 512.f) - mu0 * mu0, 0.f) + 1e-5f);
    const float r1 = 1.f / sqrtf(fmaxf(sq1 * (1.f / 512.f) - mu1 * mu1, 0.f) + 1e-5f);
    waitvm<0>();
    TS(1);
    f32x4 qa = {0.f, 0.f, 0.f, 0.f}, qb = {0.f, 0.f, 0.f, 0.f};
    {
      const unsigned a0 = L0 + L_WQ + (8 * g4 + ((lane >> 2) & 3)) * 32 + (lane & 3) * 8;
#pragma unroll
      for (int t = 0; t < 16; t += 2) {
        qa = mfma(xa[t], trfrag(a0 + t * 1024, a0 + t * 1024 + 128), qa);
        qb = mfma(xa[t + 1], trfrag(a0 + (t + 1) * 1024, a0 + (t + 1) * 1024 + 128), qb);
      }
    }
    const u64 tag = ((epoch + 1) & 0xFFFFFFFFFFFFull) << 16;
    {
      const float d0 = qa[0] + qb[0], d1 = qa[1] + qb[1], cs = qa[2] + qb[2];
      const float q0 = r0 * (d0 - mu0 * cs), q1 = r1 * (d1 - mu1 * cs);
      const unsigned p = pk(q0, q1);
      if (lane < 16) {
        GAS u64* g0 = (GAS u64*)(ws + WS_QG) + (kvh * 2 + 0) * 512 + idx * 16 + lane;
        __hip_atomic_store(g0, tag | (u64)(p & 0xffffu), RLX_AGENT);
        __hip_atomic_store(g0 + 512, tag | (u64)(p >> 16), RLX_AGENT);
      }
    }
    TS(2);
    {
      const unsigned thi = (unsigned)(tag >> 32), tlo = (unsigned)tag;
      u32x4 v[4];
      const unsigned o = WS_QG + (kvh * 2 + bat) * 4096 + lane * 16;
      for (int spin = 0; spin < SPIN_G; ++spin) {
        asm volatile("" ::: "memory");
#pragma unroll
        for (int i = 0; i < 4; ++i) v[i] = __builtin_amdgcn_raw_buffer_load_b128(rsb, o + i * 1024, 0, 16);
        bool ok = true;
#pragma unroll
        for (int i = 0; i < 4; ++i)
          ok = ok && v[i][1] == thi && v[i][3] == thi && (v[i][0] & 0xffff0000u) == tlo && (v[i][2] & 0xffff0000u) == tlo;
        if (__all(ok)) break;
      }
#pragma unroll
      for (int i = 0; i < 4; ++i) lds_st<unsigned>(L0 + L_Q + i * 256 + lane * 4, (v[i][0] & 0xffffu) | (v[i][2] << 16));
      flag_set(L0, F_Q, 1u, lane);
      TS(3);
      for (int spin = 0; spin < SPIN_G; ++spin) {
        asm volatile("" ::: "memory");
        u32x2 f[4];
#pragma unroll
        for (int i = 0; i < 4; ++i) {
          const int ww = i * 64 + lane;
          f[i] = __builtin_amdgcn_raw_buffer_load_b64(rsb, WS_QG + (ww >> 5) * 8192 + (ww & 31) * 128, 0, 16);
        }
        bool ok = true;
#pragma unroll
        for (int i = 0; i < 4; ++i) ok = ok && f[i][1] == thi && (f[i][0] & 0xffff0000u) == tlo;
        if (__all(ok)) break;
      }
      if (w == NWG - 1 && lane == 0) __hip_atomic_store((GAS u64*)(ws + WS_EPOCH), epoch + 1, RLX_AGENT);
    }
    TS(4);
    flag_wait<1>(L0, F_STAT, 8u);
    {
      const int l = lane & 31;
      const float m = lds_ld<float>(L0 + L_WM + l * 4), ls = lds_ld<float>(L0 + L_WL + l * 4);
      float M = m;
      M = fmaxf(M, __shfl_xor(M, 4)); M = fmaxf(M, __shfl_xor(M, 8)); M = fmaxf(M, __shfl_xor(M, 16));
      const float f = __builtin_amdgcn_exp2f(m - M);
      float L = f * ls;
      L += __shfl_xor(L, 4); L += __shfl_xor(L, 8); L += __shfl_xor(L, 16);
      if (lane < 32) lds_st<float>(L0 + L_F + l * 4, f);
      flag_set(L0, F_FAC, 1u, lane);
      if (lane < 4)
        __builtin_amdgcn_raw_buffer_store_b64(u32x2{nosent(__float_as_uint(M)), nosent(__float_as_uint(L))}, rsb, WS_ST + (kvh * 32 + idx) * 32 + lane * 8, 0, 16);
    }
    TS(5);
    if (role == 3) return;
    flag_wait<1>(L0, F_VISS, 8u);
    TS(6);
    if (role == 0) {
      const unsigned char* base = wo + (size_t)kvh * 512 * 1024 + cb * 64;
      const unsigned voff = (lane >> 2) * 1024 + (lane & 3) * 16;
#pragma unroll 4
      for (int j = 0; j < 32; ++j) dma_nt(base + (size_t)j * 16 * 1024, voff, L0 + (j >> 2) * 16384 + (j & 3) * 1024);
      u32x4 st = {SENT, SENT, SENT, SENT};
      const unsigned o = WS_ST + kvh * 1024 + lane * 16;
      for (int spin = 0; spin < SPIN_G; ++spin) {
        const u64 m = __ballot(pend4(st));
        if (m == 0) break;
        poll_round1(rs, st, o, m);
      }
      const float m0 = __uint_as_float(st[0]), l0 = __uint_as_float(st[1]), m1 = __uint_as_float(st[2]), l1 = __uint_as_float(st[3]);
      float M0 = m0, M1 = m1;
#pragma unroll
      for (int o2 = 2; o2 <= 16; o2 <<= 1) { M0 = fmaxf(M0, __shfl_xor(M0, o2)); M1 = fmaxf(M1, __shfl_xor(M1, o2)); }
      const float e0 = __builtin_amdgcn_exp2f(m0 - M0), e1 = __builtin_amdgcn_exp2f(m1 - M1);
      float S0 = e0 * l0, S1 = e1 * l1;
#pragma unroll
      for (int o2 = 2; o2 <= 16; o2 <<= 1) { S0 += __shfl_xor(S0, o2); S1 += __shfl_xor(S1, o2); }
      const int bb = lane >> 5, ss = (lane >> 1) & 15, h0 = (lane & 1) * 2;
      lds_st<float>(L0 + L_W + ((bb * 4 + h0) * 16 + ss) * 4, e0 / S0);
      lds_st<float>(L0 + L_W + ((bb * 4 + h0 + 1) * 16 + ss) * 4, e1 / S1);
      waitvm<0>();
      flag_set(L0, F_WTS, 1u, lane);
    } else if (role == 1) {
      const unsigned voff = (lane >> 2) * 3584 + (lane & 3) * 16;
      const unsigned char* bg = wg + cc * 64;
      const unsigned char* bu = wu + cc * 64;
#pragma unroll 4
      for (int j = 0; j < 32; ++j) {
        dma_nt(bg + (size_t)j * 16 * 3584, voff, L0 + (j >> 2) * 16384 + (j & 3) * 1024);
        dma_nt(bu + (size_t)j * 16 * 3584, voff, L0 + (j >> 2) * 16384 + 4096 + (j & 3) * 1024);
        if (j == 15) waitvm<16>();
      }
      waitvm<0>();
      flag_set(L0, F_WTS, 1u, lane);
    } else {
      const unsigned voff = (lane >> 1) * 1024 + (lane & 1) * 16;
      const unsigned char* base = wd + dd * 32;
      for (int v = 0; v < 8; ++v) {
#pragma unroll
        for (int i = 0; i < 7; ++i) dma_nt(base + (size_t)(v * 7 + i) * 32 * 1024, voff, L0 + v * 16384 + i * 1024);
        if (v == 3) waitvm<16>();
      }
      waitvm<0>();
      flag_set(L0, F_WTS, 1u, lane);
      u32x4 hv = lane < 32 ? u32x4{SENT, SENT, SENT, SENT} : u32x4{0u, 0u, 0u, 0u};
      const unsigned o = WS_HP + (((lane >> 2) & 7) * 16 + (dd >> 1)) * 128 + ((lane >> 1) & 1) * 64 + (dd & 1) * 32 + (lane & 1) * 16;
      for (int spin = 0; spin < SPIN_G; ++spin) {
        const u64 m = __ballot(pend4(hv));
        if (m == 0) break;
        poll_round1(rs, hv, o, m);
      }
      float hf[8];
#pragma unroll
      for (int j = 0; j < 4; ++j) { hf[2 * j] = bflo(hv[j]); hf[2 * j + 1] = bfhi(hv[j]); }
#pragma unroll
      for (int j = 0; j < 8; ++j) { hf[j] += __shfl_xor(hf[j], 4); hf[j] += __shfl_xor(hf[j], 8); hf[j] += __shfl_xor(hf[j], 16); }
      if (lane < 4) {
        lds_st<f32x4>(L0 + L_H + lane * 32, f32x4{hf[0], hf[1], hf[2], hf[3]});
        lds_st<f32x4>(L0 + L_H + lane * 32 + 16, f32x4{hf[4], hf[5], hf[6], hf[7]});
      }
      flag_set(L0, F_H, 1u, lane);
    }
    TS(7);
    return;
  }

  const unsigned LW = L0 + wave * 16384;
  const size_t chunk = ((size_t)(bat * 8 + kvh) * 8192 + spl * 512 + wave * 64) * 256;
  const unsigned char* kb = kc + chunk;
  const unsigned char* vb = vc + chunk;
  {
    unsigned kvo[4];
#pragma unroll
    for (int a = 0; a < 4; ++a) kvo[a] = g4 * 256 + ((n16 ^ (4 * g4 + a)) * 16);
#pragma unroll
    for (int i = 0; i < 4; ++i) dma_nt(kb + i * 1024, kvo[i & 3], LW + i * 1024);
    asm volatile("s_barrier" ::: "memory");
#pragma unroll
    for (int i = 4; i < 16; ++i) dma_nt(kb + i * 1024, kvo[i & 3], LW + i * 1024);
  }
  u32x4 V[16];
  const unsigned vo = lane * 16;
#pragma unroll
  for (int i = 0; i < 8; ++i) ldv_nt(V[i], vb + i * 1024, vo);
  TS(1);
  flag_wait<1>(L0, F_Q, 1u);
  waitvm<8>();
  TS(2);
  unsigned P[2][16];
  f32x4 acc[2] = {{0.f, 0.f, 0.f, 0.f}, {0.f, 0.f, 0.f, 0.f}};
  {
    u32x4 qf[2][4];
#pragma unroll
    for (int u = 0; u < 2; ++u)
#pragma unroll
      for (int t = 0; t < 4; ++t) qf[u][t] = lds_ld<u32x4>(L0 + L_Q + (2 * u + (n16 >> 3)) * 256 + t * 64 + g4 * 16);
    f32x4 S[2][4];
    const unsigned krow = LW + (4 * (n16 & 3) + (n16 >> 2)) * 256;
#pragma unroll
    for (int T = 0; T < 4; ++T) {
      S[0][T] = f32x4{0.f, 0.f, 0.f, 0.f};
      S[1][T] = f32x4{0.f, 0.f, 0.f, 0.f};
#pragma unroll
      for (int t = 0; t < 4; ++t) {
        const u32x4 kf = lds_ld<u32x4>(krow + T * 4096 + (((4 * t + g4) ^ n16) * 16));
        S[0][T] = mfma(kf, qf[0][t], S[0][T]);
        S[1][T] = mfma(kf, qf[1][t], S[1][T]);
      }
    }
#pragma unroll
    for (int u = 0; u < 2; ++u) {
      float m = S[u][0][0];
#pragma unroll
      for (int T = 0; T < 4; ++T)
#pragma unroll
        for (int r = 0; r < 4; ++r) m = fmaxf(m, S[u][T][r]);
      m = x32_max(x16_max(m));
      const float ms = m * QSCALE;
      float l = 0.f;
#pragma unroll
      for (int T = 0; T < 4; ++T)
#pragma unroll
        for (int r = 0; r < 4; ++r) {
          const float p = __builtin_amdgcn_exp2f(fmaf(S[u][T][r], QSCALE, -ms));
          const unsigned pp = pk(p, p);
          P[u][T * 4 + r] = pp;
          l += bfhi(pp);
        }
      l = x32_sum(x16_sum(l));
      if ((lane & 55) == 0) {
        lds_st<float>(L0 + L_WM + (wave * 4 + 2 * u + (n16 >> 3)) * 4, ms);
        lds_st<float>(L0 + L_WL + (wave * 4 + 2 * u + (n16 >> 3)) * 4, l);
      }
    }
    flag_add(L0, F_STAT, lane);
  }
  TS(3);
  u32x4 msk;
  {
    const unsigned hm = (lane & 1) ? 0xffff0000u : 0x0000ffffu;
    const int dsel = (lane & 7) >> 1;
    msk = u32x4{dsel == 0 ? hm : 0u, dsel == 1 ? hm : 0u, dsel == 2 ? hm : 0u, dsel == 3 ? hm : 0u};
  }
#define PV(i)                                                        \
  do {                                                               \
    const u32x4 b0 = msk & P[0][i], b1 = msk & P[1][i];              \
    acc[0] = mfma(V[i], b0, acc[0]);                                 \
    acc[1] = mfma(V[i], b1, acc[1]);                                 \
  } while (0)
#pragma unroll
  for (int j = 0; j < 8; ++j) {
    ldv_nt(V[8 + j], vb + (8 + j) * 1024, vo);
    asm volatile("s_waitcnt vmcnt(8)" : "+v"(V[j])::"memory");
    PV(j);
  }
  flag_add(L0, F_VISS, lane);
  TS(4);
#define PVW(j, N)                                                    \
  do {                                                               \
    asm volatile("s_waitcnt vmcnt(" #N ")" : "+v"(V[j])::"memory");  \
    PV(j);                                                           \
  } while (0)
  PVW(8, 7); PVW(9, 6); PVW(10, 5); PVW(11, 4); PVW(12, 3); PVW(13, 2); PVW(14, 1);
  flag_wait<0>(L0, F_FAC, 1u);
  const float f0 = lds_ld<float>(L0 + L_F + (wave * 4 + (n16 >> 3)) * 4), f1 = lds_ld<float>(L0 + L_F + (wave * 4 + 2 + (n16 >> 3)) * 4);
  PVW(15, 0);
  TS(5);
  lds_st<f32x4>(LW + L_RB_O + lane * 16, acc[0] * f0);
  lds_st<f32x4>(LW + L_RB_O + 1024 + lane * 16, acc[1] * f1);
  flag_add(L0, F_S1, lane);
  if (wave < 2) {
    flag_wait<0>(L0, F_S1, 8u);
    f32x4 s = lds_ld<f32x4>(L0 + L_RB_O + wave * 1024 + lane * 16);
#pragma unroll
    for (int v = 1; v < 8; ++v) s += lds_ld<f32x4>(L0 + v * 16384 + L_RB_O + wave * 1024 + lane * 16);
    __builtin_amdgcn_raw_buffer_store_b64(u32x2{nosent(pk(s[0], s[1])), nosent(pk(s[2], s[3]))}, rsb,
                                          WS_OP + prod * 1024 + (2 * wave + (n16 >> 3)) * 256 + g4 * 64 + (lane & 7) * 8, 0, 16);
  }
  TS(6);
  if (role == 3) return;

  const unsigned trq = (lane >> 2) & 3, trp = lane & 3;
  if (role == 0) {
    const int hd = wave >> 1, half = wave & 1;
    u32x4 A[4];
    unsigned o[4];
#pragma unroll
    for (int i = 0; i < 4; ++i) {
      A[i] = u32x4{SENT, SENT, SENT, SENT};
      o[i] = WS_OP + (((i >> 1) * 8 + kvh) * 16 + n16) * 1024 + hd * 256 + (2 * half + (i & 1)) * 64 + g4 * 16;
    }
    float xres = 0.f;
    if (wave == 0 && kvh == 0) xres = bflo(*(const unsigned short*)(x + ((g4 >> 1) * 512 + cb * 32 + (g4 & 1) * 16 + n16) * 2));
    flag_wait<0>(L0, F_WTS, 1u);
    u32x4 Bw[2][2];
#pragma unroll
    for (int t = 0; t < 2; ++t)
#pragma unroll
      for (int nt = 0; nt < 2; ++nt) {
        const unsigned a = LW + (32 * t + 8 * trq + 2 * g4) * 64 + nt * 32 + trp * 8;
        Bw[t][nt] = trfrag(a, a + 64);
      }
    const f32x4 w0 = lds_ld<f32x4>(L0 + L_W + ((0 * 4 + hd) * 16 + 4 * g4) * 4), w1 = lds_ld<f32x4>(L0 + L_W + ((1 * 4 + hd) * 16 + 4 * g4) * 4);
    TS(7);
    for (int spin = 0; spin < SPIN_G; ++spin) {
      u64 m[4];
#pragma unroll
      for (int i = 0; i < 4; ++i) m[i] = __ballot(pend4(A[i]));
      if ((m[0] | m[1] | m[2] | m[3]) == 0) break;
      poll_round4(rs, A, o, m);
    }
    TS(8);
    f32x4 val;
#pragma unroll
    for (int bb = 0; bb < 2; ++bb)
#pragma unroll
      for (int nt = 0; nt < 2; ++nt) {
        f32x4 d = {0.f, 0.f, 0.f, 0.f};
        d = mfma(A[bb * 2 + 0], Bw[0][nt], d);
        d = mfma(A[bb * 2 + 1], Bw[1][nt], d);
        const f32x4 ww = bb ? w1 : w0;
        val[bb * 2 + nt] = x32_sum(x16_sum(d[0] * ww[0] + d[1] * ww[1] + d[2] * ww[2] + d[3] * ww[3]));
      }
    if (lane < 16) lds_st<f32x4>(LW + L_RB_B + lane * 16, val);
    flag_add(L0, F_S2, lane);
    if (wave == 0) {
      flag_wait<0>(L0, F_S2, 8u);
      float s = xres;
#pragma unroll
      for (int v = 0; v < 8; ++v) s += lds_ld<float>(L0 + v * 16384 + L_RB_B + n16 * 16 + g4 * 4);
      const float nb = dpp<XOR1>(s);
      if ((lane & 1) == 0)
        __builtin_amdgcn_raw_buffer_store_b32(nosent(pk(s, nb)), rsb, WS_HP + (kvh * 16 + cb) * 128 + (g4 >> 1) * 64 + ((g4 & 1) * 16 + n16) * 2, 0, 16);
      TS(9);
    }
  } else if (role == 1) {
    u32x4 A0 = {SENT, SENT, SENT, SENT}, A1 = {SENT, SENT, SENT, SENT};
    const unsigned o0 = WS_HP + ((n16 & 7) * 16 + 2 * wave) * 128 + (n16 >> 3) * 64 + g4 * 16, o1 = o0 + 128;
    const u32x4 ones = {0x3F803F80u, 0x3F803F80u, 0x3F803F80u, 0x3F803F80u};
    flag_wait<0>(L0, F_WTS, 1u);
    u32x4 Bw[2][2][2];
#pragma unroll
    for (int mt = 0; mt < 2; ++mt)
#pragma unroll
      for (int t = 0; t < 2; ++t)
#pragma unroll
        for (int nt = 0; nt < 2; ++nt) {
          const unsigned a = LW + mt * 4096 + (32 * t + 8 * g4 + trq) * 64 + nt * 32 + trp * 8;
          Bw[mt][t][nt] = trfrag(a, a + 256);
        }
    {
      f32x4 cs;
#pragma unroll
      for (int nt = 0; nt < 2; ++nt)
#pragma unroll
        for (int mt = 0; mt < 2; ++mt) {
          f32x4 d = {0.f, 0.f, 0.f, 0.f};
          d = mfma(ones, Bw[mt][0][nt], d);
          d = mfma(ones, Bw[mt][1][nt], d);
          cs[nt * 2 + mt] = d[0];
        }
      if (lane < 16) lds_st<f32x4>(LW + L_RB_CS + lane * 16, cs);
      flag_add(L0, F_S3, lane);
    }
    f32x2 csum = {0.f, 0.f};
    if (wave == 0) {
      flag_wait<0>(L0, F_S3, 8u);
#pragma unroll
      for (int v = 0; v < 8; ++v) csum += lds_ld<f32x2>(L0 + v * 16384 + L_RB_CS + n16 * 16 + (g4 & 1) * 8);
    }
    TS(7);
    for (int spin = 0; spin < SPIN_G; ++spin) {
      const u64 m0 = __ballot(pend4(A0)), m1 = __ballot(pend4(A1));
      if ((m0 | m1) == 0) break;
      poll_round2(rs, A0, A1, o0, o1, m0, m1);
    }
    TS(8);
    f32x4 gr = {0.f, 0.f, 0.f, 0.f}, s1 = {0.f, 0.f, 0.f, 0.f};
    gr = mfma(A0, A0, gr); s1 = mfma(A0, ones, s1);
    gr = mfma(A1, A1, gr); s1 = mfma(A1, ones, s1);
    f32x4 val;
#pragma unroll
    for (int nt = 0; nt < 2; ++nt)
#pragma unroll
      for (int mt = 0; mt < 2; ++mt) {
        f32x4 d = {0.f, 0.f, 0.f, 0.f};
        d = mfma(A0, Bw[mt][0][nt], d);
        d = mfma(A1, Bw[mt][1][nt], d);
        val[nt * 2 + mt] = x16_sum(d[0] + d[1] + d[2] + d[3]);
      }
    float gs = ((n16 >> 3) == (g4 >> 1)) ? (gr[0] + gr[1] + gr[2] + gr[3]) : 0.f;
    gs += dpp<XOR1>(gs); gs += dpp<XOR2>(gs); gs += dpp<XOR7>(gs);
    gs = x16_sum(gs);
    const float ss = x16_sum(s1[0] + s1[1] + s1[2] + s1[3]);
    if ((g4 & 1) == 0) {
      lds_st<f32x4>(LW + L_RB_C + ((g4 >> 1) * 16 + n16) * 16, val);
      if (n16 == (g4 >> 1) * 8) lds_st<f32x2>(LW + L_RB_C + 512 + (g4 >> 1) * 8, f32x2{ss, gs});
    }
    flag_add(L0, F_S2, lane);
    if (wave == 0) {
      flag_wait<0>(L0, F_S2, 8u);
      f32x2 gu = {0.f, 0.f}, st = {0.f, 0.f};
#pragma unroll
      for (int v = 0; v < 8; ++v) {
        gu += lds_ld<f32x2>(L0 + v * 16384 + L_RB_C + ((g4 >> 1) * 16 + n16) * 16 + (g4 & 1) * 8);
        st += lds_ld<f32x2>(L0 + v * 16384 + L_RB_C + 512 + (g4 >> 1) * 8);
      }
      const float mu = st[0] * (1.f / 512.f);
      const float r = 1.f / sqrtf(fmaxf(st[1] * (1.f / 512.f) - mu * mu, 0.f) + 1e-5f);
      const float gg = r * (gu[0] - mu * csum[0]), uu = r * (gu[1] - mu * csum[1]);
      const float a = gg / (1.f + __expf(-gg)) * uu;
      const float nb = dpp<XOR1>(a);
      if ((lane & 1) == 0)
        __builtin_amdgcn_raw_buffer_store_b32(nosent(pk(a, nb)), rsb, WS_ACT + cc * 128 + (g4 >> 1) * 64 + ((g4 & 1) * 16 + n16) * 2, 0, 16);
      TS(9);
    }
  } else {
    u32x4 A[7];
#pragma unroll
    for (int i = 0; i < 7; ++i) A[i] = n16 < 2 ? u32x4{SENT, SENT, SENT, SENT} : u32x4{0u, 0u, 0u, 0u};
    const unsigned o = WS_ACT + wave * 7 * 128 + (n16 & 1) * 64 + g4 * 16;
    flag_wait<0>(L0, F_WTS, 1u);
    u32x4 Bw[7];
#pragma unroll
    for (int i = 0; i < 7; ++i) {
      const unsigned a = LW + (32 * i + 8 * g4 + trq) * 32 + trp * 8;
      Bw[i] = trfrag(a, a + 128);
    }
    TS(7);
    for (int spin = 0; spin < SPIN_G; ++spin) {
      u64 m[7], any = 0;
#pragma unroll
      for (int i = 0; i < 7; ++i) { m[i] = __ballot(pend4(A[i])); any |= m[i]; }
      if (any == 0) break;
      poll_round7(rs, A, o, m);
    }
    TS(8);
    f32x4 d0 = {0.f, 0.f, 0.f, 0.f}, d1 = {0.f, 0.f, 0.f, 0.f};
#pragma unroll
    for (int i = 0; i < 7; ++i) { if (i & 1) d1 = mfma(A[i], Bw[i], d1); else d0 = mfma(A[i], Bw[i], d0); }
    if (lane < 16) lds_st<f32x2>(LW + L_RB_D + lane * 8, f32x2{d0[0] + d1[0], d0[1] + d1[1]});
    flag_add(L0, F_S2, lane);
    if (wave == 0) {
      flag_wait<0>(L0, F_H, 1u);
      flag_wait<0>(L0, F_S2, 8u);
      float s = lds_ld<float>(L0 + L_H + ((g4 & 1) * 16 + n16) * 4);
#pragma unroll
      for (int v = 0; v < 8; ++v) s += lds_ld<float>(L0 + v * 16384 + L_RB_D + n16 * 8 + (g4 & 1) * 4);
      const float nb = dpp<XOR1>(s);
      if ((lane & 1) == 0 && lane < 32) out[((g4 & 1) * 512 + dd * 16 + n16) >> 1] = pk(s, nb);
      TS(9);
    }
  }
}
}

extern "C" void kernel_launch(void* const* d_in, const int* in_sizes, int n_in, void* d_out, int out_size, void* d_ws, size_t ws_size,
                              hipStream_t stream) {
  (void)in_sizes; (void)n_in; (void)out_size; (void)ws_size;
  (void)hipFuncSetAttribute((const void*)fk::fused_kernel, hipFuncAttributeMaxDynamicSharedMemorySize, fk::L_END);
  fk::fused_kernel<<<fk::NWG, fk::NTHR, fk::L_END, stream>>>(
      (const unsigned char*)d_in[0], (const unsigned char*)d_in[1], (const unsigned char*)d_in[2], (const unsigned char*)d_in[3],
      (const unsigned char*)d_in[4], (const unsigned char*)d_in[6], (const unsigned char*)d_in[7], (const unsigned char*)d_in[8],
      (unsigned*)d_out, (unsigned char*)d_ws);
}
```

```cpp
#include <hip/hip_runtime.h>
#include <hip/hip_bf16.h>
#include <stdint.h>

namespace fk {
typedef unsigned u32x4 __attribute__((ext_vector_type(4)));
typedef unsigned u32x2 __attribute__((ext_vector_type(2)));
typedef float f32x4 __attribute__((ext_vector_type(4)));
typedef float f32x2 __attribute__((ext_vector_type(2)));
typedef short s16x4 __attribute__((ext_vector_type(4)));
typedef __bf16 bf16x8 __attribute__((ext_vector_type(8)));
typedef __bf16 bf16x2 __attribute__((ext_vector_type(2)));
typedef unsigned long long u64;
#define LAS __attribute__((address_space(3)))
#define GAS __attribute__((address_space(1)))
#define RLX_AGENT __ATOMIC_RELAXED, __HIP_MEMORY_SCOPE_AGENT

#define FK_TS 0
#define KWIN 8
#define VN1 12

#define KW_B 6
#define KW_N 6
#define KISS_AT 31
#define DMA_B 8u
#define DMA_C 8u
#define ROT 1
#define EVENC 0
#define ST_AT 4u
#define PFL 4
#define PF_AT 0u
#define RB 3
#define RC 3
#define RE 3
#define PIPE 0
#define HC 1
#define EDLY 0
#define KTAB 0
#define KSLOW 1
#define HB 0
#define HELPB 1
#define DLB 0
#define DLC 0
#define DLH 0
#define RKA 2
#define RKB 0
#define RKC 0
#define RKD 0
#define RVA 1
#define RVB 0
#define RVC 0
#define RVD 0
#define ILV 2
#define CPF 1
#define PGAP 6
#define QDELAY 40
constexpr unsigned SENT = 0xFFFFFFFFu;
constexpr int NWG = 256, NWAVES = 13, NTHR = NWAVES * 64;
constexpr int WS_EPOCH = 0;
constexpr int WS_FL = 4096;
constexpr int WS_QP = 8192;
constexpr int WS_ST = WS_QP + 131072;
constexpr int WS_OP = WS_ST + 8192;
constexpr int WS_YP = WS_OP + 262144;
constexpr int WS_PR = WS_YP + 120 * 2048;
constexpr int WS_END = WS_PR + 8192;
constexpr int WS_TS = 1 << 20;
constexpr int L_RB_O = 12288;
constexpr int L_WQ = 131072, L_P = L_WQ  , L_X = L_WQ + 8192  , L_Q = L_WQ + 16384, L_WM = L_Q + 1024, L_WL = L_WM + 64, L_F = L_WL + 64,
              L_W = L_F + 64, L_ACT = L_W + 512, L_FLAGS = L_ACT + 128, L_END = L_FLAGS + 64;
enum { F_Q = 0, F_STAT = 1, F_FAC = 2, F_VISS = 3, F_WQ = 4, F_KISS = 5, F_S1 = 6, F_S2 = 7, F_S3 = 8, F_ALL = 9, F_W = 10, F_TAG = 11, F_P0 = 12   };
constexpr int SPIN_G = 1 << 18;
constexpr int SPIN_L = 1 << 22;
constexpr float QSCALE = 0.088388347648318440f * 1.4426950408889634f;
constexpr unsigned ONE2 = 0x3F803F80u;

__device__ __forceinline__ f32x4 mfma(u32x4 a, u32x4 b, f32x4 c) {
  return __builtin_amdgcn_mfma_f32_16x16x32_bf16(__builtin_bit_cast(bf16x8, a), __builtin_bit_cast(bf16x8, b), c, 0, 0, 0);
}
__device__ __forceinline__ unsigned pk(float lo, float hi) { bf16x2 v = {(__bf16)lo, (__bf16)hi}; return __builtin_bit_cast(unsigned, v); }
__device__ __forceinline__ unsigned nosent(unsigned x) { return x < 0xFFFFFFFEu ? x : 0xFFFFFFFEu; }
__device__ __forceinline__ float bflo(unsigned u) { return __builtin_bit_cast(float, u << 16); }
__device__ __forceinline__ float bfhi(unsigned u) { return __builtin_bit_cast(float, u & 0xffff0000u); }
__device__ __forceinline__ float x16_sum(float x) {
  auto s = __builtin_amdgcn_permlane16_swap(__float_as_uint(x), __float_as_uint(x), false, false);
  return __uint_as_float(s[0]) + __uint_as_float(s[1]);
}
__device__ __forceinline__ float x32_sum(float x) {
  auto s = __builtin_amdgcn_permlane32_swap(__float_as_uint(x), __float_as_uint(x), false, false);
  return __uint_as_float(s[0]) + __uint_as_float(s[1]);
}
__device__ __forceinline__ float x16_max(float x) {
  auto s = __builtin_amdgcn_permlane16_swap(__float_as_uint(x), __float_as_uint(x), false, false);
  return fmaxf(__uint_as_float(s[0]), __uint_as_float(s[1]));
}
__device__ __forceinline__ float x32_max(float x) {
  auto s = __builtin_amdgcn_permlane32_swap(__float_as_uint(x), __float_as_uint(x), false, false);
  return fmaxf(__uint_as_float(s[0]), __uint_as_float(s[1]));
}
template <int CTRL> __device__ __forceinline__ float dpp(float x) {
  return __builtin_bit_cast(float, __builtin_amdgcn_mov_dpp(__builtin_bit_cast(int, x), CTRL, 0xf, 0xf, true));
}
constexpr int XOR1 = 0xB1, XOR2 = 0x4E, XOR7 = 0x141, ROR4 = 0x124, ROR8 = 0x128;
__device__ __forceinline__ float row_sum16(float x) { x += dpp<XOR1>(x); x += dpp<XOR2>(x); x += dpp<XOR7>(x); x += dpp<ROR8>(x); return x; }
__device__ __forceinline__ float wave_sum(float x) { return x32_sum(x16_sum(row_sum16(x))); }

template <int N> __device__ __forceinline__ void waitvm() { asm volatile("s_waitcnt vmcnt(%0)" ::"i"(N) : "memory"); }
__device__ __forceinline__ void dma_nt(const void* sbase, unsigned voff, unsigned ldst) {
  unsigned keep;
  asm volatile("s_mov_b32 %0, m0\n\ts_mov_b32 m0, %3\n\ts_nop 0\n\tglobal_load_lds_dwordx4 %1, %2 nt\n\ts_mov_b32 m0, %0"
               : "=&s"(keep) : "v"(voff), "s"(sbase), "s"(ldst) : "memory");
}
__device__ __forceinline__ void touch(const void* sbase, unsigned voff, unsigned ldst) {
  unsigned keep;
  asm volatile("s_mov_b32 %0, m0\n\ts_mov_b32 m0, %3\n\ts_nop 0\n\tglobal_load_lds_dword %1, %2\n\ts_mov_b32 m0, %0"
               : "=&s"(keep) : "v"(voff), "s"(sbase), "s"(ldst) : "memory");
}
__device__ __forceinline__ void ldv_nt(u32x4& dst, const void* sbase, unsigned voff) {
  asm volatile("global_load_dwordx4 %0, %1, %2 nt" : "=v"(dst) : "v"(voff), "s"(sbase) : "memory");
}
__device__ __forceinline__ void ldv(u32x4& dst, const void* sbase, unsigned voff) {
  asm volatile("global_load_dwordx4 %0, %1, %2" : "=v"(dst) : "v"(voff), "s"(sbase) : "memory");
}
__device__ __forceinline__ u32x2 trread(unsigned addr) {
  return __builtin_bit_cast(u32x2, __builtin_amdgcn_ds_read_tr16_b64_v4i16((LAS s16x4*)(uintptr_t)addr));
}
__device__ __forceinline__ u32x4 trfrag(unsigned a0, unsigned a1) { u32x2 lo = trread(a0), hi = trread(a1); return u32x4{lo[0], lo[1], hi[0], hi[1]}; }
template <typename T> __device__ __forceinline__ T lds_ld(unsigned addr) { return *(LAS T*)(uintptr_t)addr; }
template <typename T> __device__ __forceinline__ void lds_st(unsigned addr, T v) { *(LAS T*)(uintptr_t)addr = v; }
__device__ __forceinline__ void flag_set(unsigned base, int f, unsigned v, int) {
  asm volatile("s_mov_b64 exec, 1\n\tds_write_b32 %0, %1\n\ts_mov_b64 exec, -1" ::"v"(base + L_FLAGS + f * 4), "v"(v) : "memory");
}
__device__ __forceinline__ void flag_add(unsigned base, int f, int) {
  asm volatile("s_mov_b64 exec, 1\n\tds_add_u32 %0, %1\n\ts_mov_b64 exec, -1" ::"v"(base + L_FLAGS + f * 4), "v"(1u) : "memory");
}
__device__ __forceinline__ void wg_barrier() { asm volatile("s_waitcnt lgkmcnt(0)\n\ts_barrier" ::: "memory"); }
template <int ST> __device__ __forceinline__ void lds_ld8_b64(f32x2 (&r)[8], unsigned a) {
  asm volatile("ds_read_b64 %0, %8\n\tds_read_b64 %1, %8 offset:%9\n\tds_read_b64 %2, %8 offset:%10\n\tds_read_b64 %3, %8 offset:%11\n\t"
               "ds_read_b64 %4, %8 offset:%12\n\tds_read_b64 %5, %8 offset:%13\n\tds_read_b64 %6, %8 offset:%14\n\tds_read_b64 %7, %8 offset:%15\n\t"
               "s_waitcnt lgkmcnt(0)"
               : "=&v"(r[0]), "=&v"(r[1]), "=&v"(r[2]), "=&v"(r[3]), "=&v"(r[4]), "=&v"(r[5]), "=&v"(r[6]), "=&v"(r[7])
               : "v"(a), "i"(ST), "i"(2 * ST), "i"(3 * ST), "i"(4 * ST), "i"(5 * ST), "i"(6 * ST), "i"(7 * ST) : "memory");
}
__device__ __forceinline__ void lds_ld16_b64(f32x2 (&r)[8], f32x2 (&c)[8], unsigned a) {
  asm volatile("ds_read_b64 %0, %16 offset:8192\n\tds_read_b64 %1, %16 offset:8704\n\tds_read_b64 %2, %16 offset:9216\n\tds_read_b64 %3, %16 offset:9728\n\t"
               "ds_read_b64 %4, %16 offset:10240\n\tds_read_b64 %5, %16 offset:10752\n\tds_read_b64 %6, %16 offset:11264\n\tds_read_b64 %7, %16 offset:11776\n\t"
               "ds_read_b64 %8, %16\n\tds_read_b64 %9, %16 offset:1024\n\tds_read_b64 %10, %16 offset:2048\n\tds_read_b64 %11, %16 offset:3072\n\t"
               "ds_read_b64 %12, %16 offset:4096\n\tds_read_b64 %13, %16 offset:5120\n\tds_read_b64 %14, %16 offset:6144\n\tds_read_b64 %15, %16 offset:7168\n\t"
               "s_waitcnt lgkmcnt(0)"
               : "=&v"(r[0]), "=&v"(r[1]), "=&v"(r[2]), "=&v"(r[3]), "=&v"(r[4]), "=&v"(r[5]), "=&v"(r[6]), "=&v"(r[7]),
                 "=&v"(c[0]), "=&v"(c[1]), "=&v"(c[2]), "=&v"(c[3]), "=&v"(c[4]), "=&v"(c[5]), "=&v"(c[6]), "=&v"(c[7])
               : "v"(a) : "memory");
}
template <int ST> __device__ __forceinline__ void lds_ld8_b128(f32x4 (&r)[8], unsigned a, unsigned b) {
  asm volatile("ds_read_b128 %0, %8\n\tds_read_b128 %1, %8 offset:%10\n\tds_read_b128 %2, %8 offset:%11\n\tds_read_b128 %3, %8 offset:%12\n\t"
               "ds_read_b128 %4, %9\n\tds_read_b128 %5, %9 offset:%10\n\tds_read_b128 %6, %9 offset:%11\n\tds_read_b128 %7, %9 offset:%12\n\t"
               "s_waitcnt lgkmcnt(0)"
               : "=&v"(r[0]), "=&v"(r[1]), "=&v"(r[2]), "=&v"(r[3]), "=&v"(r[4]), "=&v"(r[5]), "=&v"(r[6]), "=&v"(r[7])
               : "v"(a), "v"(b), "i"(ST), "i"(2 * ST), "i"(3 * ST) : "memory");
}
template <int SLP> __device__ __forceinline__ void flag_wait(unsigned base, int f, unsigned v) {
  asm volatile("" ::: "memory");
#pragma nounroll
  for (int i = 0; i < SPIN_L; ++i) {
    unsigned x = *(volatile LAS unsigned*)(uintptr_t)(base + L_FLAGS + f * 4);
    if ((unsigned)__builtin_amdgcn_readfirstlane(x) >= v) break;
    if (SLP) __builtin_amdgcn_s_sleep(SLP);
  }
  asm volatile("" ::: "memory");
}
template <int SLP> __device__ __forceinline__ unsigned flag_get(unsigned base, int f) {
  asm volatile("" ::: "memory");
  unsigned x = 0;
#pragma nounroll
  for (int i = 0; i < SPIN_L; ++i) {
    x = __builtin_amdgcn_readfirstlane(*(volatile LAS unsigned*)(uintptr_t)(base + L_FLAGS + f * 4));
    if (x) break;
    if (SLP) __builtin_amdgcn_s_sleep(SLP);
  }
  asm volatile("" ::: "memory");
  return x;
}
__device__ __forceinline__ bool pend4(u32x4 v) {
  unsigned a = v[0] > v[1] ? v[0] : v[1], b = v[2] > v[3] ? v[2] : v[3];
  return (a > b ? a : b) == SENT;
}
__device__ __forceinline__ void poll_round1d(u32x4 rs, unsigned& v0, unsigned o0, u64 m0) {
  u64 keep;
  asm volatile("s_mov_b64 %0, exec\n\t"
               "s_mov_b64 exec, %4\n\tbuffer_load_dword %1, %2, %3, 0 offen sc1\n\t"
               "s_mov_b64 exec, %0\n\ts_waitcnt vmcnt(0)"
               : "=&s"(keep), "+v"(v0) : "v"(o0), "s"(rs), "s"(m0) : "memory");
}
__device__ __forceinline__ void poll_round1(u32x4 rs, u32x4& v0, unsigned o0, u64 m0) {
  u64 keep;
  asm volatile("s_mov_b64 %0, exec\n\t"
               "s_mov_b64 exec, %4\n\tbuffer_load_dwordx4 %1, %2, %3, 0 offen sc1\n\t"
               "s_mov_b64 exec, %0\n\ts_waitcnt vmcnt(0)"
               : "=&s"(keep), "+v"(v0) : "v"(o0), "s"(rs), "s"(m0) : "memory");
}
__device__ __forceinline__ void poll_round2(u32x4 rs, u32x4& v0, u32x4& v1, unsigned o0, unsigned o1, u64 m0, u64 m1) {
  u64 keep;
  asm volatile("s_mov_b64 %0, exec\n\t"
               "s_mov_b64 exec, %6\n\tbuffer_load_dwordx4 %1, %3, %5, 0 offen sc1\n\t"
               "s_mov_b64 exec, %7\n\tbuffer_load_dwordx4 %2, %4, %5, 0 offen sc1\n\t"
               "s_mov_b64 exec, %0\n\ts_waitcnt vmcnt(0)"
               : "=&s"(keep), "+v"(v0), "+v"(v1) : "v"(o0), "v"(o1), "s"(rs), "s"(m0), "s"(m1) : "memory");
}
__device__ __forceinline__ void poll_round4(u32x4 rs, u32x4 (&v)[4], const unsigned (&o)[4], const u64 (&m)[4]) {
  u64 keep;
  asm volatile("s_mov_b64 %0, exec\n\t"
               "s_mov_b64 exec, %10\n\tbuffer_load_dwordx4 %1, %5, %9, 0 offen sc1\n\t"
               "s_mov_b64 exec, %11\n\tbuffer_load_dwordx4 %2, %6, %9, 0 offen sc1\n\t"
               "s_mov_b64 exec, %12\n\tbuffer_load_dwordx4 %3, %7, %9, 0 offen sc1\n\t"
               "s_mov_b64 exec, %13\n\tbuffer_load_dwordx4 %4, %8, %9, 0 offen sc1\n\t"
               "s_mov_b64 exec, %0\n\ts_waitcnt vmcnt(0)"
               : "=&s"(keep), "+v"(v[0]), "+v"(v[1]), "+v"(v[2]), "+v"(v[3])
               : "v"(o[0]), "v"(o[1]), "v"(o[2]), "v"(o[3]), "s"(rs), "s"(m[0]), "s"(m[1]), "s"(m[2]), "s"(m[3]) : "memory");
}
__device__ __forceinline__ void poll_round8(u32x4 rs, u32x4 (&v)[8], unsigned o, const u64 (&m)[8]) {
  u64 keep;
  asm volatile("s_mov_b64 %0, exec\n\t"
               "s_mov_b64 exec, %11\n\tbuffer_load_dwordx4 %1, %9, %10, 0 offen sc1\n\t"
               "s_mov_b64 exec, %12\n\tbuffer_load_dwordx4 %2, %9, %10, 0 offen offset:1024 sc1\n\t"
               "s_mov_b64 exec, %13\n\tbuffer_load_dwordx4 %3, %9, %10, 0 offen offset:2048 sc1\n\t"
               "s_mov_b64 exec, %14\n\tbuffer_load_dwordx4 %4, %9, %10, 0 offen offset:3072 sc1\n\t"
               "s_mov_b64 exec, %15\n\tbuffer_load_dwordx4 %5, %9, %10, %19 offen sc1\n\t"
               "s_mov_b64 exec, %16\n\tbuffer_load_dwordx4 %6, %9, %10, %19 offen offset:1024 sc1\n\t"
               "s_mov_b64 exec, %17\n\tbuffer_load_dwordx4 %7, %9, %10, %19 offen offset:2048 sc1\n\t"
               "s_mov_b64 exec, %18\n\tbuffer_load_dwordx4 %8, %9, %10, %19 offen offset:3072 sc1\n\t"
               "s_mov_b64 exec, %0\n\ts_waitcnt vmcnt(0)"
               : "=&s"(keep), "+v"(v[0]), "+v"(v[1]), "+v"(v[2]), "+v"(v[3]), "+v"(v[4]), "+v"(v[5]), "+v"(v[6]), "+v"(v[7])
               : "v"(o), "s"(rs), "s"(m[0]), "s"(m[1]), "s"(m[2]), "s"(m[3]), "s"(m[4]), "s"(m[5]), "s"(m[6]), "s"(m[7]), "s"(4096u) : "memory");
}
__device__ __forceinline__ void issue4(u32x4 rs, u32x4 (&v)[4], unsigned o, unsigned st) {
  asm volatile("buffer_load_dwordx4 %0, %4, %5, 0 offen sc1\n\tbuffer_load_dwordx4 %1, %4, %5, %6 offen sc1\n\t"
               "buffer_load_dwordx4 %2, %4, %5, %7 offen sc1\n\tbuffer_load_dwordx4 %3, %4, %5, %8 offen sc1"
               : "=&v"(v[0]), "=&v"(v[1]), "=&v"(v[2]), "=&v"(v[3]) : "v"(o), "s"(rs), "s"(st), "s"(2 * st), "s"(3 * st) : "memory");
}
__device__ __forceinline__ void land4(u32x4 (&v)[4]) {
  asm volatile("s_waitcnt vmcnt(0)" : "+v"(v[0]), "+v"(v[1]), "+v"(v[2]), "+v"(v[3])::"memory");
}
__device__ __forceinline__ void poll2(u32x4 rs, u32x4& A0, u32x4& A1, unsigned o0, unsigned o1) {
#pragma nounroll
  for (int spin = 0; spin < SPIN_G; ++spin) {
    const u64 m0 = __ballot(pend4(A0)), m1 = __ballot(pend4(A1));
    if ((m0 | m1) == 0) break;
    poll_round2(rs, A0, A1, o0, o1, m0, m1);
  }
}
__device__ __forceinline__ void poll4(u32x4 rs, u32x4 (&A)[4], const unsigned (&o)[4]) {
#pragma nounroll
  for (int spin = 0; spin < SPIN_G; ++spin) {
    u64 m[4];
#pragma unroll
    for (int i = 0; i < 4; ++i) m[i] = __ballot(pend4(A[i]));
    if ((m[0] | m[1] | m[2] | m[3]) == 0) break;
    poll_round4(rs, A, o, m);
  }
}
__device__ __forceinline__ void poll1(u32x4 rs, u32x4& A, unsigned o) {
#pragma nounroll
  for (int spin = 0; spin < SPIN_G; ++spin) {
    const u64 m = __ballot(pend4(A));
    if (m == 0) break;
    poll_round1(rs, A, o, m);
  }
}

__device__ __forceinline__ void ppoll2(u32x4 rs, u32x4 (&A)[2], u32x4 (&S)[6], unsigned o, unsigned st, u64 (&m)[2]) {
  unsigned t, c = SPIN_G;
  u64 x;
  asm volatile(
      "s_or_b64 exec, %[m0], 1\n\t"
      "buffer_load_dwordx4 v[80:83], %[o], %[rs], 0 offen sc1\n\t"
      "s_or_b64 exec, %[m1], 1\n\t"
      "buffer_load_dwordx4 v[84:87], %[o], %[rs], %[s1] offen sc1\n\t"
      "s_sleep %[gap]\n\t"
      "s_or_b64 exec, %[m0], 1\n\t"
      "buffer_load_dwordx4 v[88:91], %[o], %[rs], 0 offen sc1\n\t"
      "s_or_b64 exec, %[m1], 1\n\t"
      "buffer_load_dwordx4 v[92:95], %[o], %[rs], %[s1] offen sc1\n\t"
      "s_sleep %[gap]\n\t"
      "s_or_b64 exec, %[m0], 1\n\t"
      "buffer_load_dwordx4 v[96:99], %[o], %[rs], 0 offen sc1\n\t"
      "s_or_b64 exec, %[m1], 1\n\t"
      "buffer_load_dwordx4 v[100:103], %[o], %[rs], %[s1] offen sc1\n\t"
      "1:\n\t"
      "s_waitcnt vmcnt(4)\n\t"
      "s_mov_b64 exec, %[m0]\n\t"
      "v_max3_u32 %[t], v80, v81, v82\n\t"
      "v_mov_b64 v[64:65], v[80:81]\n\t"
      "v_max_u32 %[t], %[t], v83\n\t"
      "v_mov_b64 v[66:67], v[82:83]\n\t"
      "v_cmp_eq_u32 %[m0], -1, %[t]\n\t"
      "s_mov_b64 exec, %[m1]\n\t"
      "v_max3_u32 %[t], v84, v85, v86\n\t"
      "v_mov_b64 v[68:69], v[84:85]\n\t"
      "v_max_u32 %[t], %[t], v87\n\t"
      "v_mov_b64 v[70:71], v[86:87]\n\t"
      "v_cmp_eq_u32 %[m1], -1, %[t]\n\t"
      "s_or_b64 %[x], %[m0], %[m1]\n\t"
      "s_cbranch_scc0 2f\n\t"
      "s_or_b64 exec, %[m0], 1\n\t"
      "buffer_load_dwordx4 v[80:83], %[o], %[rs], 0 offen sc1\n\t"
      "s_or_b64 exec, %[m1], 1\n\t"
      "buffer_load_dwordx4 v[84:87], %[o], %[rs], %[s1] offen sc1\n\t"
      "s_waitcnt vmcnt(4)\n\t"
      "s_mov_b64 exec, %[m0]\n\t"
      "v_max3_u32 %[t], v88, v89, v90\n\t"
      "v_mov_b64 v[64:65], v[88:89]\n\t"
      "v_max_u32 %[t], %[t], v91\n\t"
      "v_mov_b64 v[66:67], v[90:91]\n\t"
      "v_cmp_eq_u32 %[m0], -1, %[t]\n\t"
      "s_mov_b64 exec, %[m1]\n\t"
      "v_max3_u32 %[t], v92, v93, v94\n\t"
      "v_mov_b64 v[68:69], v[92:93]\n\t"
      "v_max_u32 %[t], %[t], v95\n\t"
      "v_mov_b64 v[70:71], v[94:95]\n\t"
      "v_cmp_eq_u32 %[m1], -1, %[t]\n\t"
      "s_or_b64 %[x], %[m0], %[m1]\n\t"
      "s_cbranch_scc0 2f\n\t"
      "s_or_b64 exec, %[m0], 1\n\t"
      "buffer_load_dwordx4 v[88:91], %[o], %[rs], 0 offen sc1\n\t"
      "s_or_b64 exec, %[m1], 1\n\t"
      "buffer_load_dwordx4 v[92:95], %[o], %[rs], %[s1] offen sc1\n\t"
      "s_waitcnt vmcnt(4)\n\t"
      "s_mov_b64 exec, %[m0]\n\t"
      "v_max3_u32 %[t], v96, v97, v98\n\t"
      "v_mov_b64 v[64:65], v[96:97]\n\t"
      "v_max_u32 %[t], %[t], v99\n\t"
      "v_mov_b64 v[66:67], v[98:99]\n\t"
      "v_cmp_eq_u32 %[m0], -1, %[t]\n\t"
      "s_mov_b64 exec, %[m1]\n\t"
      "v_max3_u32 %[t], v100, v101, v102\n\t"
      "v_mov_b64 v[68:69], v[100:101]\n\t"
      "v_max_u32 %[t], %[t], v103\n\t"
      "v_mov_b64 v[70:71], v[102:103]\n\t"
      "v_cmp_eq_u32 %[m1], -1, %[t]\n\t"
      "s_or_b64 %[x], %[m0], %[m1]\n\t"
      "s_cbranch_scc0 2f\n\t"
      "s_or_b64 exec, %[m0], 1\n\t"
      "buffer_load_dwordx4 v[96:99], %[o], %[rs], 0 offen sc1\n\t"
      "s_or_b64 exec, %[m1], 1\n\t"
      "buffer_load_dwordx4 v[100:103], %[o], %[rs], %[s1] offen sc1\n\t"
      "s_sub_u32 %[c], %[c], 1\n\t"
      "s_cbranch_scc0 1b\n\t"
      "2:\n\t"
      "s_mov_b64 exec, -1\n\t"
      : "+{v[64:67]}"(A[0]), "+{v[68:71]}"(A[1]), "=&{v[80:83]}"(S[0]), "=&{v[84:87]}"(S[1]), "=&{v[88:91]}"(S[2]), "=&{v[92:95]}"(S[3]), "=&{v[96:99]}"(S[4]), "=&{v[100:103]}"(S[5]), [m0] "+s"(m[0]), [m1] "+s"(m[1]), [t] "=&v"(t), [x] "=&s"(x), [c] "+s"(c)
      : [o] "v"(o), [s1] "s"(st), [rs] "s"(rs), [gap] "i"(PGAP) : "memory", "scc");
}
__device__ __forceinline__ void ppoll2_drain(u32x4 (&S)[6]) {
  asm volatile("s_waitcnt vmcnt(0)" : "+{v[80:83]}"(S[0]), "+{v[84:87]}"(S[1]), "+{v[88:91]}"(S[2]), "+{v[92:95]}"(S[3]), "+{v[96:99]}"(S[4]), "+{v[100:103]}"(S[5]) :: "memory");
}
__device__ __forceinline__ void ppoll4(u32x4 rs, u32x4 (&A)[4], u32x4 (&S)[12], unsigned o, unsigned st, u64 (&m)[4]) {
  unsigned t, c = SPIN_G;
  u64 x;
  asm volatile(
      "s_or_b64 exec, %[m0], 1\n\t"
      "buffer_load_dwordx4 v[80:83], %[o], %[rs], 0 offen sc1\n\t"
      "s_or_b64 exec, %[m1], 1\n\t"
      "buffer_load_dwordx4 v[84:87], %[o], %[rs], %[s1] offen sc1\n\t"
      "s_or_b64 exec, %[m2], 1\n\t"
      "buffer_load_dwordx4 v[88:91], %[o], %[rs], %[s2] offen sc1\n\t"
      "s_or_b64 exec, %[m3], 1\n\t"
      "buffer_load_dwordx4 v[92:95], %[o], %[rs], %[s3] offen sc1\n\t"
      "s_sleep %[gap]\n\t"
      "s_or_b64 exec, %[m0], 1\n\t"
      "buffer_load_dwordx4 v[96:99], %[o], %[rs], 0 offen sc1\n\t"
      "s_or_b64 exec, %[m1], 1\n\t"
      "buffer_load_dwordx4 v[100:103], %[o], %[rs], %[s1] offen sc1\n\t"
      "s_or_b64 exec, %[m2], 1\n\t"
      "buffer_load_dwordx4 v[104:107], %[o], %[rs], %[s2] offen sc1\n\t"
      "s_or_b64 exec, %[m3], 1\n\t"
      "buffer_load_dwordx4 v[108:111], %[o], %[rs], %[s3] offen sc1\n\t"
      "s_sleep %[gap]\n\t"
      "s_or_b64 exec, %[m0], 1\n\t"
      "buffer_load_dwordx4 v[112:115], %[o], %[rs], 0 offen sc1\n\t"
      "s_or_b64 exec, %[m1], 1\n\t"
      "buffer_load_dwordx4 v[116:119], %[o], %[rs], %[s1] offen sc1\n\t"
      "s_or_b64 exec, %[m2], 1\n\t"
      "buffer_load_dwordx4 v[120:123], %[o], %[rs], %[s2] offen sc1\n\t"
      "s_or_b64 exec, %[m3], 1\n\t"
      "buffer_load_dwordx4 v[124:127], %[o], %[rs], %[s3] offen sc1\n\t"
      "1:\n\t"
      "s_waitcnt vmcnt(8)\n\t"
      "s_mov_b64 exec, %[m0]\n\t"
      "v_max3_u32 %[t], v80, v81, v82\n\t"
      "v_mov_b64 v[64:65], v[80:81]\n\t"
      "v_max_u32 %[t], %[t], v83\n\t"
      "v_mov_b64 v[66:67], v[82:83]\n\t"
      "v_cmp_eq_u32 %[m0], -1, %[t]\n\t"
      "s_mov_b64 exec, %[m1]\n\t"
      "v_max3_u32 %[t], v84, v85, v86\n\t"
      "v_mov_b64 v[68:69], v[84:85]\n\t"
      "v_max_u32 %[t], %[t], v87\n\t"
      "v_mov_b64 v[70:71], v[86:87]\n\t"
      "v_cmp_eq_u32 %[m1], -1, %[t]\n\t"
      "s_mov_b64 exec, %[m2]\n\t"
      "v_max3_u32 %[t], v88, v89, v90\n\t"
      "v_mov_b64 v[72:73], v[88:89]\n\t"
      "v_max_u32 %[t], %[t], v91\n\t"
      "v_mov_b64 v[74:75], v[90:91]\n\t"
      "v_cmp_eq_u32 %[m2], -1, %[t]\n\t"
      "s_mov_b64 exec, %[m3]\n\t"
      "v_max3_u32 %[t], v92, v93, v94\n\t"
      "v_mov_b64 v[76:77], v[92:93]\n\t"
      "v_max_u32 %[t], %[t], v95\n\t"
      "v_mov_b64 v[78:79], v[94:95]\n\t"
      "v_cmp_eq_u32 %[m3], -1, %[t]\n\t"
      "s_or_b64 %[x], %[m0], %[m1]\n\t"
      "s_or_b64 %[x], %[x], %[m2]\n\t"
      "s_or_b64 %[x], %[x], %[m3]\n\t"
      "s_cbranch_scc0 2f\n\t"
      "s_or_b64 exec, %[m0], 1\n\t"
      "buffer_load_dwordx4 v[80:83], %[o], %[rs], 0 offen sc1\n\t"
      "s_or_b64 exec, %[m1], 1\n\t"
      "buffer_load_dwordx4 v[84:87], %[o], %[rs], %[s1] offen sc1\n\t"
      "s_or_b64 exec, %[m2], 1\n\t"
      "buffer_load_dwordx4 v[88:91], %[o], %[rs], %[s2] offen sc1\n\t"
      "s_or_b64 exec, %[m3], 1\n\t"
      "buffer_load_dwordx4 v[92:95], %[o], %[rs], %[s3] offen sc1\n\t"
      "s_waitcnt vmcnt(8)\n\t"
      "s_mov_b64 exec, %[m0]\n\t"
      "v_max3_u32 %[t], v96, v97, v98\n\t"
      "v_mov_b64 v[64:65], v[96:97]\n\t"
      "v_max_u32 %[t], %[t], v99\n\t"
      "v_mov_b64 v[66:67], v[98:99]\n\t"
      "v_cmp_eq_u32 %[m0], -1, %[t]\n\t"
      "s_mov_b64 exec, %[m1]\n\t"
      "v_max3_u32 %[t], v100, v101, v102\n\t"
      "v_mov_b64 v[68:69], v[100:101]\n\t"
      "v_max_u32 %[t], %[t], v103\n\t"
      "v_mov_b64 v[70:71], v[102:103]\n\t"
      "v_cmp_eq_u32 %[m1], -1, %[t]\n\t"
      "s_mov_b64 exec, %[m2]\n\t"
      "v_max3_u32 %[t], v104, v105, v106\n\t"
      "v_mov_b64 v[72:73], v[104:105]\n\t"
      "v_max_u32 %[t], %[t], v107\n\t"
      "v_mov_b64 v[74:75], v[106:107]\n\t"
      "v_cmp_eq_u32 %[m2], -1, %[t]\n\t"
      "s_mov_b64 exec, %[m3]\n\t"
      "v_max3_u32 %[t], v108, v109, v110\n\t"
      "v_mov_b64 v[76:77], v[108:109]\n\t"
      "v_max_u32 %[t], %[t], v111\n\t"
      "v_mov_b64 v[78:79], v[110:111]\n\t"
      "v_cmp_eq_u32 %[m3], -1, %[t]\n\t"
      "s_or_b64 %[x], %[m0], %[m1]\n\t"
      "s_or_b64 %[x], %[x], %[m2]\n\t"
      "s_or_b64 %[x], %[x], %[m3]\n\t"
      "s_cbranch_scc0 2f\n\t"
      "s_or_b64 exec, %[m0], 1\n\t"
      "buffer_load_dwordx4 v[96:99], %[o], %[rs], 0 offen sc1\n\t"
      "s_or_b64 exec, %[m1], 1\n\t"
      "buffer_load_dwordx4 v[100:103], %[o], %[rs], %[s1] offen sc1\n\t"
      "s_or_b64 exec, %[m2], 1\n\t"
      "buffer_load_dwordx4 v[104:107], %[o], %[rs], %[s2] offen sc1\n\t"
      "s_or_b64 exec, %[m3], 1\n\t"
      "buffer_load_dwordx4 v[108:111], %[o], %[rs], %[s3] offen sc1\n\t"
      "s_waitcnt vmcnt(8)\n\t"
      "s_mov_b64 exec, %[m0]\n\t"
      "v_max3_u32 %[t], v112, v113, v114\n\t"
      "v_mov_b64 v[64:65], v[112:113]\n\t"
      "v_max_u32 %[t], %[t], v115\n\t"
      "v_mov_b64 v[66:67], v[114:115]\n\t"
      "v_cmp_eq_u32 %[m0], -1, %[t]\n\t"
      "s_mov_b64 exec, %[m1]\n\t"
      "v_max3_u32 %[t], v116, v117, v118\n\t"
      "v_mov_b64 v[68:69], v[116:117]\n\t"
      "v_max_u32 %[t], %[t], v119\n\t"
      "v_mov_b64 v[70:71], v[118:119]\n\t"
      "v_cmp_eq_u32 %[m1], -1, %[t]\n\t"
      "s_mov_b64 exec, %[m2]\n\t"
      "v_max3_u32 %[t], v120, v121, v122\n\t"
      "v_mov_b64 v[72:73], v[120:121]\n\t"
      "v_max_u32 %[t], %[t], v123\n\t"
      "v_mov_b64 v[74:75], v[122:123]\n\t"
      "v_cmp_eq_u32 %[m2], -1, %[t]\n\t"
      "s_mov_b64 exec, %[m3]\n\t"
      "v_max3_u32 %[t], v124, v125, v126\n\t"
      "v_mov_b64 v[76:77], v[124:125]\n\t"
      "v_max_u32 %[t], %[t], v127\n\t"
      "v_mov_b64 v[78:79], v[126:127]\n\t"
      "v_cmp_eq_u32 %[m3], -1, %[t]\n\t"
      "s_or_b64 %[x], %[m0], %[m1]\n\t"
      "s_or_b64 %[x], %[x], %[m2]\n\t"
      "s_or_b64 %[x], %[x], %[m3]\n\t"
      "s_cbranch_scc0 2f\n\t"
      "s_or_b64 exec, %[m0], 1\n\t"
      "buffer_load_dwordx4 v[112:115], %[o], %[rs], 0 offen sc1\n\t"
      "s_or_b64 exec, %[m1], 1\n\t"
      "buffer_load_dwordx4 v[116:119], %[o], %[rs], %[s1] offen sc1\n\t"
      "s_or_b64 exec, %[m2], 1\n\t"
      "buffer_load_dwordx4 v[120:123], %[o], %[rs], %[s2] offen sc1\n\t"
      "s_or_b64 exec, %[m3], 1\n\t"
      "buffer_load_dwordx4 v[124:127], %[o], %[rs], %[s3] offen sc1\n\t"
      "s_sub_u32 %[c], %[c], 1\n\t"
      "s_cbranch_scc0 1b\n\t"
      "2:\n\t"
      "s_mov_b64 exec, -1\n\t"
      : "+{v[64:67]}"(A[0]), "+{v[68:71]}"(A[1]), "+{v[72:75]}"(A[2]), "+{v[76:79]}"(A[3]), "=&{v[80:83]}"(S[0]), "=&{v[84:87]}"(S[1]), "=&{v[88:91]}"(S[2]), "=&{v[92:95]}"(S[3]), "=&{v[96:99]}"(S[4]), "=&{v[100:103]}"(S[5]), "=&{v[104:107]}"(S[6]), "=&{v[108:111]}"(S[7]), "=&{v[112:115]}"(S[8]), "=&{v[116:119]}"(S[9]), "=&{v[120:123]}"(S[10]), "=&{v[124:127]}"(S[11]), [m0] "+s"(m[0]), [m1] "+s"(m[1]), [m2] "+s"(m[2]), [m3] "+s"(m[3]), [t] "=&v"(t), [x] "=&s"(x), [c] "+s"(c)
      : [o] "v"(o), [s1] "s"(st), [s2] "s"(2 * st), [s3] "s"(3 * st), [rs] "s"(rs), [gap] "i"(PGAP) : "memory", "scc");
}
__device__ __forceinline__ void ppoll4_drain(u32x4 (&S)[12]) {
  asm volatile("s_waitcnt vmcnt(0)" : "+{v[80:83]}"(S[0]), "+{v[84:87]}"(S[1]), "+{v[88:91]}"(S[2]), "+{v[92:95]}"(S[3]), "+{v[96:99]}"(S[4]), "+{v[100:103]}"(S[5]), "+{v[104:107]}"(S[6]), "+{v[108:111]}"(S[7]), "+{v[112:115]}"(S[8]), "+{v[116:119]}"(S[9]), "+{v[120:123]}"(S[10]), "+{v[124:127]}"(S[11]) :: "memory");
}

#define TS(k) do {} while (0)
#define TSEND() do {} while (0)

__global__ void __launch_bounds__(NTHR, 4)
fused_kernel(unsigned char* __restrict__ ws, const unsigned char* __restrict__ x, const unsigned char* __restrict__ wq, const unsigned char* __restrict__ kc,
             const unsigned char* __restrict__ vc, const unsigned char* __restrict__ wo, const unsigned char* __restrict__ wg,
             const unsigned char* __restrict__ wu, const unsigned char* __restrict__ wd, unsigned short* __restrict__ out) {
  extern __shared__ __attribute__((aligned(16))) unsigned char smem[];
  const unsigned L0 = (unsigned)(uintptr_t)(LAS unsigned char*)smem;
  const int tid = threadIdx.x, lane = tid & 63, wave0 = __builtin_amdgcn_readfirstlane(tid >> 6);
  const int w = blockIdx.x, kvh = w & 7, idx = w >> 3, bat = idx >> 4, spl = idx & 15;
  const int n16 = lane & 15, g4 = lane >> 4;
  const int prod = (bat * 8 + kvh) * 16 + spl;
  const int nC = kvh < 4 ? 8 : 6;
  const int tk = idx < nC ? (kvh < 4 ? 8 * kvh : 8 + 6 * kvh) + idx : idx < nC + 16 ? 56 + 16 * kvh + (idx - nC) : idx < nC + 20 ? 184 + 4 * kvh + (idx - nC - 16) : 255;
  const int role = tk < 56 ? 1 : tk < 184 ? 0 : tk < 216 ? 2 : 3;
  const int rq = idx & 3, cbk = idx >> 2;
  const __amdgpu_buffer_rsrc_t rsb = __builtin_amdgcn_make_buffer_rsrc(ws, 0, WS_END, 0x00020000);
  const u32x4 rs = {(unsigned)(uintptr_t)ws, (unsigned)((uintptr_t)ws >> 32) & 0xffffu, (unsigned)WS_END, 0x00020000u};
#define TRDEF()          \
  int lane_ = lane;      \
  asm volatile("" : "+v"(lane_)); \
  const unsigned trq = (lane_ >> 2) & 3, trp = lane_ & 3
  const u32x4 ones = {ONE2, ONE2, ONE2, ONE2};
  const size_t chunk = ((size_t)(bat * 8 + kvh) * 8192 + spl * 512) * 256;

  if (wave0 == 0) {
    __builtin_amdgcn_s_setprio(3);
    if (lane < 16) lds_st<unsigned>(L0 + L_FLAGS + lane * 4, 0u);
    u32x2 ep;
    asm volatile("global_load_dwordx2 %0, %1, %2 sc1" : "=v"(ep) : "v"(0u), "s"(ws) : "memory");
    u32x4 xs0, xs1, xa[4];
    ldv(xs0, x, lane * 16);
    ldv(xs1, x + 1024, lane * 16);
    asm volatile("s_waitcnt lgkmcnt(0)\n\ts_barrier" ::: "memory");
    {
      const u32x4 s4 = {SENT, SENT, SENT, SENT};
      const int OOB = 0x7ffffff0;
      int o1 = OOB;
      if (lane < 2) o1 = WS_ST + (kvh * 32 + idx) * 32 + lane * 16;
      const int o2 = (idx >= 16 && idx < 31) ? WS_YP + (kvh * 15 + idx - 16) * 2048 + lane * 16 : OOB;
      asm volatile("buffer_store_dwordx4 %0, %1, %4, 0 offen sc1\n\t"
                   "buffer_store_dwordx4 %0, %2, %4, 0 offen sc1\n\t"
                   "buffer_store_dwordx4 %0, %3, %4, 0 offen sc1\n\t"
                   "buffer_store_dwordx4 %0, %3, %4, 0 offen offset:1024 sc1\n\ts_nop 1"
                   :: "v"(s4), "v"(WS_OP + prod * 1024 + lane * 16), "v"(o1), "v"(o2), "s"(rs) : "memory");
    }
    asm volatile("s_waitcnt vmcnt(4)" : "+v"(ep), "+v"(xs0), "+v"(xs1)::"memory");
    if (g4 == rq) {
      lds_st<u32x4>(L0 + L_Q + n16 * 16, xs0);
      lds_st<u32x4>(L0 + L_Q + 256 + n16 * 16, xs1);
    }
#pragma unroll
    for (int t = 0; t < 4; ++t) xa[t] = lds_ld<u32x4>(L0 + L_Q + (n16 & 1) * 256 + t * 64 + g4 * 16);
    const u64 tag = (((u64)ep[1] << 32) | ep[0]) + 1;
    const unsigned tlo = __builtin_amdgcn_readfirstlane((unsigned)tag), thi = __builtin_amdgcn_readfirstlane((unsigned)(tag >> 32));
    flag_set(L0, F_TAG, tlo << 2, lane);
    float mu, rr;
    {
      float s0 = 0.f, q0 = 0.f, s1 = 0.f, q1 = 0.f;
#pragma unroll
      for (int j = 0; j < 4; ++j) {
        const float a = bflo(xs0[j]), b = bfhi(xs0[j]), c = bflo(xs1[j]), d = bfhi(xs1[j]);
        s0 += a + b; q0 += a * a + b * b; s1 += c + d; q1 += c * c + d * d;
      }
      s0 = wave_sum(s0); q0 = wave_sum(q0); s1 = wave_sum(s1); q1 = wave_sum(q1);
      const float s = (lane & 1) ? s1 : s0, q = (lane & 1) ? q1 : q0;
      mu = s * (1.f / 512.f);
      rr = 1.f / sqrtf(fmaxf(q * (1.f / 512.f) - mu * mu, 0.f) + 1e-5f);
    }
#pragma unroll
    for (int t = 0; t < 4; ++t) xa[t] = (n16 & 3) == 2 ? ones : xa[t];
    flag_wait<0>(L0, F_WQ, 4u);
    TS(1);
    {
      f32x4 d[4];
      TRDEF();
      const unsigned a0 = L0 + L_WQ + (8 * g4 + trq) * 128 + trp * 8, sw = (g4 & 1) * 32;
#pragma unroll
      for (int nt = 0; nt < 4; ++nt) {
        d[nt] = f32x4{0.f, 0.f, 0.f, 0.f};
#pragma unroll
        for (int t = 0; t < 4; ++t) d[nt] = mfma(trfrag(a0 + t * 4096 + ((nt * 32) ^ sw), a0 + t * 4096 + ((nt * 32) ^ sw) + 512), xa[t], d[nt]);
      }
      const f32x4 lo = (n16 & 4) ? d[1] : d[0], hi = (n16 & 4) ? d[3] : d[2];
      const f32x4 dd = (n16 & 8) ? hi : lo;
      float qv[4];
#pragma unroll
      for (int r = 0; r < 4; ++r) qv[r] = rr * (dd[r] - mu * dpp<0xAA>(dd[r]));
      if ((n16 & 3) < 2)
        __builtin_amdgcn_raw_buffer_store_b128(u32x4{tlo, thi, pk(qv[0], qv[1]), pk(qv[2], qv[3])}, rsb,
                                               WS_QP + (((kvh * 2 + (n16 & 1)) * 4 + rq) * 128 + cbk * 16 + (n16 >> 2) * 4 + g4) * 16, 0, 16);
    }
    waitvm<1>();
    if (lane == 0) __builtin_amdgcn_raw_buffer_store_b64(u32x2{tlo, thi}, rsb, WS_FL + w * 8, 0, 16);
    TS(2);
    {
      u32x4 v[8];
      const unsigned ob = WS_QP + (kvh * 2 + bat) * 8192 + lane * 16;
#pragma unroll
      for (int i = 0; i < 8; ++i) v[i] = u32x4{~tlo, 0u, 0u, 0u};
      if (QDELAY) __builtin_amdgcn_s_sleep(QDELAY);
#pragma nounroll
      for (int spin = 0; spin < SPIN_G; ++spin) {
        u64 m[8];
#pragma unroll
        for (int i = 0; i < 8; ++i) m[i] = __ballot(v[i][0] != tlo || v[i][1] != thi);
        if ((m[0] | m[1] | m[2] | m[3] | m[4] | m[5] | m[6] | m[7]) == 0) break;
        poll_round8(rs, v, ob, m);
      }
#pragma unroll
      for (int h = 0; h < 2; ++h) {
        u32x2 r;
#pragma unroll
        for (int j = 0; j < 2; ++j) {
          const float lo = (bflo(v[h][2 + j]) + bflo(v[2 + h][2 + j])) + (bflo(v[4 + h][2 + j]) + bflo(v[6 + h][2 + j]));
          const float hi = (bfhi(v[h][2 + j]) + bfhi(v[2 + h][2 + j])) + (bfhi(v[4 + h][2 + j]) + bfhi(v[6 + h][2 + j]));
          r[j] = pk(lo, hi);
        }
        lds_st<u32x2>(L0 + L_Q + (64 * h + lane) * 8, r);
      }
      flag_set(L0, F_Q, 1u, lane);
    }
    TS(3);
#pragma nounroll
    for (int spin = 0; spin < SPIN_G; ++spin) {
      asm volatile("" ::: "memory");
      const u32x4 f0 = __builtin_amdgcn_raw_buffer_load_b128(rsb, WS_FL + lane * 16, 0, 16);
      const u32x4 f1 = __builtin_amdgcn_raw_buffer_load_b128(rsb, WS_FL + 1024 + lane * 16, 0, 16);
      const bool ok = f0[0] == tlo && f0[1] == thi && f0[2] == tlo && f0[3] == thi && f1[0] == tlo && f1[1] == thi && f1[2] == tlo && f1[3] == thi;
      if (__all(ok)) break;
    }
    flag_set(L0, F_ALL, 1u, lane);
    if (w == NWG - 1 && lane == 0) __hip_atomic_store((GAS u64*)(ws + WS_EPOCH), tag, RLX_AGENT);
    TS(4);
    flag_wait<1>(L0, F_STAT, 4u);
    {
      const float m = lds_ld<float>(L0 + L_WM + n16 * 4), ls = lds_ld<float>(L0 + L_WL + n16 * 4);
      float M = fmaxf(m, dpp<ROR4>(m)); M = fmaxf(M, dpp<ROR8>(M));
      const float f = __builtin_amdgcn_exp2f(m - M);
      float L = f * ls;
      L += dpp<ROR4>(L); L += dpp<ROR8>(L);
      if (lane < 16) lds_st<float>(L0 + L_F + lane * 4, f);
      flag_set(L0, F_FAC, 1u, lane);
      if (lane < 4)
        __builtin_amdgcn_raw_buffer_store_b64(u32x2{nosent(__float_as_uint(M)), nosent(__float_as_uint(L))}, rsb, WS_ST + (kvh * 32 + idx) * 32 + lane * 8, 0, 16);
    }
    TS(5);
    if (role == 0) {
      u32x4 st = {SENT, SENT, SENT, SENT};
      flag_wait<1>(L0, F_VISS, ST_AT);
      poll1(rs, st, WS_ST + ((tk - 56) >> 4) * 1024 + lane * 16);
      const float m0 = __uint_as_float(st[0]), l0 = __uint_as_float(st[1]), m1 = __uint_as_float(st[2]), l1 = __uint_as_float(st[3]);
      float M0 = m0, M1 = m1;
#pragma unroll
      for (int o2 = 2; o2 <= 16; o2 <<= 1) { M0 = fmaxf(M0, __shfl_xor(M0, o2)); M1 = fmaxf(M1, __shfl_xor(M1, o2)); }
      const float e0 = __builtin_amdgcn_exp2f(m0 - M0), e1 = __builtin_amdgcn_exp2f(m1 - M1);
      float S0 = e0 * l0, S1 = e1 * l1;
#pragma unroll
      for (int o2 = 2; o2 <= 16; o2 <<= 1) { S0 += __shfl_xor(S0, o2); S1 += __shfl_xor(S1, o2); }
      const int bb = lane >> 5, ss = (lane >> 1) & 15, h0 = (lane & 1) * 2;
      lds_st<float>(L0 + L_W + ((bb * 4 + h0) * 16 + ss) * 4, e0 / S0);
      lds_st<float>(L0 + L_W + ((bb * 4 + h0 + 1) * 16 + ss) * 4, e1 / S1);
      flag_set(L0, F_W, 1u, lane);
      TS(6);
    }
    TSEND();
    return;
  }

  if (wave0 <= 4) {
    const int kw = wave0 - 1;
    const u64 tk0 = __builtin_amdgcn_s_memrealtime();
    __builtin_amdgcn_s_setprio(2);
    const unsigned LK = L0 + kw * 32768;
    const unsigned char* kb = kc + chunk + kw * (ILV == 2 ? 8192 : ILV ? 4096 : 32768);
    const int rk8 = (RKA * idx + RKB * kw + RKC * kvh + RKD * (idx >> 2)) & 7;
    {
      unsigned kvo[4];
#pragma unroll
      for (int a = 0; a < 4; ++a) kvo[a] = g4 * 256 + ((n16 ^ (4 * g4 + a)) * 16);
      {
        const unsigned char* base = wq + (size_t)(rq * 128 + kw * 32) * 8192 + (kvh * 512 + cbk * 64) * 2;
        const unsigned voff = (lane >> 3) * 8192 + (lane & 7) * 16;
#pragma unroll
        for (int i = 0; i < 4; ++i) dma_nt(base + (size_t)i * 8 * 8192, voff ^ ((i & 1) * 32), L0 + L_WQ + (kw * 4 + i) * 1024);
      }
#pragma unroll
      for (int i = 0; i < 32; ++i) {
        const unsigned ko = ((i + 4 * rk8) & 31) * 1024;
        dma_nt(kb + (ILV == 2 ? (ko & 0x6000) * 4 + (ko & 0x1c00) : ILV ? (ko & 0x7000) * 4 + (ko & 0xc00) : ko), kvo[i & 3], LK + ko);
        if (i == 3) {
          asm volatile("s_barrier" ::: "memory");
          waitvm<4>();
          flag_add(L0, F_WQ, lane);
        }
        if (i == KISS_AT) flag_add(L0, F_KISS, lane);
        if (i > 3 && i < 31) {
          if (i >= KWIN - 1) waitvm<KWIN - 1>();
          if (KW_B < KWIN && i >= KW_B - 1 && role == 0) waitvm<KW_B - 1>();
          if (KW_N < KWIN && i >= KW_N - 1 && role >= 2) waitvm<KW_N - 1>();
        }
      }
    }
    TS(1);
    flag_wait<1>(L0, F_Q, 1u);
    TS(2);
    u32x4 qf[4];
#pragma unroll
    for (int t = 0; t < 4; ++t) qf[t] = lds_ld<u32x4>(L0 + L_Q + (n16 & 3) * 256 + t * 64 + g4 * 16);
    f32x4 sel[2];
    const unsigned krow = LK + (4 * (n16 & 3) + (n16 >> 2)) * 256;
#pragma unroll
    for (int hf = 0; hf < 2; ++hf) {
      if (hf == 1) waitvm<0>();
      f32x4 S[4];
#pragma unroll
      for (int T = 0; T < 4; ++T) {
        S[T] = f32x4{0.f, 0.f, 0.f, 0.f};
#pragma unroll
        for (int t = 0; t < 4; ++t) S[T] = mfma(lds_ld<u32x4>(krow + ((4 * hf + T + rk8) & 7) * 4096 + (((4 * t + g4) ^ n16) * 16)), qf[t], S[T]);
      }
      const f32x4 lo = (n16 & 4) ? S[1] : S[0], hi = (n16 & 4) ? S[3] : S[2];
      sel[hf] = (n16 & 8) ? hi : lo;
    }
    float m = fmaxf(fmaxf(fmaxf(sel[0][0], sel[0][1]), fmaxf(sel[0][2], sel[0][3])), fmaxf(fmaxf(sel[1][0], sel[1][1]), fmaxf(sel[1][2], sel[1][3])));
    m = fmaxf(m, dpp<ROR4>(m)); m = fmaxf(m, dpp<ROR8>(m));
    m = x32_max(x16_max(m));
    const float ms = m * QSCALE;
    float l = 0.f;
#pragma unroll
    for (int hf = 0; hf < 2; ++hf) {
      u32x4 pp;
#pragma unroll
      for (int r = 0; r < 4; ++r) {
        const float p = __builtin_amdgcn_exp2f(fmaf(sel[hf][r], QSCALE, -ms));
        pp[r] = pk(p, p);
        l += bfhi(pp[r]);
      }
      const int ta = (4 * hf + (n16 >> 2) + rk8) & 7;
      lds_st<u32x4>(L0 + L_P + (2 * kw + (ILV ? ta & 1 : ta >> 2)) * 1024 + (((n16 & 3) * 4 + g4) * 16 + 4 * (ILV ? ta >> 1 : ta & 3)) * 4, pp);
    }
    flag_set(L0, F_P0 + kw, 1u, lane);
    l += dpp<ROR4>(l); l += dpp<ROR8>(l);
    l = x32_sum(x16_sum(l));
    if (lane < 4) {
      lds_st<float>(L0 + L_WM + (kw * 4 + lane) * 4, ms);
      lds_st<float>(L0 + L_WL + (kw * 4 + lane) * 4, l);
    }
    flag_add(L0, F_STAT, lane);
    TS(3);
    __builtin_amdgcn_s_setprio(0);
    const int cc = tk;
    if (role == 3 || CPF == 2) {
      u64 pc;
      int dlt, len;
      asm volatile("s_getpc_b64 %0\n\t.Lfk_h:\n\ts_mov_b32 %1, .Lfk_tb-.Lfk_h\n\ts_mov_b32 %2, .Lfk_te-.Lfk_tb" : "=s"(pc), "=s"(dlt), "=s"(len));
      const int o = (kw * 64 + lane) * 64;
      if (o + 4 <= len) touch((const void*)(pc + (long long)dlt), o, LK + 15360);
    }
    auto help = [&]() {
      const unsigned pv = lds_ld<unsigned>(L0 + L_FLAGS + F_TAG * 4);
      const unsigned d = __builtin_amdgcn_raw_buffer_load_b32(rsb, WS_PR + kvh * 1024 + kw * 256 + lane * 4, 0, 16) ^ pv;
      const int rot = (idx * 11) & 63;
      int budget = HB;
#pragma unroll
      for (int pass = 0; pass < 2; ++pass) {
        u64 m = __ballot(pass == 0 ? (d == 0u || d > 3u) : d == 1u);
        m = (m >> rot) | (rot ? m << (64 - rot) : 0ull);
        while (m && budget > 0) {
          const int pp = (__builtin_ctzll(m) + rot) & 63, vi = 8 * kw + (pp >> 3);
          m &= m - 1;
          --budget;
          const unsigned char* base = vc + ((size_t)((vi >> 4) * 8 + kvh) * 8192 + (vi & 15) * 512) * 256 + (pp & 7) * 16384;
          const unsigned off = ((3 - (lane >> 5) + (ROT ? vi : 0)) & 3) * 4096 + (lane & 31) * 128;
          if (pass == 0 || lane < 32) touch(base, off, LK + 15360);
        }
      }
    };
    if (role <= 1) flag_wait<1>(L0, F_VISS, role == 0 ? DMA_B : DMA_C);
    if (DLB | DLC | DLH) {
      const long long dl = role == 0 ? DLB : role == 1 ? DLC : DLH;
#pragma nounroll
      for (int i = 0; i < 4096 && (long long)(__builtin_amdgcn_s_memrealtime() - tk0) < dl; ++i) __builtin_amdgcn_s_sleep(2);
    }
    if (role == 0) {
      const int rk = (tk - 56) >> 3, cq = (tk - 56) & 7;
      const unsigned char* base = wo + (size_t)(rk * 256 + kw * 64) * 1024 + cq * 128;
      const unsigned voff = (lane >> 3) * 1024 + (((lane & 7) ^ (g4 << 1)) * 16);
#pragma unroll
      for (int j = 0; j < 8; ++j) dma_nt(base + (size_t)j * 8 * 1024, voff, LK + j * 1024);
      TS(4);
      waitvm<4>();
      flag_set(L0, F_P0 + kw, 2u, lane);
      waitvm<0>();
      flag_set(L0, F_P0 + kw, 3u, lane);
      TS(5);
      if (HB != 0 && HELPB != 0) { flag_wait<1>(L0, F_S2, 2u); help(); }
    } else if (role == 1) {
      const unsigned vf[2] = {(lane >> 2) * 3584u + (lane & 3) * 16u, ((lane >> 2) ^ 1) * 3584u + (lane & 3) * 16u};
      const unsigned char* bg = wg + (size_t)kw * 128 * 3584 + cc * 64;
      const unsigned char* bu = wu + (size_t)kw * 128 * 3584 + cc * 64;
#pragma unroll
      for (int j = 0; j < 8; ++j) {
        const int jj = (j + 4 * (cc & 1)) & 7;
        dma_nt(bg + (size_t)jj * 16 * 3584, vf[j & 1], LK + (jj >> 2) * 16384 + (jj & 3) * 1024);
        dma_nt(bu + (size_t)jj * 16 * 3584, vf[j & 1], LK + (jj >> 2) * 16384 + 4096 + (jj & 3) * 1024);
      }
      const unsigned char* bd = wd + (size_t)cc * 32 * 1024 + kw * 256;
      const unsigned voff2 = (lane >> 3) * 1024 + (lane & 7) * 16;
#pragma unroll
      for (int j = 0; j < 8; ++j) dma_nt(bd + (size_t)(j & 3) * 8 * 1024 + (j >> 2) * 128, voff2 ^ ((j & 1) * 32), LK + (j >> 2) * 16384 + 8192 + (j & 3) * 1024);
      TS(4);
      waitvm<16>();
      flag_set(L0, F_P0 + kw, 2u, lane);
      waitvm<8>();
      flag_set(L0, F_P0 + kw, 3u, lane);
      TS(5);
      waitvm<0>();
      flag_set(L0, F_P0 + kw, 4u, lane);
      TS(6);
    }
    else {
      const int nh = (16 - nC) * 4, cc0 = kvh < 4 ? 8 * kvh : 8 + 6 * kvh;
      if (PF_AT) flag_wait<1>(L0, F_VISS, PF_AT); else flag_wait<1>(L0, F_S2, 2u);
      if (HB) help();
      for (int t = (idx - nC - 16) * 4 + kw; t < 4 * nC + 16 * PFL; t += nh) {
        if (t < 4 * nC) {
          touch(wd + (size_t)cc0 * 32768 + t * 8192, lane * 128, LK + 15360);
        } else {
          const int u = t - 4 * nC, row = ((u >> 2) & 3) * 128 + ((u & 1) * 4 + 3 - (u >> 4)) * 16;
          if (2 * (lane & 3) < nC)
            touch(((u & 2) ? wu : wg) + (size_t)row * 3584 + cc0 * 64, (lane >> 2) * 3584 + (lane & 3) * 128, LK + 15360);
        }
      }
      TS(4);
    }
    waitvm<0>();
    TS(7);
    TSEND();
    return;
  }

  const int wave = wave0 - 5;
  const unsigned LW = L0 + wave * 16384;
  const unsigned char* vb = vc + chunk + (ILV == 2 ? wave * 4096 : ILV ? (wave & 1) * 16384 + (wave >> 1) * 4096 : wave * 16384);
  constexpr int VTS = ILV ? 32768 : 4096;
  __builtin_amdgcn_s_setprio(2);
  asm volatile("s_barrier" ::: "memory");
  u32x4 msk;
  {
    const unsigned hm = (lane & 1) ? 0xffff0000u : 0x0000ffffu;
    const int dsel = (lane & 7) >> 1;
    msk = u32x4{dsel == 0 ? hm : 0u, dsel == 1 ? hm : 0u, dsel == 2 ? hm : 0u, dsel == 3 ? hm : 0u};
  }
  u32x4 V[16];
  const unsigned vo = lane * 16;
  const int rv = (RVA * idx + RVB * wave + RVC * kvh + RVD * (idx >> 2)) & 3;
  flag_wait<1>(L0, F_KISS, 4u);
  TS(1);
#pragma unroll
  for (int i = 0; i < VN1; ++i) {
    ldv_nt(V[i], vb + (((i >> 2) + rv) & 3) * VTS + (i & 3) * 1024, vo);
  }
  TS(2);
  const unsigned pvt = lds_ld<unsigned>(L0 + L_FLAGS + F_TAG * 4), pro = WS_PR + (kvh * 32 + idx) * 32 + wave * 4;
  if (HB) asm volatile("s_mov_b64 exec, 1\n\tbuffer_store_dword %0, %1, %2, 0 offen sc1\n\ts_mov_b64 exec, -1" ::"v"(pvt | 1u), "v"(pro), "s"(rs) : "memory");
  flag_wait<1>(L0, F_P0 + (wave >> 1), 1u);
  u32x4 P[2][4];
#pragma unroll
  for (int u = 0; u < 2; ++u)
#pragma unroll
    for (int k = 0; k < 4; ++k) P[u][k] = lds_ld<u32x4>(L0 + L_P + wave * 1024 + ((2 * u + (n16 >> 3)) * 4 + g4) * 64 + ((k + rv) & 3) * 16);
  f32x4 acc[2] = {{0.f, 0.f, 0.f, 0.f}, {0.f, 0.f, 0.f, 0.f}};
  TS(3);
#define PV(j, N)                                                                        \
  do {                                                                                  \
    asm volatile("s_waitcnt vmcnt(%1)" : "+v"(V[j]) : "i"(N) : "memory");               \
    const u32x4 b0 = msk & P[0][(j) >> 2][(j) & 3], b1 = msk & P[1][(j) >> 2][(j) & 3]; \
    acc[0] = mfma(V[j], b0, acc[0]);                                                    \
    acc[1] = mfma(V[j], b1, acc[1]);                                                    \
    __builtin_amdgcn_sched_barrier(0);                                                  \
  } while (0)
#pragma unroll
  for (int j = 0; j < 16 - VN1; ++j) {
    ldv_nt(V[VN1 + j], vb + ((((VN1 + j) >> 2) + rv) & 3) * VTS + ((VN1 + j) & 3) * 1024, vo);
    if (j == 15 - VN1) {
      flag_add(L0, F_VISS, lane);
      if (HB) asm volatile("s_mov_b64 exec, 1\n\tbuffer_store_dword %0, %1, %2, 0 offen sc1\n\ts_mov_b64 exec, -1" ::"v"(pvt | 2u), "v"(pro), "s"(rs) : "memory");
    }
    PV(2 * j, VN1 - j);
    PV(2 * j + 1, VN1 - j - 1);
  }
  TS(4);
  flag_wait<0>(L0, F_FAC, 1u);
  unsigned oa = L0 + L_RB_O + wave * 1024 + lane * 16, og = WS_OP + prod * 1024 + (2 * wave + (n16 >> 3)) * 256 + g4 * 64 + (lane & 7) * 8;
  asm volatile("" : "+v"(oa), "+v"(og));
  const float f0 = lds_ld<float>(L0 + L_F + ((wave >> 1) * 4 + (n16 >> 3)) * 4), f1 = lds_ld<float>(L0 + L_F + ((wave >> 1) * 4 + 2 + (n16 >> 3)) * 4);
#pragma unroll
  for (int j = 2 * (16 - VN1); j < 16; ++j) PV(j, 15 - j);
  TS(5);
  lds_st<f32x4>(LW + L_RB_O + lane * 16, acc[0] * f0);
  lds_st<f32x4>(LW + L_RB_O + 1024 + lane * 16, acc[1] * f1);
  flag_add(L0, F_S1, lane);
  if (wave < 2) {
    flag_wait<0>(L0, F_S1, 8u);
    f32x4 r[8];
    lds_ld8_b128<16384>(r, oa, oa + 65536);
    const f32x4 s = ((r[0] + r[1]) + (r[2] + r[3])) + ((r[4] + r[5]) + (r[6] + r[7]));
    __builtin_amdgcn_raw_buffer_store_b64(u32x2{nosent(pk(s[0], s[1])), nosent(pk(s[2], s[3]))}, rsb, og, 0, 16);
    flag_add(L0, F_S2, lane);
  }
  TS(6);
  if (role == 3) { TSEND(); return; }
  asm volatile(".Lfk_tb:" ::: "memory");
  const u32x4 sel = msk & ones;
  TRDEF();

  if (role == 0) {
    const int rk = (tk - 56) >> 3, cq = (tk - 56) & 7, kq = wave >> 1, bb = wave & 1;
    const int hd = 2 * (rk & 1) + (kq >> 1);
    const unsigned LK = L0 + kq * 32768;
    u32x4 A0, A1;
    const unsigned o0 = WS_OP + ((bb * 8 + (rk >> 1)) * 16 + (lane >> 3)) * 1024 + hd * 256 + (kq & 1) * 128 + (lane & 7) * 16, o1 = o0 + 8192;
    f32x4 xr0 = {0.f, 0.f, 0.f, 0.f}, xr1 = {0.f, 0.f, 0.f, 0.f};
    if (wave == 0 && rk == 0) {
#pragma unroll
      for (int j = 0; j < 4; ++j) {
        xr0[j] = bflo(*(const unsigned short*)(x + (cq * 64 + j * 16 + n16) * 2));
        xr1[j] = bflo(*(const unsigned short*)(x + (512 + cq * 64 + j * 16 + n16) * 2));
      }
    }
    flag_wait<0>(L0, F_W, 1u);
    const float wa = lds_ld<float>(L0 + L_W + ((bb * 4 + hd) * 16 + 2 * g4 + (n16 >> 3)) * 4), wb = lds_ld<float>(L0 + L_W + ((bb * 4 + hd) * 16 + 8 + 2 * g4 + (n16 >> 3)) * 4);
    u32x4 sa = msk & pk(wa, wa), sb = msk & pk(wb, wb);
    unsigned sw = LW + 8192 + n16 * 16 + (g4 & 1) * 8, sr = LW + 8192 + (n16 & 1) * 128 + g4 * 16, xw = L0 + L_X + wave * 256 + n16 * 16, xa = L0 + L_X + n16 * 16,
             og2 = WS_YP + (rk + (g4 ? 56 + 16 * g4 : 0)) * 2048 + cq * 256 + n16 * 16;
    const u64 vmask = __ballot((g4 >> 1) == (n16 >> 3));
    u32x4 S[6];
    asm volatile("" : "+v"(sw), "+v"(sr), "+v"(xw), "+v"(xa), "+v"(og2), "+v"(xr0), "+v"(xr1), "+v"(sa), "+v"(sb));
    u32x4 Bw[2][4];
#pragma unroll
    for (int t = 0; t < 2; ++t) {
      flag_wait<0>(L0, F_P0 + kq, 2u + t);
      if (t == 1)
        asm volatile("buffer_load_dwordx4 %0, %2, %4, 0 offen sc1\n\tbuffer_load_dwordx4 %1, %3, %4, 0 offen sc1"
                     : "=&v"(A0), "=&v"(A1) : "v"(o0), "v"(o1), "s"(rs) : "memory");
#pragma unroll
      for (int nt = 0; nt < 4; ++nt) {
        const unsigned a = LK + (32 * t + 8 * trq + 2 * g4) * 128 + ((nt ^ g4) * 32) + trp * 8;
        Bw[t][nt] = trfrag(a, a + 128);
      }
    }
    TS(7);
    asm volatile("s_waitcnt vmcnt(0)" : "+v"(A0), "+v"(A1)::"memory");
    {
      u32x4 A[2] = {A0, A1};
      u64 m[2] = {__ballot(pend4(A0)), __ballot(pend4(A1))};
      if (PIPE & 1) { if (m[0] | m[1]) ppoll2(rs, A, S, o0, 8192u, m); } else poll2(rs, A[0], A[1], o0, o1);
      A0 = A[0]; A1 = A[1];
    }
    TS(8);
    f32x4 d = mfma(sa, A0, f32x4{0.f, 0.f, 0.f, 0.f});
    d = mfma(sb, A1, d);
    asm volatile("s_mov_b64 exec, %2\n\tds_write_b64 %0, %1\n\ts_mov_b64 exec, -1" ::"v"(sw), "v"(u32x2{pk(d[0], d[1]), pk(d[2], d[3])}), "s"(vmask) : "memory");
    A0 = lds_ld<u32x4>(sr);
    A1 = lds_ld<u32x4>(sr + 64);
    f32x4 val;
#pragma unroll
    for (int nt = 0; nt < 4; ++nt) {
      f32x4 e = mfma(A0, Bw[0][nt], f32x4{0.f, 0.f, 0.f, 0.f});
      e = mfma(A1, Bw[1][nt], e);
      val[nt] = e[0] + e[1];
    }
    asm volatile("s_mov_b64 exec, 0xffff\n\tds_write_b128 %0, %1\n\ts_mov_b64 exec, -1" ::"v"(xw), "v"(val) : "memory");
    wg_barrier();
    if (wave == 0) {
      f32x4 r[8];
      lds_ld8_b128<256>(r, xa, xa + 1024);
      xr0 += (r[0] + r[2]) + (r[4] + r[6]);
      xr1 += (r[1] + r[3]) + (r[5] + r[7]);
      if (lane < 16 * HC)
        __builtin_amdgcn_raw_buffer_store_b128(u32x4{nosent(pk(xr0[0], xr0[1])), nosent(pk(xr0[2], xr0[3])), nosent(pk(xr1[0], xr1[1])), nosent(pk(xr1[2], xr1[3]))},
                                               rsb, og2, 0, 16);
      TS(9);
    }
    if (PIPE & 1) ppoll2_drain(S);
  } else if (role == 1) {
    u32x4 H[4];
    const int hc = tk & (HC - 1);
    const unsigned o = WS_YP + (g4 + (hc ? 56 + 16 * hc : 0)) * 2048 + wave * 256 + n16 * 16;
    flag_wait<1>(L0, F_ALL, 1u);
    flag_wait<1>(L0, F_P0 + (wave >> 1), 2u + ((wave ^ tk) & 1));
    issue4(rs, H, o, 8192u);
    u32x4 Bw[2][2][2];
#pragma unroll
    for (int mt = 0; mt < 2; ++mt)
#pragma unroll
      for (int t = 0; t < 2; ++t)
#pragma unroll
        for (int nt = 0; nt < 2; ++nt) {
          const unsigned a = LW + mt * 4096 + (16 * trq + 8 * t + 2 * g4 + (trq & 1)) * 64 + nt * 32 + trp * 8;
          Bw[mt][t][nt] = trfrag(a, a ^ 64);
        }
    unsigned xw = L0 + L_X + wave * 512 + lane * 8, xa = L0 + L_P + lane * 8;
    {
      f32x4 c[2][2];
#pragma unroll
      for (int nt = 0; nt < 2; ++nt)
#pragma unroll
        for (int mt = 0; mt < 2; ++mt) {
          c[nt][mt] = mfma(ones, Bw[mt][0][nt], f32x4{0.f, 0.f, 0.f, 0.f});
          c[nt][mt] = mfma(ones, Bw[mt][1][nt], c[nt][mt]);
        }
      lds_st<f32x2>(L0 + L_P + wave * 1024 + lane * 8, lane < 32 ? f32x2{c[0][0][0], c[0][1][0]} : f32x2{c[1][0][0], c[1][1][0]});
    }
    unsigned hw = LW + lane * 8, hr = LW + ((n16 >> 2) & 1) * 128 + g4 * 16, stw = L0 + L_X + 4096 + wave * 16 + g4 * 8, str = L0 + L_X + 4096 + (n16 & 7) * 16 + (g4 & 1) * 8,
             aw = LW + 256 + (g4 & 1) * 64 + (g4 >> 1) * 32 + n16 * 2, ar = LW + 256 + (n16 & 1) * 64 + g4 * 16, og2 = WS_YP + (16 + tk) * 2048 + wave * 256 + n16 * 16;
    asm volatile("" : "+v"(hw), "+v"(hr), "+v"(stw), "+v"(str), "+v"(aw), "+v"(ar), "+v"(og2), "+v"(xw), "+v"(xa));
    unsigned da = LW + 8192 + (8 * g4 + trq) * 128 + (g4 & 1) * 32 + trp * 8;
    unsigned db = da ^ 32;
    asm volatile("" : "+v"(da), "+v"(db));
    TS(7);
    land4(H);
    u32x4 S[12];
    {
      u64 m[4] = {__ballot(pend4(H[0])), __ballot(pend4(H[1])), __ballot(pend4(H[2])), __ballot(pend4(H[3]))};
      const unsigned oo[4] = {o, o + 8192u, o + 16384u, o + 24576u};
      if (PIPE & 2) { if (m[0] | m[1] | m[2] | m[3]) ppoll4(rs, H, S, o, 8192u, m); } else poll4(rs, H, oo);
    }
    TS(8);
    f32x4 h = mfma(sel, H[0], f32x4{0.f, 0.f, 0.f, 0.f}), h2 = mfma(sel, H[1], f32x4{0.f, 0.f, 0.f, 0.f});
    h = mfma(sel, H[2], h);
    h2 = mfma(sel, H[3], h2);
    h += h2;
    asm volatile("s_mov_b32 exec_hi, 0\n\tds_write_b64 %0, %1\n\ts_mov_b32 exec_hi, -1" ::"v"(hw), "v"(u32x2{pk(h[0], h[1]), pk(h[2], h[3])}) : "memory");
    const u32x4 A0 = lds_ld<u32x4>(hr), A1 = lds_ld<u32x4>(hr + 64);
    const float ss = row_sum16((h[0] + h[1]) + (h[2] + h[3])), gs = row_sum16((h[0] * h[0] + h[1] * h[1]) + (h[2] * h[2] + h[3] * h[3]));
    asm volatile("s_mov_b64 exec, 0x10001\n\tds_write_b64 %0, %1\n\ts_mov_b64 exec, -1" ::"v"(stw), "v"(f32x2{ss, gs}) : "memory");
    f32x4 e[2][2];
#pragma unroll
    for (int nt = 0; nt < 2; ++nt)
#pragma unroll
      for (int mt = 0; mt < 2; ++mt) {
        e[nt][mt] = mfma(A0, Bw[mt][0][nt], f32x4{0.f, 0.f, 0.f, 0.f});
        e[nt][mt] = mfma(A1, Bw[mt][1][nt], e[nt][mt]);
      }
    lds_st<f32x2>(xw, lane < 32 ? f32x2{e[0][0][0], e[0][1][0]} : f32x2{e[1][0][0], e[1][1][0]});
    wg_barrier();
    f32x2 st = lds_ld<f32x2>(str);
    f32x2 r[8];
    f32x2 cs[8];
    lds_ld16_b64(r, cs, xa);
    u32x4 Bd[4];
#pragma unroll
    for (int j = 0; j < 4; ++j) Bd[j] = trfrag(((j & 1) ? db : da) + (j >> 1) * 64, ((j & 1) ? db : da) + (j >> 1) * 64 + 512);
    __builtin_amdgcn_sched_barrier(0);
    const f32x2 csum = ((cs[0] + cs[1]) + (cs[2] + cs[3])) + ((cs[4] + cs[5]) + (cs[6] + cs[7]));
    st[0] += dpp<XOR1>(st[0]); st[1] += dpp<XOR1>(st[1]);
    st[0] += dpp<XOR2>(st[0]); st[1] += dpp<XOR2>(st[1]);
    st[0] += dpp<XOR7>(st[0]); st[1] += dpp<XOR7>(st[1]);
    const f32x2 gu = ((r[0] + r[1]) + (r[2] + r[3])) + ((r[4] + r[5]) + (r[6] + r[7]));
    const float mu = st[0] * (1.f / 512.f);
    const float rr = __builtin_amdgcn_rsqf(fmaxf(st[1] * (1.f / 512.f) - mu * mu, 0.f) + 1e-5f);
    const float gg = rr * (gu[0] - mu * csum[0]), uu = rr * (gu[1] - mu * csum[1]);
    const float a = gg * uu * __builtin_amdgcn_rcpf(1.f + __builtin_amdgcn_exp2f(-1.4426950408889634f * gg));
    lds_st<__bf16>(aw, (__bf16)a);
    const u32x4 Aa = lds_ld<u32x4>(ar);
    f32x4 y[4];
#pragma unroll
    for (int j = 0; j < 4; ++j) y[j] = mfma(Aa, Bd[j], f32x4{0.f, 0.f, 0.f, 0.f});
    if (lane < 16)
      __builtin_amdgcn_raw_buffer_store_b128(
          u32x4{nosent(pk(y[0][0], y[1][0])), nosent(pk(y[2][0], y[3][0])), nosent(pk(y[0][1], y[1][1])), nosent(pk(y[2][1], y[3][1]))}, rsb, og2, 0, 16);
    TS(9);
    if (PIPE & 2) ppoll4_drain(S);
  } else if (wave == 2) {
    const int ee = tk - 184, hc = ee & (HC - 1);
    u32x4 Y0 = {SENT, SENT, SENT, SENT}, Y[4];
    const unsigned ob = WS_YP + (lane >> 2) * 2048 + ee * 64 + (lane & 3) * 16;
#pragma unroll
    for (int i = 0; i < 4; ++i) {
      Y[i] = (i < 3 || lane < 32) ? u32x4{SENT, SENT, SENT, SENT} : u32x4{0u, 0u, 0u, 0u};
    }
    flag_wait<1>(L0, F_ALL, 1u);
#pragma nounroll
    for (int spin = 0; spin < SPIN_G; ++spin) {
      const u64 m = __ballot(pend4(Y0));
      if (m == 0) break;
      __builtin_amdgcn_s_sleep(4);
      poll_round1(rs, Y0, ob + (hc ? 56 + 16 * hc : 0) * 2048, m);
    }
    f32x4 d0 = mfma(sel, Y0, f32x4{0.f, 0.f, 0.f, 0.f});
    TS(7);
    u32x4 S[12];
    {
      u64 m[4] = {__ballot(pend4(Y[0])), __ballot(pend4(Y[1])), __ballot(pend4(Y[2])), __ballot(pend4(Y[3]))};
      const unsigned oo[4] = {ob + 32768u, ob + 65536u, ob + 98304u, ob + 131072u};
      if (EDLY) __builtin_amdgcn_s_sleep(EDLY);
      if (PIPE & 4) { if (m[0] | m[1] | m[2] | m[3]) ppoll4(rs, Y, S, ob + 32768u, 32768u, m); } else poll4(rs, Y, oo);
    }
    TS(8);
    f32x4 d1 = mfma(sel, Y[1], f32x4{0.f, 0.f, 0.f, 0.f});
    d0 = mfma(sel, Y[0], d0);
    d1 = mfma(sel, Y[3], d1);
    d0 = mfma(sel, Y[2], d0);
    f32x4 d = d0 + d1;
#pragma unroll
    for (int r = 0; r < 4; ++r) { d[r] += dpp<ROR4>(d[r]); d[r] += dpp<ROR8>(d[r]); }
    const float lo = (n16 & 4) ? d[1] : d[0], hi = (n16 & 4) ? d[3] : d[2];
    const float res = (n16 & 8) ? hi : lo;
    const float nb = dpp<XOR1>(res);
    if (lane < 32 && (lane & 1) == 0)
      *(unsigned*)(out + g4 * 512 + 64 * (ee >> 2) + 16 * (n16 >> 2) + 4 * (ee & 3) + (n16 & 3)) = pk(res, nb);
    TS(9);
    if (PIPE & 4) ppoll4_drain(S);
  }
  asm volatile(".Lfk_te:" ::: "memory");
  TSEND();
}
}

extern "C" void kernel_launch(void* const* d_in, const int* in_sizes, int n_in, void* d_out, int out_size, void* d_ws, size_t ws_size,
                              hipStream_t stream) {
  (void)in_sizes; (void)n_in; (void)out_size; (void)ws_size;
  (void)hipFuncSetAttribute((const void*)fk::fused_kernel, hipFuncAttributeMaxDynamicSharedMemorySize, fk::L_END);
  fk::fused_kernel<<<fk::NWG, fk::NTHR, fk::L_END, stream>>>(
      (unsigned char*)d_ws, (const unsigned char*)d_in[0], (const unsigned char*)d_in[3], (const unsigned char*)d_in[1],
      (const unsigned char*)d_in[2], (const unsigned char*)d_in[4], (const unsigned char*)d_in[6], (const unsigned char*)d_in[7],
      (const unsigned char*)d_in[8], (unsigned short*)d_out);
}
```

```cpp
#include <hip/hip_runtime.h>
#include <hip/hip_bf16.h>
#include <stdint.h>

namespace fk {
typedef unsigned u32x4 __attribute__((ext_vector_type(4)));
typedef unsigned u32x2 __attribute__((ext_vector_type(2)));
typedef float f32x4 __attribute__((ext_vector_type(4)));
typedef float f32x2 __attribute__((ext_vector_type(2)));
typedef short s16x4 __attribute__((ext_vector_type(4)));
typedef __bf16 bf16x8 __attribute__((ext_vector_type(8)));
typedef __bf16 bf16x2 __attribute__((ext_vector_type(2)));
typedef unsigned long long u64;
#define LAS __attribute__((address_space(3)))
#define GAS __attribute__((address_space(1)))
#define RLX_AGENT __ATOMIC_RELAXED, __HIP_MEMORY_SCOPE_AGENT

#define FK_TS 0
#define KW_N 6
#define APF 1
#define APM 7
#define K0 1
#define ST_AT 4u
#define QDELAY 40
constexpr int VN1 = 12;
constexpr unsigned SENT = 0xFFFFFFFFu;
constexpr int NWG = 256, NWAVES = 13, NTHR = NWAVES * 64;
constexpr int WS_EPOCH = 0;
constexpr int WS_FL = 4096;
constexpr int WS_QP = 8192;
constexpr int WS_ST = WS_QP + 131072;
constexpr int WS_OP = WS_ST + 8192;
constexpr int WS_YP = WS_OP + 262144;
constexpr int WS_END = WS_YP + 72 * 2048;
constexpr int WS_TS = 1 << 20;
constexpr int L_RB_O = 12288;
constexpr int L_WQ = 131072, L_P = L_WQ  , L_X = L_WQ + 8192  , L_Q = L_WQ + 16384, L_WM = L_Q + 1024, L_WL = L_WM + 64, L_F = L_WL + 64,
              L_W = L_F + 64, L_FLAGS = L_W + 512, L_SCR = L_FLAGS + 64, L_XS = L_SCR + 256, L_END = L_XS + 2048;
enum { F_Q = 0, F_STAT = 1, F_FAC = 2, F_VISS = 3, F_WQ = 4, F_KISS = 5, F_S1 = 6, F_S2 = 7, F_ALL = 9, F_W = 10, F_P0 = 12   };
constexpr int SPIN_G = 1 << 18;
constexpr int SPIN_L = 1 << 22;
constexpr float QSCALE = 0.088388347648318440f * 1.4426950408889634f;
constexpr unsigned ONE2 = 0x3F803F80u;

__device__ __forceinline__ f32x4 mfma(u32x4 a, u32x4 b, f32x4 c) {
  return __builtin_amdgcn_mfma_f32_16x16x32_bf16(__builtin_bit_cast(bf16x8, a), __builtin_bit_cast(bf16x8, b), c, 0, 0, 0);
}
__device__ __forceinline__ unsigned pk(float lo, float hi) { bf16x2 v = {(__bf16)lo, (__bf16)hi}; return __builtin_bit_cast(unsigned, v); }
__device__ __forceinline__ unsigned nosent(unsigned x) { return x < 0xFFFFFFFEu ? x : 0xFFFFFFFEu; }
__device__ __forceinline__ float bflo(unsigned u) { return __builtin_bit_cast(float, u << 16); }
__device__ __forceinline__ float bfhi(unsigned u) { return __builtin_bit_cast(float, u & 0xffff0000u); }
__device__ __forceinline__ float x16_sum(float x) {
  auto s = __builtin_amdgcn_permlane16_swap(__float_as_uint(x), __float_as_uint(x), false, false);
  return __uint_as_float(s[0]) + __uint_as_float(s[1]);
}
__device__ __forceinline__ float x32_sum(float x) {
  auto s = __builtin_amdgcn_permlane32_swap(__float_as_uint(x), __float_as_uint(x), false, false);
  return __uint_as_float(s[0]) + __uint_as_float(s[1]);
}
__device__ __forceinline__ float x16_max(float x) {
  auto s = __builtin_amdgcn_permlane16_swap(__float_as_uint(x), __float_as_uint(x), false, false);
  return fmaxf(__uint_as_float(s[0]), __uint_as_float(s[1]));
}
__device__ __forceinline__ float x32_max(float x) {
  auto s = __builtin_amdgcn_permlane32_swap(__float_as_uint(x), __float_as_uint(x), false, false);
  return fmaxf(__uint_as_float(s[0]), __uint_as_float(s[1]));
}
template <int CTRL> __device__ __forceinline__ float dpp(float x) {
  return __builtin_bit_cast(float, __builtin_amdgcn_mov_dpp(__builtin_bit_cast(int, x), CTRL, 0xf, 0xf, true));
}
constexpr int XOR1 = 0xB1, XOR2 = 0x4E, XOR7 = 0x141, ROR4 = 0x124, ROR8 = 0x128;
__device__ __forceinline__ float row_sum16(float x) { x += dpp<XOR1>(x); x += dpp<XOR2>(x); x += dpp<XOR7>(x); x += dpp<ROR8>(x); return x; }
__device__ __forceinline__ float wave_sum(float x) { return x32_sum(x16_sum(row_sum16(x))); }

template <int N> __device__ __forceinline__ void waitvm() { asm volatile("s_waitcnt vmcnt(%0)" ::"i"(N) : "memory"); }
__device__ __forceinline__ void dma_nt(const void* sbase, unsigned voff, unsigned ldst) {
  asm volatile("s_mov_b32 m0, %2\n\ts_nop 0\n\tglobal_load_lds_dwordx4 %0, %1 nt" ::"v"(voff), "s"(sbase), "s"(ldst) : "memory");
}
template <int W> __device__ __forceinline__ void dma4_nt(const void* sbase, const unsigned (&v)[4], unsigned ldst) {
  if constexpr (W == 0)
    asm volatile("s_mov_b32 m0, %5\n\ts_nop 0\n\tglobal_load_lds_dwordx4 %0, %4 nt\n\tglobal_load_lds_dwordx4 %1, %4 offset:1024 nt\n\t"
                 "global_load_lds_dwordx4 %2, %4 offset:2048 nt\n\tglobal_load_lds_dwordx4 %3, %4 offset:3072 nt"
                 ::"v"(v[0]), "v"(v[1]), "v"(v[2]), "v"(v[3]), "s"(sbase), "s"(ldst) : "memory");
  else
    asm volatile("s_mov_b32 m0, %5\n\ts_waitcnt vmcnt(%6)\n\tglobal_load_lds_dwordx4 %0, %4 nt\n\ts_waitcnt vmcnt(%6)\n\tglobal_load_lds_dwordx4 %1, %4 offset:1024 nt\n\t"
                 "s_waitcnt vmcnt(%6)\n\tglobal_load_lds_dwordx4 %2, %4 offset:2048 nt\n\ts_waitcnt vmcnt(%6)\n\tglobal_load_lds_dwordx4 %3, %4 offset:3072 nt"
                 ::"v"(v[0]), "v"(v[1]), "v"(v[2]), "v"(v[3]), "s"(sbase), "s"(ldst), "i"(W - 1) : "memory");
}
__device__ __forceinline__ void touch(const void* sbase, unsigned voff, unsigned ldst) {
  asm volatile("s_mov_b32 m0, %2\n\ts_nop 0\n\tglobal_load_lds_dword %0, %1" ::"v"(voff), "s"(sbase), "s"(ldst) : "memory");
}
template <int OFF> __device__ __forceinline__ void ldv_nt(u32x4& dst, const void* sbase, unsigned voff) {
  asm volatile("global_load_dwordx4 %0, %1, %2 offset:%3 nt" : "=v"(dst) : "v"(voff), "s"(sbase), "i"(OFF) : "memory");
}
__device__ __forceinline__ void ldv(u32x4& dst, const void* sbase, unsigned voff) {
  asm volatile("global_load_dwordx4 %0, %1, %2" : "=v"(dst) : "v"(voff), "s"(sbase) : "memory");
}
__device__ __forceinline__ u32x2 trread(unsigned addr) {
  return __builtin_bit_cast(u32x2, __builtin_amdgcn_ds_read_tr16_b64_v4i16((LAS s16x4*)(uintptr_t)addr));
}
__device__ __forceinline__ u32x4 trfrag(unsigned a0, unsigned a1) { u32x2 lo = trread(a0), hi = trread(a1); return u32x4{lo[0], lo[1], hi[0], hi[1]}; }
template <typename T> __device__ __forceinline__ T lds_ld(unsigned addr) { return *(LAS T*)(uintptr_t)addr; }
template <typename T> __device__ __forceinline__ void lds_st(unsigned addr, T v) { *(LAS T*)(uintptr_t)addr = v; }
__device__ __forceinline__ void flag_set(unsigned base, int f, unsigned v) {
  asm volatile("s_mov_b64 exec, 1\n\tds_write_b32 %0, %1\n\ts_mov_b64 exec, -1" ::"v"(base + L_FLAGS + f * 4), "v"(v) : "memory");
}
__device__ __forceinline__ void flag_add(unsigned base, int f) {
  asm volatile("s_mov_b64 exec, 1\n\tds_add_u32 %0, %1\n\ts_mov_b64 exec, -1" ::"v"(base + L_FLAGS + f * 4), "v"(1u) : "memory");
}
__device__ __forceinline__ void wg_barrier() { asm volatile("s_waitcnt lgkmcnt(0)\n\ts_barrier" ::: "memory"); }
__device__ __forceinline__ void lds_ld16_b64(f32x2 (&r)[8], f32x2 (&c)[8], unsigned a) {
  asm volatile("ds_read_b64 %0, %16 offset:8192\n\tds_read_b64 %1, %16 offset:8704\n\tds_read_b64 %2, %16 offset:9216\n\tds_read_b64 %3, %16 offset:9728\n\t"
               "ds_read_b64 %4, %16 offset:10240\n\tds_read_b64 %5, %16 offset:10752\n\tds_read_b64 %6, %16 offset:11264\n\tds_read_b64 %7, %16 offset:11776\n\t"
               "ds_read_b64 %8, %16\n\tds_read_b64 %9, %16 offset:1024\n\tds_read_b64 %10, %16 offset:2048\n\tds_read_b64 %11, %16 offset:3072\n\t"
               "ds_read_b64 %12, %16 offset:4096\n\tds_read_b64 %13, %16 offset:5120\n\tds_read_b64 %14, %16 offset:6144\n\tds_read_b64 %15, %16 offset:7168\n\t"
               "s_waitcnt lgkmcnt(0)"
               : "=&v"(r[0]), "=&v"(r[1]), "=&v"(r[2]), "=&v"(r[3]), "=&v"(r[4]), "=&v"(r[5]), "=&v"(r[6]), "=&v"(r[7]),
                 "=&v"(c[0]), "=&v"(c[1]), "=&v"(c[2]), "=&v"(c[3]), "=&v"(c[4]), "=&v"(c[5]), "=&v"(c[6]), "=&v"(c[7])
               : "v"(a) : "memory");
}
template <int ST> __device__ __forceinline__ void lds_ld8_b128(f32x4 (&r)[8], unsigned a, unsigned b) {
  asm volatile("ds_read_b128 %0, %8\n\tds_read_b128 %1, %8 offset:%10\n\tds_read_b128 %2, %8 offset:%11\n\tds_read_b128 %3, %8 offset:%12\n\t"
               "ds_read_b128 %4, %9\n\tds_read_b128 %5, %9 offset:%10\n\tds_read_b128 %6, %9 offset:%11\n\tds_read_b128 %7, %9 offset:%12\n\t"
               "s_waitcnt lgkmcnt(0)"
               : "=&v"(r[0]), "=&v"(r[1]), "=&v"(r[2]), "=&v"(r[3]), "=&v"(r[4]), "=&v"(r[5]), "=&v"(r[6]), "=&v"(r[7])
               : "v"(a), "v"(b), "i"(ST), "i"(2 * ST), "i"(3 * ST) : "memory");
}
template <int SLP> __device__ __forceinline__ void flag_wait(unsigned base, int f, unsigned v) {
  asm volatile("" ::: "memory");
#pragma nounroll
  for (int i = 0; i < SPIN_L; ++i) {
    unsigned x = *(volatile LAS unsigned*)(uintptr_t)(base + L_FLAGS + f * 4);
    if ((unsigned)__builtin_amdgcn_readfirstlane(x) >= v) break;
    if (SLP) __builtin_amdgcn_s_sleep(SLP);
  }
  asm volatile("" ::: "memory");
}
__device__ __forceinline__ bool pend4(u32x4 v) {
  unsigned a = v[0] > v[1] ? v[0] : v[1], b = v[2] > v[3] ? v[2] : v[3];
  return (a > b ? a : b) == SENT;
}
__device__ __forceinline__ void poll_round1(u32x4 rs, u32x4& v0, unsigned o0, u64 m0) {
  asm volatile("s_mov_b64 exec, %3\n\tbuffer_load_dwordx4 %0, %1, %2, 0 offen sc1\n\t"
               "s_mov_b64 exec, -1\n\ts_waitcnt vmcnt(0)"
               : "+v"(v0) : "v"(o0), "s"(rs), "s"(m0) : "memory");
}
__device__ __forceinline__ void poll_round8(u32x4 rs, u32x4 (&v)[8], unsigned o, const u64 (&m)[8]) {
  asm volatile("s_mov_b64 exec, %10\n\tbuffer_load_dwordx4 %0, %8, %9, 0 offen sc1\n\t"
               "s_mov_b64 exec, %11\n\tbuffer_load_dwordx4 %1, %8, %9, 0 offen offset:1024 sc1\n\t"
               "s_mov_b64 exec, %12\n\tbuffer_load_dwordx4 %2, %8, %9, 0 offen offset:2048 sc1\n\t"
               "s_mov_b64 exec, %13\n\tbuffer_load_dwordx4 %3, %8, %9, 0 offen offset:3072 sc1\n\t"
               "s_mov_b64 exec, %14\n\tbuffer_load_dwordx4 %4, %8, %9, %18 offen sc1\n\t"
               "s_mov_b64 exec, %15\n\tbuffer_load_dwordx4 %5, %8, %9, %18 offen offset:1024 sc1\n\t"
               "s_mov_b64 exec, %16\n\tbuffer_load_dwordx4 %6, %8, %9, %18 offen offset:2048 sc1\n\t"
               "s_mov_b64 exec, %17\n\tbuffer_load_dwordx4 %7, %8, %9, %18 offen offset:3072 sc1\n\t"
               "s_mov_b64 exec, -1\n\ts_waitcnt vmcnt(0)"
               : "+v"(v[0]), "+v"(v[1]), "+v"(v[2]), "+v"(v[3]), "+v"(v[4]), "+v"(v[5]), "+v"(v[6]), "+v"(v[7])
               : "v"(o), "s"(rs), "s"(m[0]), "s"(m[1]), "s"(m[2]), "s"(m[3]), "s"(m[4]), "s"(m[5]), "s"(m[6]), "s"(m[7]), "s"(4096u) : "memory");
}
__device__ __forceinline__ void issue4(u32x4 rs, u32x4 (&v)[4], unsigned o, unsigned st) {
  asm volatile("buffer_load_dwordx4 %0, %4, %5, 0 offen sc1\n\tbuffer_load_dwordx4 %1, %4, %5, %6 offen sc1\n\t"
               "buffer_load_dwordx4 %2, %4, %5, %7 offen sc1\n\tbuffer_load_dwordx4 %3, %4, %5, %8 offen sc1"
               : "=&v"(v[0]), "=&v"(v[1]), "=&v"(v[2]), "=&v"(v[3]) : "v"(o), "s"(rs), "s"(st), "s"(2 * st), "s"(3 * st) : "memory");
}
__device__ __forceinline__ void land4(u32x4 (&v)[4]) {
  asm volatile("s_waitcnt vmcnt(0)" : "+v"(v[0]), "+v"(v[1]), "+v"(v[2]), "+v"(v[3])::"memory");
}
__device__ __forceinline__ void poll2(u32x4 rs, u32x4& A0, u32x4& A1, unsigned o, unsigned st) {
#pragma nounroll
  for (int spin = 0; spin < SPIN_G; ++spin) {
    const u64 m0 = __ballot(pend4(A0)), m1 = __ballot(pend4(A1));
    if ((m0 | m1) == 0) break;
    asm volatile("s_mov_b64 exec, %5\n\tbuffer_load_dwordx4 %0, %2, %3, 0 offen sc1\n\t"
                 "s_mov_b64 exec, %6\n\tbuffer_load_dwordx4 %1, %2, %3, %4 offen sc1\n\t"
                 "s_mov_b64 exec, -1\n\ts_waitcnt vmcnt(0)"
                 : "+v"(A0), "+v"(A1) : "v"(o), "s"(rs), "s"(st), "s"(m0), "s"(m1) : "memory");
  }
}
__device__ __forceinline__ void poll4(u32x4 rs, u32x4 (&A)[4], unsigned o, unsigned st) {
#pragma nounroll
  for (int spin = 0; spin < SPIN_G; ++spin) {
    u64 m[4];
#pragma unroll
    for (int i = 0; i < 4; ++i) m[i] = __ballot(pend4(A[i]));
    if ((m[0] | m[1] | m[2] | m[3]) == 0) break;
    asm volatile("s_mov_b64 exec, %9\n\tbuffer_load_dwordx4 %0, %4, %5, 0 offen sc1\n\t"
                 "s_mov_b64 exec, %10\n\tbuffer_load_dwordx4 %1, %4, %5, %6 offen sc1\n\t"
                 "s_mov_b64 exec, %11\n\tbuffer_load_dwordx4 %2, %4, %5, %7 offen sc1\n\t"
                 "s_mov_b64 exec, %12\n\tbuffer_load_dwordx4 %3, %4, %5, %8 offen sc1\n\t"
                 "s_mov_b64 exec, -1\n\ts_waitcnt vmcnt(0)"
                 : "+v"(A[0]), "+v"(A[1]), "+v"(A[2]), "+v"(A[3])
                 : "v"(o), "s"(rs), "s"(st), "s"(2 * st), "s"(3 * st), "s"(m[0]), "s"(m[1]), "s"(m[2]), "s"(m[3]) : "memory");
  }
}
__device__ __forceinline__ void poll1(u32x4 rs, u32x4& A, unsigned o) {
#pragma nounroll
  for (int spin = 0; spin < SPIN_G; ++spin) {
    const u64 m = __ballot(pend4(A));
    if (m == 0) break;
    poll_round1(rs, A, o, m);
  }
}

#define TS(k) do {} while (0)
#define TSEND() do {} while (0)

__global__ void __launch_bounds__(NTHR, 4)
fused_kernel(const unsigned char* wq_, const unsigned char* kc_, unsigned char* ws_, const unsigned char* x_,
             const unsigned char* __restrict__ vc, const unsigned char* __restrict__ wo, const unsigned char* __restrict__ wg,
             const unsigned char* __restrict__ wu, const unsigned char* __restrict__ wd, unsigned short* __restrict__ out) {
  typedef unsigned long long u64x4 __attribute__((ext_vector_type(4)));
  u64x4 ka;
  asm volatile(".Lfk_start:\n\ts_load_dwordx8 %0, %1, 0x0" : "=s"(ka) : "s"(__builtin_amdgcn_kernarg_segment_ptr()) : "memory");
  extern __shared__ __attribute__((aligned(16))) unsigned char smem[];
  const unsigned L0 = (unsigned)(uintptr_t)(LAS unsigned char*)smem;
  const int tid = threadIdx.x, lane = tid & 63, wave0 = __builtin_amdgcn_readfirstlane(tid >> 6);
  const int w = blockIdx.x, kvh = w & 7, idx = w >> 3, bat = idx >> 4, spl = idx & 15;
  const int n16 = lane & 15, g4 = lane >> 4;
  const int rq = idx & 3, cbk = idx >> 2;
  const size_t chunk = ((size_t)(bat * 8 + kvh) * 8192 + spl * 512) * 256;
  const int kw = wave0 - 1;
  const bool isk = (unsigned)kw < 4u;
  if ((idx & APM) == 0 && wave0 >= 10) {
    u64 pc;
    int dlt, len;
    asm volatile("s_getpc_b64 %0\n\t.Lfk_h2:\n\ts_mov_b32 %1, .Lfk_start-.Lfk_h2\n\ts_mov_b32 %2, .Lfk_te-.Lfk_start\n\t"
                 "s_max_i32 %2, %2, .Lfk_ke-.Lfk_start\n\ts_max_i32 %2, %2, .Lfk_ce-.Lfk_start\n\ts_max_i32 %2, %2, .Lfk_e0-.Lfk_start\n\t"
                 "s_max_i32 %2, %2, .Lfk_e1-.Lfk_start\n\ts_max_i32 %2, %2, .Lfk_e2-.Lfk_start" : "=s"(pc), "=s"(dlt), "=s"(len)::"scc");
    const int o = ((wave0 - 10) * 64 + lane) * 128;
    if (o + 4 <= len) touch((const void*)(pc + (long long)dlt), o, L0 + L_SCR);
  }
  asm volatile("s_waitcnt lgkmcnt(0)" : "+s"(ka)::"memory");
  const unsigned char* wq = (const unsigned char*)ka[0];
  const unsigned char* kc = (const unsigned char*)ka[1];
  unsigned char* ws = (unsigned char*)ka[2];
  const unsigned char* x = (const unsigned char*)ka[3];
  const unsigned LK = L0 + kw * 32768;
  const unsigned char* kb = kc + chunk + kw * 8192;
#define KVODEF()                  \
  int lane_k = lane;              \
  asm volatile("" : "+v"(lane_k)); \
  unsigned kvo[4];                \
  _Pragma("unroll") for (int a = 0; a < 4; ++a) kvo[a] = (lane_k >> 4) * 256 + (((lane_k & 15) ^ (4 * (lane_k >> 4) + a)) * 16)
  if (isk) {
    const unsigned v0 = (lane >> 3) * 8192 + (lane & 7) * 16;
    const unsigned wv[4] = {v0, (v0 ^ 32) + 64512, v0 + 2 * 64512, (v0 ^ 32) + 3 * 64512};
    dma4_nt<0>(wq + (size_t)(rq * 128 + kw * 32) * 8192 + (kvh * 512 + cbk * 64) * 2, wv, L0 + L_WQ + kw * 4096);
    KVODEF();
    if (K0 >= 1) dma4_nt<0>(kb + rq * 32768, kvo, LK + rq * 8192);
    if (K0 == 2) dma4_nt<0>(kb + rq * 32768 + 4096, kvo, LK + rq * 8192 + 4096);
  }
  const int prod = (bat * 8 + kvh) * 16 + spl;
  const u32x4 rs = {(unsigned)(uintptr_t)ws, (unsigned)((uintptr_t)ws >> 32) & 0xffffu, (unsigned)WS_END, 0x00020000u};
  unsigned tlo = 0, thi = 0;
  if (wave0 == 0) {
    __builtin_amdgcn_s_setprio(3);
    u32x2 ep;
    asm volatile("global_load_dwordx2 %0, %1, %2 sc1" : "=v"(ep) : "v"(0u), "s"(ws) : "memory");
    asm volatile("s_mov_b32 m0, %2\n\ts_nop 0\n\tglobal_load_lds_dwordx4 %0, %1\n\tglobal_load_lds_dwordx4 %0, %1 offset:1024" ::"v"(lane * 16), "s"(x), "s"(L0 + L_XS) : "memory");
    if (lane < 16) lds_st<unsigned>(L0 + L_FLAGS + lane * 4, 0u);
    asm volatile("s_waitcnt lgkmcnt(0)\n\ts_barrier" ::: "memory");
    const u32x4 s4 = {SENT, SENT, SENT, SENT};
    const int OOB = 0x7ffffff0;
    int o1 = OOB;
    if (lane < 2) o1 = WS_ST + (kvh * 32 + idx) * 32 + lane * 16;
    const int o2 = (idx >= 16 && idx < 25) ? WS_YP + (kvh * 9 + idx - 16) * 2048 + lane * 16 : OOB;
    asm volatile("buffer_store_dwordx4 %1, %2, %5, 0 offen sc1\n\t"
                 "buffer_store_dwordx4 %1, %3, %5, 0 offen sc1\n\t"
                 "buffer_store_dwordx4 %1, %4, %5, 0 offen sc1\n\t"
                 "buffer_store_dwordx4 %1, %4, %5, 0 offen offset:1024 sc1\n\ts_waitcnt vmcnt(4)"
                 : "+v"(ep) : "v"(s4), "v"(WS_OP + prod * 1024 + lane * 16), "v"(o1), "v"(o2), "s"(rs) : "memory");
    const u64 tag = (((u64)ep[1] << 32) | ep[0]) + 1;
    tlo = __builtin_amdgcn_readfirstlane((unsigned)tag);
    thi = __builtin_amdgcn_readfirstlane((unsigned)(tag >> 32));
  } else {
    asm volatile("s_barrier" ::: "memory");
  }
  const int nC = 8 - 2 * (kvh >> 2), cc0 = 8 * kvh - 2 * (kvh >> 2) * (kvh - 4), ri = idx - nC;
  const int role = (ri < 0) + 2 * (ri >= 16) + (ri >= 20);
  const int tk = cc0 + idx;
  const int rk = 2 * kvh + (ri >> 3), cq = ri & 7;
  const __amdgpu_buffer_rsrc_t rsb = __builtin_amdgcn_make_buffer_rsrc(ws, 0, WS_END, 0x00020000);
#define TRDEF()                   \
  int lane_ = lane;               \
  asm volatile("" : "+v"(lane_)); \
  const unsigned trq = (lane_ >> 2) & 3, trp = lane_ & 3
  const u32x4 ones = {ONE2, ONE2, ONE2, ONE2};

  if (isk) {
    __builtin_amdgcn_s_setprio(2);
    KVODEF();
    waitvm<4 * K0>();
    flag_add(L0, F_WQ);
    if (role == 1) {
#pragma nounroll
      for (int t = K0; t < 8; ++t) {
        const int tl = (t + 2 * rq) & 7;
        dma4_nt<8>(kb + (tl >> 1) * 32768 + (tl & 1) * 4096, kvo, LK + tl * 4096);
      }
    } else {
#pragma nounroll
      for (int t = K0; t < 8; ++t) {
        const int tl = (t + 2 * rq) & 7;
        dma4_nt<KW_N>(kb + (tl >> 1) * 32768 + (tl & 1) * 4096, kvo, LK + tl * 4096);
      }
    }
    flag_add(L0, F_KISS);
    TS(1);
    flag_wait<1>(L0, F_Q, 1u);
    TS(2);
    u32x4 qf[4];
#pragma unroll
    for (int t = 0; t < 4; ++t) qf[t] = lds_ld<u32x4>(L0 + L_Q + (n16 & 3) * 256 + t * 64 + g4 * 16);
    f32x4 sel[2];
    const unsigned krow = LK + (4 * (n16 & 3) + (n16 >> 2)) * 256;
#pragma unroll
    for (int hf = 0; hf < 2; ++hf) {
      if (hf == 1) waitvm<0>();
      f32x4 S[4];
#pragma unroll
      for (int T = 0; T < 4; ++T) {
        S[T] = f32x4{0.f, 0.f, 0.f, 0.f};
#pragma unroll
        for (int t = 0; t < 4; ++t) S[T] = mfma(lds_ld<u32x4>(krow + ((((2 * hf + (T >> 1) + rq) & 3) * 2 + (T & 1)) * 4096) + (((4 * t + g4) ^ n16) * 16)), qf[t], S[T]);
      }
      const f32x4 lo = (n16 & 4) ? S[1] : S[0], hi = (n16 & 4) ? S[3] : S[2];
      sel[hf] = (n16 & 8) ? hi : lo;
    }
    float m = fmaxf(fmaxf(fmaxf(sel[0][0], sel[0][1]), fmaxf(sel[0][2], sel[0][3])), fmaxf(fmaxf(sel[1][0], sel[1][1]), fmaxf(sel[1][2], sel[1][3])));
    m = fmaxf(m, dpp<ROR4>(m)); m = fmaxf(m, dpp<ROR8>(m));
    m = x32_max(x16_max(m));
    const float ms = m * QSCALE;
    float l = 0.f;
#pragma unroll
    for (int hf = 0; hf < 2; ++hf) {
      u32x4 pp;
#pragma unroll
      for (int r = 0; r < 4; ++r) {
        const float p = __builtin_amdgcn_exp2f(fmaf(sel[hf][r], QSCALE, -ms));
        pp[r] = pk(p, p);
        l += bfhi(pp[r]);
      }
      lds_st<u32x4>(L0 + L_P + (2 * kw + ((n16 >> 2) & 1)) * 1024 + (((n16 & 3) * 4 + g4) * 16 + 4 * (2 * hf + (n16 >> 3))) * 4, pp);
    }
    flag_set(L0, F_P0 + kw, 1u);
    l += dpp<ROR4>(l); l += dpp<ROR8>(l);
    l = x32_sum(x16_sum(l));
    if (lane < 4) {
      lds_st<float>(L0 + L_WM + (kw * 4 + lane) * 4, ms);
      lds_st<float>(L0 + L_WL + (kw * 4 + lane) * 4, l);
    }
    flag_add(L0, F_STAT);
    TS(3);
    __builtin_amdgcn_s_setprio(0);
    if (role == 0) {
      const unsigned char* base = wo + (size_t)(rk * 256 + kw * 64) * 1024 + cq * 128;
      const unsigned v0 = (lane >> 3) * 1024 + (((lane & 7) ^ (g4 << 1)) * 16);
      const unsigned wv[4] = {v0, v0 + 7168, v0 + 2 * 7168, v0 + 3 * 7168};
      flag_wait<1>(L0, F_VISS, 8u);
      dma4_nt<0>(base, wv, LK);
      dma4_nt<0>(base + 32768, wv, LK + 4096);
      TS(4);
      waitvm<4>();
      flag_set(L0, F_P0 + kw, 2u);
      waitvm<0>();
      flag_set(L0, F_P0 + kw, 3u);
      TS(5);
    } else if (role == 1) {
      const unsigned va = (lane >> 2) * 3584u + (lane & 3) * 16u, vbb = ((lane >> 2) ^ 1) * 3584u + (lane & 3) * 16u;
      const unsigned wv[4] = {va, vbb + 56320, va + 2 * 56320, vbb + 3 * 56320};
      const unsigned v2 = (lane >> 3) * 1024 + (lane & 7) * 16;
      const unsigned dv[4] = {v2, (v2 ^ 32) + 7168, v2 + 2 * 7168, (v2 ^ 32) + 3 * 7168};
      const size_t go = (size_t)kw * 128 * 3584 + tk * 64;
      const unsigned char* bd = wd + (size_t)tk * 32 * 1024 + kw * 256;
      flag_wait<1>(L0, F_VISS, 8u);
#pragma nounroll
      for (int j = 0; j < 2; ++j) {
        const int hh = (j ^ tk) & 1;
        dma4_nt<0>(wg + go + hh * 229376, wv, LK + hh * 16384);
        dma4_nt<0>(wu + go + hh * 229376, wv, LK + hh * 16384 + 4096);
      }
      dma4_nt<0>(bd, dv, LK + 8192);
      dma4_nt<0>(bd + 128, dv, LK + 16384 + 8192);
      TS(4);
      waitvm<16>();
      flag_set(L0, F_P0 + kw, 2u);
      waitvm<8>();
      flag_set(L0, F_P0 + kw, 3u);
      TS(5);
      waitvm<0>();
      TS(6);
    } else {
      const int nh = (16 - nC) * 4;
      flag_wait<1>(L0, F_S2, 2u);
#pragma nounroll
      for (int t = (ri - 16) * 4 + kw; t < 4 * nC + 64; t += nh) {
        if (t < 4 * nC) {
          touch(wd + (size_t)cc0 * 32768 + t * 8192, lane * 128, L0 + L_SCR);
        } else {
          const int u = t - 4 * nC, row = ((u >> 2) & 3) * 128 + ((u & 1) * 4 + 3 - (u >> 4)) * 16;
          if (2 * (lane & 3) < nC) touch(((u & 2) ? wu : wg) + (size_t)row * 3584 + cc0 * 64, (lane >> 2) * 3584 + (lane & 3) * 128, L0 + L_SCR);
        }
      }
      TS(4);
      waitvm<0>();
      TS(7);
    }
    asm volatile(".Lfk_ke:" ::: "memory");
    TSEND();
    return;
  }

  if (wave0 == 0) {
    const u32x4 xs0 = lds_ld<u32x4>(L0 + L_XS + lane * 16), xs1 = lds_ld<u32x4>(L0 + L_XS + 1024 + lane * 16);
    u32x4 xa[4];
#pragma unroll
    for (int t = 0; t < 4; ++t) xa[t] = lds_ld<u32x4>(L0 + L_XS + (n16 & 1) * 1024 + rq * 256 + t * 64 + g4 * 16);
    float mu, rr;
    {
      float s0 = 0.f, q0 = 0.f, s1 = 0.f, q1 = 0.f;
#pragma unroll
      for (int j = 0; j < 4; ++j) {
        const float a = bflo(xs0[j]), b = bfhi(xs0[j]), c = bflo(xs1[j]), d = bfhi(xs1[j]);
        s0 += a + b; q0 += a * a + b * b; s1 += c + d; q1 += c * c + d * d;
      }
      s0 = wave_sum(s0); q0 = wave_sum(q0); s1 = wave_sum(s1); q1 = wave_sum(q1);
      const float s = (lane & 1) ? s1 : s0, q = (lane & 1) ? q1 : q0;
      mu = s * (1.f / 512.f);
      rr = 1.f / sqrtf(fmaxf(q * (1.f / 512.f) - mu * mu, 0.f) + 1e-5f);
    }
#pragma unroll
    for (int t = 0; t < 4; ++t) xa[t] = (n16 & 3) == 2 ? ones : xa[t];
    flag_wait<0>(L0, F_WQ, 4u);
    TS(1);
    {
      f32x4 d[4];
      TRDEF();
      const unsigned a0 = L0 + L_WQ + (8 * g4 + trq) * 128 + trp * 8, sw = (g4 & 1) * 32;
#pragma unroll
      for (int nt = 0; nt < 4; ++nt) {
        d[nt] = f32x4{0.f, 0.f, 0.f, 0.f};
#pragma unroll
        for (int t = 0; t < 4; ++t) d[nt] = mfma(trfrag(a0 + t * 4096 + ((nt * 32) ^ sw), a0 + t * 4096 + ((nt * 32) ^ sw) + 512), xa[t], d[nt]);
      }
      const f32x4 lo = (n16 & 4) ? d[1] : d[0], hi = (n16 & 4) ? d[3] : d[2];
      const f32x4 dd = (n16 & 8) ? hi : lo;
      float qv[4];
#pragma unroll
      for (int r = 0; r < 4; ++r) qv[r] = rr * (dd[r] - mu * dpp<0xAA>(dd[r]));
      if ((n16 & 3) < 2)
        __builtin_amdgcn_raw_buffer_store_b128(u32x4{tlo, thi, pk(qv[0], qv[1]), pk(qv[2], qv[3])}, rsb,
                                               WS_QP + (((kvh * 2 + (n16 & 1)) * 4 + rq) * 128 + cbk * 16 + (n16 >> 2) * 4 + g4) * 16, 0, 16);
    }
    waitvm<1>();
    if (lane == 0) __builtin_amdgcn_raw_buffer_store_b64(u32x2{tlo, thi}, rsb, WS_FL + w * 8, 0, 16);
    TS(2);
    {
      u32x4 v[8];
      const unsigned ob = WS_QP + (kvh * 2 + bat) * 8192 + lane * 16;
#pragma unroll
      for (int i = 0; i < 8; ++i) v[i] = u32x4{~tlo, 0u, 0u, 0u};
      if (QDELAY) __builtin_amdgcn_s_sleep(QDELAY);
#pragma nounroll
      for (int spin = 0; spin < SPIN_G; ++spin) {
        u64 m[8];
#pragma unroll
        for (int i = 0; i < 8; ++i) m[i] = __ballot(v[i][0] != tlo || v[i][1] != thi);
        if ((m[0] | m[1] | m[2] | m[3] | m[4] | m[5] | m[6] | m[7]) == 0) break;
        poll_round8(rs, v, ob, m);
      }
#pragma unroll
      for (int h = 0; h < 2; ++h) {
        u32x2 r;
#pragma unroll
        for (int j = 0; j < 2; ++j) {
          const float lo = (bflo(v[h][2 + j]) + bflo(v[2 + h][2 + j])) + (bflo(v[4 + h][2 + j]) + bflo(v[6 + h][2 + j]));
          const float hi = (bfhi(v[h][2 + j]) + bfhi(v[2 + h][2 + j])) + (bfhi(v[4 + h][2 + j]) + bfhi(v[6 + h][2 + j]));
          r[j] = pk(lo, hi);
        }
        lds_st<u32x2>(L0 + L_Q + (64 * h + lane) * 8, r);
      }
      flag_set(L0, F_Q, 1u);
    }
    TS(3);
#pragma nounroll
    for (int spin = 0; spin < SPIN_G; ++spin) {
      asm volatile("" ::: "memory");
      const u32x4 f0 = __builtin_amdgcn_raw_buffer_load_b128(rsb, WS_FL + lane * 16, 0, 16);
      const u32x4 f1 = __builtin_amdgcn_raw_buffer_load_b128(rsb, WS_FL + 1024 + lane * 16, 0, 16);
      const bool ok = f0[0] == tlo && f0[1] == thi && f0[2] == tlo && f0[3] == thi && f1[0] == tlo && f1[1] == thi && f1[2] == tlo && f1[3] == thi;
      if (__all(ok)) break;
    }
    flag_set(L0, F_ALL, 1u);
    if (w == NWG - 1 && lane == 0) __hip_atomic_store((GAS u64*)(ws + WS_EPOCH), ((u64)thi << 32) | tlo, RLX_AGENT);
    TS(4);
    flag_wait<1>(L0, F_STAT, 4u);
    {
      const float m = lds_ld<float>(L0 + L_WM + n16 * 4), ls = lds_ld<float>(L0 + L_WL + n16 * 4);
      float M = fmaxf(m, dpp<ROR4>(m)); M = fmaxf(M, dpp<ROR8>(M));
      const float f = __builtin_amdgcn_exp2f(m - M);
      float L = f * ls;
      L += dpp<ROR4>(L); L += dpp<ROR8>(L);
      if (lane < 16) lds_st<float>(L0 + L_F + lane * 4, f);
      flag_set(L0, F_FAC, 1u);
      if (lane < 4)
        __builtin_amdgcn_raw_buffer_store_b64(u32x2{nosent(__float_as_uint(M)), nosent(__float_as_uint(L))}, rsb, WS_ST + (kvh * 32 + idx) * 32 + lane * 8, 0, 16);
    }
    TS(5);
    if (role == 0) {
      u32x4 st = {SENT, SENT, SENT, SENT};
      flag_wait<1>(L0, F_VISS, ST_AT);
      poll1(rs, st, WS_ST + kvh * 1024 + lane * 16);
      const float m0 = __uint_as_float(st[0]), l0 = __uint_as_float(st[1]), m1 = __uint_as_float(st[2]), l1 = __uint_as_float(st[3]);
      float M0 = m0, M1 = m1;
#pragma unroll
      for (int o2 = 2; o2 <= 16; o2 <<= 1) { M0 = fmaxf(M0, __shfl_xor(M0, o2)); M1 = fmaxf(M1, __shfl_xor(M1, o2)); }
      const float e0 = __builtin_amdgcn_exp2f(m0 - M0), e1 = __builtin_amdgcn_exp2f(m1 - M1);
      float S0 = e0 * l0, S1 = e1 * l1;
#pragma unroll
      for (int o2 = 2; o2 <= 16; o2 <<= 1) { S0 += __shfl_xor(S0, o2); S1 += __shfl_xor(S1, o2); }
      const int bb = lane >> 5, ss = (lane >> 1) & 15, h0 = (lane & 1) * 2;
      lds_st<float>(L0 + L_W + ((bb * 4 + h0) * 16 + ss) * 4, e0 / S0);
      lds_st<float>(L0 + L_W + ((bb * 4 + h0 + 1) * 16 + ss) * 4, e1 / S1);
      flag_set(L0, F_W, 1u);
      TS(6);
    }
    asm volatile(".Lfk_ce:" ::: "memory");
    TSEND();
    return;
  }

  const int wave = wave0 - 5;
  const unsigned LW = L0 + wave * 16384;
  const unsigned char* vb = vc + chunk + wave * 4096;
  __builtin_amdgcn_s_setprio(2);
  u32x4 msk;
  {
    const unsigned hm = (lane & 1) ? 0xffff0000u : 0x0000ffffu;
    const int dsel = (lane & 7) >> 1;
    msk = u32x4{dsel == 0 ? hm : 0u, dsel == 1 ? hm : 0u, dsel == 2 ? hm : 0u, dsel == 3 ? hm : 0u};
  }
  u32x4 V[16];
  const unsigned vo = lane * 16;
  flag_wait<1>(L0, F_KISS, 4u);
  TS(1);
#define LDV(i) ldv_nt<((i) & 3) * 1024>(V[i], vb + ((((i) >> 2) + rq) & 3) * 32768, vo)
  LDV(0); LDV(1); LDV(2); LDV(3); LDV(4); LDV(5); LDV(6); LDV(7); LDV(8); LDV(9); LDV(10); LDV(11);
  TS(2);
  flag_wait<1>(L0, F_P0 + (wave >> 1), 1u);
  u32x4 P[2][4];
#pragma unroll
  for (int u = 0; u < 2; ++u)
#pragma unroll
    for (int k = 0; k < 4; ++k) P[u][k] = lds_ld<u32x4>(L0 + L_P + wave * 1024 + ((2 * u + (n16 >> 3)) * 4 + g4) * 64 + k * 16);
  f32x4 acc[2] = {{0.f, 0.f, 0.f, 0.f}, {0.f, 0.f, 0.f, 0.f}};
  TS(3);
#define PV(j, N)                                                                        \
  do {                                                                                  \
    asm volatile("s_waitcnt vmcnt(%1)" : "+v"(V[j]) : "i"(N) : "memory");               \
    const u32x4 b0 = msk & P[0][(j) >> 2][(j) & 3], b1 = msk & P[1][(j) >> 2][(j) & 3]; \
    acc[0] = mfma(V[j], b0, acc[0]);                                                    \
    acc[1] = mfma(V[j], b1, acc[1]);                                                    \
    __builtin_amdgcn_sched_barrier(0);                                                  \
  } while (0)
  LDV(12); PV(0, 12); PV(1, 11);
  LDV(13); PV(2, 11); PV(3, 10);
  LDV(14); PV(4, 10); PV(5, 9);
  LDV(15); flag_add(L0, F_VISS); PV(6, 9); PV(7, 8);
  asm volatile("" : "+v"(acc[0]), "+v"(acc[1]));
  TS(4);
  flag_wait<0>(L0, F_FAC, 1u);
  unsigned oa = L0 + L_RB_O + wave * 1024 + lane * 16, og = WS_OP + prod * 1024 + (2 * wave + (n16 >> 3)) * 256 + g4 * 64 + (lane & 7) * 8;
  asm volatile("" : "+v"(oa), "+v"(og));
  const float f0 = lds_ld<float>(L0 + L_F + ((wave >> 1) * 4 + (n16 >> 3)) * 4), f1 = lds_ld<float>(L0 + L_F + ((wave >> 1) * 4 + 2 + (n16 >> 3)) * 4);
#pragma unroll
  for (int j = 8; j < 16; ++j) PV(j, 15 - j);
  TS(5);
  lds_st<f32x4>(LW + L_RB_O + lane * 16, acc[0] * f0);
  lds_st<f32x4>(LW + L_RB_O + 1024 + lane * 16, acc[1] * f1);
  flag_add(L0, F_S1);
  if (wave < 2) {
    flag_wait<0>(L0, F_S1, 8u);
    f32x4 r[8];
    lds_ld8_b128<16384>(r, oa, oa + 65536);
    const f32x4 s = ((r[0] + r[1]) + (r[2] + r[3])) + ((r[4] + r[5]) + (r[6] + r[7]));
    __builtin_amdgcn_raw_buffer_store_b64(u32x2{nosent(pk(s[0], s[1])), nosent(pk(s[2], s[3]))}, rsb, og, 0, 16);
    flag_add(L0, F_S2);
  }
  TS(6);
  if (role == 3) { TSEND(); return; }
  const u32x4 sel = msk & ones;
  TRDEF();

  if (role == 0) {
    const int kq = wave >> 1, bb = wave & 1;
    const int hd = 2 * (rk & 1) + (kq >> 1);
    const unsigned LB = L0 + kq * 32768;
    u32x4 A0, A1;
    const unsigned o0 = WS_OP + ((bb * 8 + (rk >> 1)) * 16 + (lane >> 3)) * 1024 + hd * 256 + (kq & 1) * 128 + (lane & 7) * 16;
    f32x4 xr0 = {0.f, 0.f, 0.f, 0.f}, xr1 = {0.f, 0.f, 0.f, 0.f};
    if (wave == 0 && rk == 0) {
#pragma unroll
      for (int j = 0; j < 4; ++j) {
        xr0[j] = bflo(lds_ld<unsigned short>(L0 + L_XS + (cq * 64 + j * 16 + n16) * 2));
        xr1[j] = bflo(lds_ld<unsigned short>(L0 + L_XS + (512 + cq * 64 + j * 16 + n16) * 2));
      }
    }
    flag_wait<0>(L0, F_W, 1u);
    const float wa = lds_ld<float>(L0 + L_W + ((bb * 4 + hd) * 16 + 2 * g4 + (n16 >> 3)) * 4), wb = lds_ld<float>(L0 + L_W + ((bb * 4 + hd) * 16 + 8 + 2 * g4 + (n16 >> 3)) * 4);
    u32x4 sa = msk & pk(wa, wa), sb = msk & pk(wb, wb);
    unsigned sw = LW + 8192 + n16 * 16 + (g4 & 1) * 8, sr = LW + 8192 + (n16 & 1) * 128 + g4 * 16, xw = L0 + L_X + wave * 256 + n16 * 16, xa = L0 + L_X + n16 * 16,
             og2 = WS_YP + rk * 2048 + cq * 256 + n16 * 16;
    const u64 vmask = __ballot((g4 >> 1) == (n16 >> 3));
    asm volatile("" : "+v"(sw), "+v"(sr), "+v"(xw), "+v"(xa), "+v"(og2), "+v"(xr0), "+v"(xr1), "+v"(sa), "+v"(sb));
    u32x4 Bw[2][4];
#pragma unroll
    for (int t = 0; t < 2; ++t) {
      flag_wait<0>(L0, F_P0 + kq, 2u + t);
      if (t == 1)
        asm volatile("buffer_load_dwordx4 %0, %2, %3, 0 offen sc1\n\tbuffer_load_dwordx4 %1, %2, %3, %4 offen sc1"
                     : "=&v"(A0), "=&v"(A1) : "v"(o0), "s"(rs), "s"(8192u) : "memory");
#pragma unroll
      for (int nt = 0; nt < 4; ++nt) {
        const unsigned a = LB + (32 * t + 8 * trq + 2 * g4) * 128 + ((nt ^ g4) * 32) + trp * 8;
        Bw[t][nt] = trfrag(a, a + 128);
      }
    }
    TS(7);
    asm volatile("s_waitcnt vmcnt(0)" : "+v"(A0), "+v"(A1)::"memory");
    poll2(rs, A0, A1, o0, 8192u);
    TS(8);
    f32x4 d = mfma(sa, A0, f32x4{0.f, 0.f, 0.f, 0.f});
    d = mfma(sb, A1, d);
    asm volatile("s_mov_b64 exec, %2\n\tds_write_b64 %0, %1\n\ts_mov_b64 exec, -1" ::"v"(sw), "v"(u32x2{pk(d[0], d[1]), pk(d[2], d[3])}), "s"(vmask) : "memory");
    A0 = lds_ld<u32x4>(sr);
    A1 = lds_ld<u32x4>(sr + 64);
    f32x4 val;
#pragma unroll
    for (int nt = 0; nt < 4; ++nt) {
      f32x4 e = mfma(A0, Bw[0][nt], f32x4{0.f, 0.f, 0.f, 0.f});
      e = mfma(A1, Bw[1][nt], e);
      val[nt] = e[0] + e[1];
    }
    asm volatile("s_mov_b64 exec, 0xffff\n\tds_write_b128 %0, %1\n\ts_mov_b64 exec, -1" ::"v"(xw), "v"(val) : "memory");
    wg_barrier();
    if (wave == 0) {
      f32x4 r[8];
      lds_ld8_b128<256>(r, xa, xa + 1024);
      xr0 += (r[0] + r[2]) + (r[4] + r[6]);
      xr1 += (r[1] + r[3]) + (r[5] + r[7]);
      if (lane < 16)
        __builtin_amdgcn_raw_buffer_store_b128(u32x4{nosent(pk(xr0[0], xr0[1])), nosent(pk(xr0[2], xr0[3])), nosent(pk(xr1[0], xr1[1])), nosent(pk(xr1[2], xr1[3]))},
                                               rsb, og2, 0, 16);
      asm volatile(".Lfk_e0:" ::: "memory");
      TS(9);
    }
  } else if (role == 1) {
    u32x4 H[4];
    const unsigned o = WS_YP + g4 * 2048 + wave * 256 + n16 * 16;
    flag_wait<1>(L0, F_ALL, 1u);
    flag_wait<1>(L0, F_P0 + (wave >> 1), 2u + ((wave ^ tk) & 1));
    issue4(rs, H, o, 8192u);
    u32x4 Bw[2][2][2];
#pragma unroll
    for (int mt = 0; mt < 2; ++mt)
#pragma unroll
      for (int t = 0; t < 2; ++t)
#pragma unroll
        for (int nt = 0; nt < 2; ++nt) {
          const unsigned a = LW + mt * 4096 + (16 * trq + 8 * t + 2 * g4 + (trq & 1)) * 64 + nt * 32 + trp * 8;
          Bw[mt][t][nt] = trfrag(a, a ^ 64);
        }
    unsigned xw = L0 + L_X + wave * 512 + lane * 8, xa = L0 + L_P + lane * 8;
    {
      f32x4 c[2][2];
#pragma unroll
      for (int nt = 0; nt < 2; ++nt)
#pragma unroll
        for (int mt = 0; mt < 2; ++mt) {
          c[nt][mt] = mfma(ones, Bw[mt][0][nt], f32x4{0.f, 0.f, 0.f, 0.f});
          c[nt][mt] = mfma(ones, Bw[mt][1][nt], c[nt][mt]);
        }
      lds_st<f32x2>(L0 + L_P + wave * 1024 + lane * 8, lane < 32 ? f32x2{c[0][0][0], c[0][1][0]} : f32x2{c[1][0][0], c[1][1][0]});
    }
    unsigned hw = LW + lane * 8, hr = LW + ((n16 >> 2) & 1) * 128 + g4 * 16, stw = L0 + L_X + 4096 + wave * 16 + g4 * 8, str = L0 + L_X + 4096 + (n16 & 7) * 16 + (g4 & 1) * 8,
             aw = LW + 256 + (g4 & 1) * 64 + (g4 >> 1) * 32 + n16 * 2, ar = LW + 256 + (n16 & 1) * 64 + g4 * 16, og2 = WS_YP + (16 + tk) * 2048 + wave * 256 + n16 * 16;
    asm volatile("" : "+v"(hw), "+v"(hr), "+v"(stw), "+v"(str), "+v"(aw), "+v"(ar), "+v"(og2), "+v"(xw), "+v"(xa));
    unsigned da = LW + 8192 + (8 * g4 + trq) * 128 + (g4 & 1) * 32 + trp * 8;
    unsigned db = da ^ 32;
    asm volatile("" : "+v"(da), "+v"(db));
    TS(7);
    land4(H);
    poll4(rs, H, o, 8192u);
    TS(8);
    f32x4 h = mfma(sel, H[0], f32x4{0.f, 0.f, 0.f, 0.f}), h2 = mfma(sel, H[1], f32x4{0.f, 0.f, 0.f, 0.f});
    h = mfma(sel, H[2], h);
    h2 = mfma(sel, H[3], h2);
    h += h2;
    asm volatile("s_mov_b32 exec_hi, 0\n\tds_write_b64 %0, %1\n\ts_mov_b32 exec_hi, -1" ::"v"(hw), "v"(u32x2{pk(h[0], h[1]), pk(h[2], h[3])}) : "memory");
    const u32x4 A0 = lds_ld<u32x4>(hr), A1 = lds_ld<u32x4>(hr + 64);
    const float ss = row_sum16((h[0] + h[1]) + (h[2] + h[3])), gs = row_sum16((h[0] * h[0] + h[1] * h[1]) + (h[2] * h[2] + h[3] * h[3]));
    asm volatile("s_mov_b64 exec, 0x10001\n\tds_write_b64 %0, %1\n\ts_mov_b64 exec, -1" ::"v"(stw), "v"(f32x2{ss, gs}) : "memory");
    f32x4 e[2][2];
#pragma unroll
    for (int nt = 0; nt < 2; ++nt)
#pragma unroll
      for (int mt = 0; mt < 2; ++mt) {
        e[nt][mt] = mfma(A0, Bw[mt][0][nt], f32x4{0.f, 0.f, 0.f, 0.f});
        e[nt][mt] = mfma(A1, Bw[mt][1][nt], e[nt][mt]);
      }
    lds_st<f32x2>(xw, lane < 32 ? f32x2{e[0][0][0], e[0][1][0]} : f32x2{e[1][0][0], e[1][1][0]});
    wg_barrier();
    f32x2 st = lds_ld<f32x2>(str);
    f32x2 r[8];
    f32x2 cs[8];
    lds_ld16_b64(r, cs, xa);
    u32x4 Bd[4];
#pragma unroll
    for (int j = 0; j < 4; ++j) Bd[j] = trfrag(((j & 1) ? db : da) + (j >> 1) * 64, ((j & 1) ? db : da) + (j >> 1) * 64 + 512);
    __builtin_amdgcn_sched_barrier(0);
    const f32x2 csum = ((cs[0] + cs[1]) + (cs[2] + cs[3])) + ((cs[4] + cs[5]) + (cs[6] + cs[7]));
    st[0] += dpp<XOR1>(st[0]); st[1] += dpp<XOR1>(st[1]);
    st[0] += dpp<XOR2>(st[0]); st[1] += dpp<XOR2>(st[1]);
    st[0] += dpp<XOR7>(st[0]); st[1] += dpp<XOR7>(st[1]);
    const f32x2 gu = ((r[0] + r[1]) + (r[2] + r[3])) + ((r[4] + r[5]) + (r[6] + r[7]));
    const float mu = st[0] * (1.f / 512.f);
    const float rr = __builtin_amdgcn_rsqf(fmaxf(st[1] * (1.f / 512.f) - mu * mu, 0.f) + 1e-5f);
    const float gg = rr * (gu[0] - mu * csum[0]), uu = rr * (gu[1] - mu * csum[1]);
    const float a = gg * uu * __builtin_amdgcn_rcpf(1.f + __builtin_amdgcn_exp2f(-1.4426950408889634f * gg));
    lds_st<__bf16>(aw, (__bf16)a);
    const u32x4 Aa = lds_ld<u32x4>(ar);
    f32x4 y[4];
#pragma unroll
    for (int j = 0; j < 4; ++j) y[j] = mfma(Aa, Bd[j], f32x4{0.f, 0.f, 0.f, 0.f});
    if (lane < 16)
      __builtin_amdgcn_raw_buffer_store_b128(
          u32x4{nosent(pk(y[0][0], y[1][0])), nosent(pk(y[2][0], y[3][0])), nosent(pk(y[0][1], y[1][1])), nosent(pk(y[2][1], y[3][1]))}, rsb, og2, 0, 16);
    asm volatile(".Lfk_e1:" ::: "memory");
    TS(9);
  } else if (wave == 2) {
    const int ee = 4 * kvh + ri - 16;
    u32x4 Y0 = {SENT, SENT, SENT, SENT}, Y[4];
    const unsigned ob = WS_YP + (lane >> 2) * 2048 + ee * 64 + (lane & 3) * 16;
#pragma unroll
    for (int i = 0; i < 4; ++i) Y[i] = (i < 3 || lane < 32) ? u32x4{SENT, SENT, SENT, SENT} : u32x4{0u, 0u, 0u, 0u};
    flag_wait<1>(L0, F_ALL, 1u);
#pragma nounroll
    for (int spin = 0; spin < SPIN_G; ++spin) {
      const u64 m = __ballot(pend4(Y0));
      if (m == 0) break;
      __builtin_amdgcn_s_sleep(4);
      poll_round1(rs, Y0, ob, m);
    }
    f32x4 d0 = mfma(sel, Y0, f32x4{0.f, 0.f, 0.f, 0.f});
    TS(7);
    poll4(rs, Y, ob + 32768u, 32768u);
    TS(8);
    f32x4 d1 = mfma(sel, Y[1], f32x4{0.f, 0.f, 0.f, 0.f});
    d0 = mfma(sel, Y[0], d0);
    d1 = mfma(sel, Y[3], d1);
    d0 = mfma(sel, Y[2], d0);
    f32x4 d = d0 + d1;
#pragma unroll
    for (int r = 0; r < 4; ++r) { d[r] += dpp<ROR4>(d[r]); d[r] += dpp<ROR8>(d[r]); }
    const float lo = (n16 & 4) ? d[1] : d[0], hi = (n16 & 4) ? d[3] : d[2];
    const float res = (n16 & 8) ? hi : lo;
    const float nb = dpp<XOR1>(res);
    if (lane < 32 && (lane & 1) == 0)
      *(unsigned*)(out + g4 * 512 + 64 * (ee >> 2) + 16 * (n16 >> 2) + 4 * (ee & 3) + (n16 & 3)) = pk(res, nb);
    asm volatile(".Lfk_e2:" ::: "memory");
    TS(9);
  }
  asm volatile(".Lfk_te:" ::: "memory");
  TSEND();
}
}

extern "C" void kernel_launch(void* const* d_in, const int* in_sizes, int n_in, void* d_out, int out_size, void* d_ws, size_t ws_size,
                              hipStream_t stream) {
  (void)in_sizes; (void)n_in; (void)out_size; (void)ws_size;
  (void)hipFuncSetAttribute((const void*)fk::fused_kernel, hipFuncAttributeMaxDynamicSharedMemorySize, fk::L_END);
  fk::fused_kernel<<<fk::NWG, fk::NTHR, fk::L_END, stream>>>(
      (const unsigned char*)d_in[3], (const unsigned char*)d_in[1], (unsigned char*)d_ws, (const unsigned char*)d_in[0],
      (const unsigned char*)d_in[2], (const unsigned char*)d_in[4], (const unsigned char*)d_in[6], (const unsigned char*)d_in[7],
      (const unsigned char*)d_in[8], (unsigned short*)d_out);
}
```

```cpp
#include <hip/hip_runtime.h>
#include <hip/hip_bf16.h>
#include <stdint.h>

namespace fk {
typedef unsigned u32x4 __attribute__((ext_vector_type(4)));
typedef unsigned u32x2 __attribute__((ext_vector_type(2)));
typedef float f32x4 __attribute__((ext_vector_type(4)));
typedef float f32x2 __attribute__((ext_vector_type(2)));
typedef short s16x4 __attribute__((ext_vector_type(4)));
typedef __bf16 bf16x8 __attribute__((ext_vector_type(8)));
typedef __bf16 bf16x2 __attribute__((ext_vector_type(2)));
typedef unsigned long long u64;
#define LAS __attribute__((address_space(3)))
#define GAS __attribute__((address_space(1)))
#define RLX_AGENT __ATOMIC_RELAXED, __HIP_MEMORY_SCOPE_AGENT

#define FK_TS 0
#define KW_N 6
#define APF 1
#define APM 7
#define K0 1
#define TPF 1
#define TPN 2
#define GNT 1
#define EL 0
#define ST_AT 4u
#define QDELAY 40
constexpr int VN1 = 12;
constexpr unsigned SENT = 0xFFFFFFFFu;
constexpr int NWG = 256, NWAVES = 13, NTHR = NWAVES * 64;
constexpr int WS_EPOCH = 0;
constexpr int WS_FL = 4096;
constexpr int WS_QP = 8192;
constexpr int WS_ST = WS_QP + 131072;
constexpr int WS_OP = WS_ST + 8192;
constexpr int WS_YP = WS_OP + 262144;
constexpr int WS_END = WS_YP + 72 * 2048;
constexpr int WS_TS = 1 << 20;
constexpr int L_RB_O = 12288;
constexpr int L_WQ = 131072, L_P = L_WQ  , L_X = L_WQ + 8192  , L_Q = L_WQ + 16384, L_WM = L_Q + 1024, L_WL = L_WM + 64, L_F = L_WL + 64,
              L_W = L_F + 64, L_FLAGS = L_W + 512, L_SCR = L_FLAGS + 64, L_XS = L_SCR + 256, L_END = L_XS + 2048;
enum { F_Q = 0, F_STAT = 1, F_FAC = 2, F_VISS = 3, F_WQ = 4, F_KISS = 5, F_S1 = 6, F_S2 = 7, F_ALL = 9, F_W = 10, F_P0 = 12   };
constexpr int SPIN_G = 1 << 18;
constexpr int SPIN_L = 1 << 22;
constexpr float QSCALE = 0.088388347648318440f * 1.4426950408889634f;
constexpr unsigned ONE2 = 0x3F803F80u;

__device__ __forceinline__ f32x4 mfma(u32x4 a, u32x4 b, f32x4 c) {
  return __builtin_amdgcn_mfma_f32_16x16x32_bf16(__builtin_bit_cast(bf16x8, a), __builtin_bit_cast(bf16x8, b), c, 0, 0, 0);
}
__device__ __forceinline__ unsigned pk(float lo, float hi) { bf16x2 v = {(__bf16)lo, (__bf16)hi}; return __builtin_bit_cast(unsigned, v); }
__device__ __forceinline__ unsigned nosent(unsigned x) { return x < 0xFFFFFFFEu ? x : 0xFFFFFFFEu; }
__device__ __forceinline__ float bflo(unsigned u) { return __builtin_bit_cast(float, u << 16); }
__device__ __forceinline__ float bfhi(unsigned u) { return __builtin_bit_cast(float, u & 0xffff0000u); }
__device__ __forceinline__ float x16_sum(float x) {
  auto s = __builtin_amdgcn_permlane16_swap(__float_as_uint(x), __float_as_uint(x), false, false);
  return __uint_as_float(s[0]) + __uint_as_float(s[1]);
}
__device__ __forceinline__ float x32_sum(float x) {
  auto s = __builtin_amdgcn_permlane32_swap(__float_as_uint(x), __float_as_uint(x), false, false);
  return __uint_as_float(s[0]) + __uint_as_float(s[1]);
}
__device__ __forceinline__ float x16_max(float x) {
  auto s = __builtin_amdgcn_permlane16_swap(__float_as_uint(x), __float_as_uint(x), false, false);
  return fmaxf(__uint_as_float(s[0]), __uint_as_float(s[1]));
}
__device__ __forceinline__ float x32_max(float x) {
  auto s = __builtin_amdgcn_permlane32_swap(__float_as_uint(x), __float_as_uint(x), false, false);
  return fmaxf(__uint_as_float(s[0]), __uint_as_float(s[1]));
}
template <int CTRL> __device__ __forceinline__ float dpp(float x) {
  return __builtin_bit_cast(float, __builtin_amdgcn_mov_dpp(__builtin_bit_cast(int, x), CTRL, 0xf, 0xf, true));
}
constexpr int XOR1 = 0xB1, XOR2 = 0x4E, XOR7 = 0x141, ROR4 = 0x124, ROR8 = 0x128;
__device__ __forceinline__ float row_sum16(float x) { x += dpp<XOR1>(x); x += dpp<XOR2>(x); x += dpp<XOR7>(x); x += dpp<ROR8>(x); return x; }
__device__ __forceinline__ float wave_sum(float x) { return x32_sum(x16_sum(row_sum16(x))); }

template <int N> __device__ __forceinline__ void waitvm() { asm volatile("s_waitcnt vmcnt(%0)" ::"i"(N) : "memory"); }
__device__ __forceinline__ void dma_nt(const void* sbase, unsigned voff, unsigned ldst) {
  asm volatile("s_mov_b32 m0, %2\n\ts_nop 0\n\tglobal_load_lds_dwordx4 %0, %1 nt" ::"v"(voff), "s"(sbase), "s"(ldst) : "memory");
}
template <int W> __device__ __forceinline__ void dma4_nt(const void* sbase, const unsigned (&v)[4], unsigned ldst) {
  if constexpr (W == 0)
    asm volatile("s_mov_b32 m0, %5\n\ts_nop 0\n\tglobal_load_lds_dwordx4 %0, %4 nt\n\tglobal_load_lds_dwordx4 %1, %4 offset:1024 nt\n\t"
                 "global_load_lds_dwordx4 %2, %4 offset:2048 nt\n\tglobal_load_lds_dwordx4 %3, %4 offset:3072 nt"
                 ::"v"(v[0]), "v"(v[1]), "v"(v[2]), "v"(v[3]), "s"(sbase), "s"(ldst) : "memory");
  else
    asm volatile("s_mov_b32 m0, %5\n\ts_waitcnt vmcnt(%6)\n\tglobal_load_lds_dwordx4 %0, %4 nt\n\ts_waitcnt vmcnt(%6)\n\tglobal_load_lds_dwordx4 %1, %4 offset:1024 nt\n\t"
                 "s_waitcnt vmcnt(%6)\n\tglobal_load_lds_dwordx4 %2, %4 offset:2048 nt\n\ts_waitcnt vmcnt(%6)\n\tglobal_load_lds_dwordx4 %3, %4 offset:3072 nt"
                 ::"v"(v[0]), "v"(v[1]), "v"(v[2]), "v"(v[3]), "s"(sbase), "s"(ldst), "i"(W - 1) : "memory");
}
__device__ __forceinline__ void dma4(const void* sbase, const unsigned (&v)[4], unsigned ldst) {
  asm volatile("s_mov_b32 m0, %5\n\ts_nop 0\n\tglobal_load_lds_dwordx4 %0, %4\n\tglobal_load_lds_dwordx4 %1, %4 offset:1024\n\t"
               "global_load_lds_dwordx4 %2, %4 offset:2048\n\tglobal_load_lds_dwordx4 %3, %4 offset:3072"
               ::"v"(v[0]), "v"(v[1]), "v"(v[2]), "v"(v[3]), "s"(sbase), "s"(ldst) : "memory");
}
__device__ __forceinline__ void touch(const void* sbase, unsigned voff, unsigned ldst) {
  asm volatile("s_mov_b32 m0, %2\n\ts_nop 0\n\tglobal_load_lds_dword %0, %1" ::"v"(voff), "s"(sbase), "s"(ldst) : "memory");
}
template <int OFF> __device__ __forceinline__ void ldv_nt(u32x4& dst, const void* sbase, unsigned voff) {
  asm volatile("global_load_dwordx4 %0, %1, %2 offset:%3 nt" : "=v"(dst) : "v"(voff), "s"(sbase), "i"(OFF) : "memory");
}
__device__ __forceinline__ void ldv(u32x4& dst, const void* sbase, unsigned voff) {
  asm volatile("global_load_dwordx4 %0, %1, %2" : "=v"(dst) : "v"(voff), "s"(sbase) : "memory");
}
__device__ __forceinline__ u32x2 trread(unsigned addr) {
  return __builtin_bit_cast(u32x2, __builtin_amdgcn_ds_read_tr16_b64_v4i16((LAS s16x4*)(uintptr_t)addr));
}
__device__ __forceinline__ u32x4 trfrag(unsigned a0, unsigned a1) { u32x2 lo = trread(a0), hi = trread(a1); return u32x4{lo[0], lo[1], hi[0], hi[1]}; }
template <typename T> __device__ __forceinline__ T lds_ld(unsigned addr) { return *(LAS T*)(uintptr_t)addr; }
template <typename T> __device__ __forceinline__ void lds_st(unsigned addr, T v) { *(LAS T*)(uintptr_t)addr = v; }
__device__ __forceinline__ void flag_set(unsigned base, int f, unsigned v) {
  asm volatile("s_mov_b64 exec, 1\n\tds_write_b32 %0, %1\n\ts_mov_b64 exec, -1" ::"v"(base + L_FLAGS + f * 4), "v"(v) : "memory");
}
__device__ __forceinline__ void flag_add(unsigned base, int f) {
  asm volatile("s_mov_b64 exec, 1\n\tds_add_u32 %0, %1\n\ts_mov_b64 exec, -1" ::"v"(base + L_FLAGS + f * 4), "v"(1u) : "memory");
}
__device__ __forceinline__ void wg_barrier() { asm volatile("s_waitcnt lgkmcnt(0)\n\ts_barrier" ::: "memory"); }
__device__ __forceinline__ void lds_ld16_b64(f32x2 (&r)[8], f32x2 (&c)[8], unsigned a) {
  asm volatile("ds_read_b64 %0, %16 offset:8192\n\tds_read_b64 %1, %16 offset:8704\n\tds_read_b64 %2, %16 offset:9216\n\tds_read_b64 %3, %16 offset:9728\n\t"
               "ds_read_b64 %4, %16 offset:10240\n\tds_read_b64 %5, %16 offset:10752\n\tds_read_b64 %6, %16 offset:11264\n\tds_read_b64 %7, %16 offset:11776\n\t"
               "ds_read_b64 %8, %16\n\tds_read_b64 %9, %16 offset:1024\n\tds_read_b64 %10, %16 offset:2048\n\tds_read_b64 %11, %16 offset:3072\n\t"
               "ds_read_b64 %12, %16 offset:4096\n\tds_read_b64 %13, %16 offset:5120\n\tds_read_b64 %14, %16 offset:6144\n\tds_read_b64 %15, %16 offset:7168\n\t"
               "s_waitcnt lgkmcnt(0)"
               : "=&v"(r[0]), "=&v"(r[1]), "=&v"(r[2]), "=&v"(r[3]), "=&v"(r[4]), "=&v"(r[5]), "=&v"(r[6]), "=&v"(r[7]),
                 "=&v"(c[0]), "=&v"(c[1]), "=&v"(c[2]), "=&v"(c[3]), "=&v"(c[4]), "=&v"(c[5]), "=&v"(c[6]), "=&v"(c[7])
               : "v"(a) : "memory");
}
template <int ST> __device__ __forceinline__ void lds_ld8_b128(f32x4 (&r)[8], unsigned a, unsigned b) {
  asm volatile("ds_read_b128 %0, %8\n\tds_read_b128 %1, %8 offset:%10\n\tds_read_b128 %2, %8 offset:%11\n\tds_read_b128 %3, %8 offset:%12\n\t"
               "ds_read_b128 %4, %9\n\tds_read_b128 %5, %9 offset:%10\n\tds_read_b128 %6, %9 offset:%11\n\tds_read_b128 %7, %9 offset:%12\n\t"
               "s_waitcnt lgkmcnt(0)"
               : "=&v"(r[0]), "=&v"(r[1]), "=&v"(r[2]), "=&v"(r[3]), "=&v"(r[4]), "=&v"(r[5]), "=&v"(r[6]), "=&v"(r[7])
               : "v"(a), "v"(b), "i"(ST), "i"(2 * ST), "i"(3 * ST) : "memory");
}
template <int SLP> __device__ __forceinline__ void flag_wait(unsigned base, int f, unsigned v) {
  asm volatile("" ::: "memory");
#pragma nounroll
  for (int i = 0; i < SPIN_L; ++i) {
    unsigned x = *(volatile LAS unsigned*)(uintptr_t)(base + L_FLAGS + f * 4);
    if ((unsigned)__builtin_amdgcn_readfirstlane(x) >= v) break;
    if (SLP) __builtin_amdgcn_s_sleep(SLP);
  }
  asm volatile("" ::: "memory");
}
__device__ __forceinline__ bool pend4(u32x4 v) {
  unsigned a = v[0] > v[1] ? v[0] : v[1], b = v[2] > v[3] ? v[2] : v[3];
  return (a > b ? a : b) == SENT;
}
__device__ __forceinline__ void poll_round1(u32x4 rs, u32x4& v0, unsigned o0, u64 m0) {
  asm volatile("s_mov_b64 exec, %3\n\tbuffer_load_dwordx4 %0, %1, %2, 0 offen sc1\n\t"
               "s_mov_b64 exec, -1\n\ts_waitcnt vmcnt(0)"
               : "+v"(v0) : "v"(o0), "s"(rs), "s"(m0) : "memory");
}
__device__ __forceinline__ void poll_round8(u32x4 rs, u32x4 (&v)[8], unsigned o, const u64 (&m)[8]) {
  asm volatile("s_mov_b64 exec, %10\n\tbuffer_load_dwordx4 %0, %8, %9, 0 offen sc1\n\t"
               "s_mov_b64 exec, %11\n\tbuffer_load_dwordx4 %1, %8, %9, 0 offen offset:1024 sc1\n\t"
               "s_mov_b64 exec, %12\n\tbuffer_load_dwordx4 %2, %8, %9, 0 offen offset:2048 sc1\n\t"
               "s_mov_b64 exec, %13\n\tbuffer_load_dwordx4 %3, %8, %9, 0 offen offset:3072 sc1\n\t"
               "s_mov_b64 exec, %14\n\tbuffer_load_dwordx4 %4, %8, %9, %18 offen sc1\n\t"
               "s_mov_b64 exec, %15\n\tbuffer_load_dwordx4 %5, %8, %9, %18 offen offset:1024 sc1\n\t"
               "s_mov_b64 exec, %16\n\tbuffer_load_dwordx4 %6, %8, %9, %18 offen offset:2048 sc1\n\t"
               "s_mov_b64 exec, %17\n\tbuffer_load_dwordx4 %7, %8, %9, %18 offen offset:3072 sc1\n\t"
               "s_mov_b64 exec, -1\n\ts_waitcnt vmcnt(0)"
               : "+v"(v[0]), "+v"(v[1]), "+v"(v[2]), "+v"(v[3]), "+v"(v[4]), "+v"(v[5]), "+v"(v[6]), "+v"(v[7])
               : "v"(o), "s"(rs), "s"(m[0]), "s"(m[1]), "s"(m[2]), "s"(m[3]), "s"(m[4]), "s"(m[5]), "s"(m[6]), "s"(m[7]), "s"(4096u) : "memory");
}
__device__ __forceinline__ void issue4(u32x4 rs, u32x4 (&v)[4], unsigned o, unsigned st) {
  asm volatile("buffer_load_dwordx4 %0, %4, %5, 0 offen sc1\n\tbuffer_load_dwordx4 %1, %4, %5, %6 offen sc1\n\t"
               "buffer_load_dwordx4 %2, %4, %5, %7 offen sc1\n\tbuffer_load_dwordx4 %3, %4, %5, %8 offen sc1"
               : "=&v"(v[0]), "=&v"(v[1]), "=&v"(v[2]), "=&v"(v[3]) : "v"(o), "s"(rs), "s"(st), "s"(2 * st), "s"(3 * st) : "memory");
}
__device__ __forceinline__ void land4(u32x4 (&v)[4]) {
  asm volatile("s_waitcnt vmcnt(0)" : "+v"(v[0]), "+v"(v[1]), "+v"(v[2]), "+v"(v[3])::"memory");
}
__device__ __forceinline__ void poll2(u32x4 rs, u32x4& A0, u32x4& A1, unsigned o, unsigned st) {
#pragma nounroll
  for (int spin = 0; spin < SPIN_G; ++spin) {
    const u64 m0 = __ballot(pend4(A0)), m1 = __ballot(pend4(A1));
    if ((m0 | m1) == 0) break;
    asm volatile("s_mov_b64 exec, %5\n\tbuffer_load_dwordx4 %0, %2, %3, 0 offen sc1\n\t"
                 "s_mov_b64 exec, %6\n\tbuffer_load_dwordx4 %1, %2, %3, %4 offen sc1\n\t"
                 "s_mov_b64 exec, -1\n\ts_waitcnt vmcnt(0)"
                 : "+v"(A0), "+v"(A1) : "v"(o), "s"(rs), "s"(st), "s"(m0), "s"(m1) : "memory");
  }
}
__device__ __forceinline__ void poll4(u32x4 rs, u32x4 (&A)[4], unsigned o, unsigned st) {
#pragma nounroll
  for (int spin = 0; spin < SPIN_G; ++spin) {
    u64 m[4];
#pragma unroll
    for (int i = 0; i < 4; ++i) m[i] = __ballot(pend4(A[i]));
    if ((m[0] | m[1] | m[2] | m[3]) == 0) break;
    asm volatile("s_mov_b64 exec, %9\n\tbuffer_load_dwordx4 %0, %4, %5, 0 offen sc1\n\t"
                 "s_mov_b64 exec, %10\n\tbuffer_load_dwordx4 %1, %4, %5, %6 offen sc1\n\t"
                 "s_mov_b64 exec, %11\n\tbuffer_load_dwordx4 %2, %4, %5, %7 offen sc1\n\t"
                 "s_mov_b64 exec, %12\n\tbuffer_load_dwordx4 %3, %4, %5, %8 offen sc1\n\t"
                 "s_mov_b64 exec, -1\n\ts_waitcnt vmcnt(0)"
                 : "+v"(A[0]), "+v"(A[1]), "+v"(A[2]), "+v"(A[3])
                 : "v"(o), "s"(rs), "s"(st), "s"(2 * st), "s"(3 * st), "s"(m[0]), "s"(m[1]), "s"(m[2]), "s"(m[3]) : "memory");
  }
}
__device__ __forceinline__ void poll1(u32x4 rs, u32x4& A, unsigned o) {
#pragma nounroll
  for (int spin = 0; spin < SPIN_G; ++spin) {
    const u64 m = __ballot(pend4(A));
    if (m == 0) break;
    poll_round1(rs, A, o, m);
  }
}

#define FK_LAST ".Lfk_ke"
#define FK_END() asm volatile("s_endpgm" ::: "memory")
#define TS(k) do {} while (0)
#define TSEND() do {} while (0)

__global__ void __launch_bounds__(NTHR, 4)
fused_kernel(const unsigned char* wq_, const unsigned char* kc_, unsigned char* ws_, const unsigned char* x_,
             const unsigned char* __restrict__ vc, const unsigned char* __restrict__ wo, const unsigned char* __restrict__ wg,
             const unsigned char* __restrict__ wu, const unsigned char* __restrict__ wd, unsigned short* __restrict__ out) {
  typedef unsigned long long u64x4 __attribute__((ext_vector_type(4)));
  u64x4 ka;
  asm volatile(".Lfk_start:\n\ts_load_dwordx8 %0, %1, 0x0" : "=s"(ka) : "s"(__builtin_amdgcn_kernarg_segment_ptr()) : "memory");
  extern __shared__ __attribute__((aligned(16))) unsigned char smem[];
  const unsigned L0 = (unsigned)(uintptr_t)(LAS unsigned char*)smem;
  const int tid = threadIdx.x, lane = tid & 63, wave0 = __builtin_amdgcn_readfirstlane(tid >> 6);
  const int w = blockIdx.x, kvh = w & 7, idx = w >> 3, bat = idx >> 4, spl = idx & 15;
  const int n16 = lane & 15, g4 = lane >> 4;
  const int rq = idx & 3, cbk = idx >> 2;
  const size_t chunk = ((size_t)(bat * 8 + kvh) * 8192 + spl * 512) * 256;
  const int kw = wave0 - 1;
  const bool isk = (unsigned)kw < 4u;
  if ((w & (8 * APM)) == 0) {
    u64 pc;
    unsigned o;
    asm volatile("s_getpc_b64 %0\n\t.Lfk_h2:\n\tv_lshlrev_b32 %1, 5, %2\n\tv_min_u32 %1, " FK_LAST "-.Lfk_h2-4, %1\n\ts_mov_b32 m0, %3\n\ts_nop 0\n\t"
                 "global_load_lds_dword %1, %0" : "=&s"(pc), "=&v"(o) : "v"(tid), "s"(L0 + L_SCR) : "memory");
  }
  asm volatile("s_waitcnt lgkmcnt(0)" : "+s"(ka)::"memory");
  const unsigned char* wq = (const unsigned char*)ka[0];
  const unsigned char* kc = (const unsigned char*)ka[1];
  unsigned char* ws = (unsigned char*)ka[2];
  const unsigned char* x = (const unsigned char*)ka[3];
  const unsigned LK = L0 + kw * 32768;
  const unsigned char* kb = kc + chunk + kw * 8192;
#define KVODEF()                  \
  int lane_k = lane;              \
  asm volatile("" : "+v"(lane_k)); \
  unsigned kvo[4];                \
  _Pragma("unroll") for (int a = 0; a < 4; ++a) kvo[a] = (lane_k >> 4) * 256 + (((lane_k & 15) ^ (4 * (lane_k >> 4) + a)) * 16)
  if (isk) {
    const unsigned v0 = (lane >> 3) * 8192 + (lane & 7) * 16;
    const unsigned wv[4] = {v0, (v0 ^ 32) + 64512, v0 + 2 * 64512, (v0 ^ 32) + 3 * 64512};
    dma4_nt<0>(wq + (size_t)(rq * 128 + kw * 32) * 8192 + (kvh * 512 + cbk * 64) * 2, wv, L0 + L_WQ + kw * 4096);
    KVODEF();
    if (K0 >= 1) dma4_nt<0>(kb + rq * 32768, kvo, LK + rq * 8192);
    if (K0 == 2) dma4_nt<0>(kb + rq * 32768 + 4096, kvo, LK + rq * 8192 + 4096);
  }
  const int prod = (bat * 8 + kvh) * 16 + spl;
  const u32x4 rs = {(unsigned)(uintptr_t)ws, (unsigned)((uintptr_t)ws >> 32) & 0xffffu, (unsigned)WS_END, 0x00020000u};
  unsigned tlo = 0, thi = 0;
  if (wave0 == 0) {
    __builtin_amdgcn_s_setprio(3);
    u32x2 ep;
    asm volatile("global_load_dwordx2 %0, %1, %2 sc1" : "=v"(ep) : "v"(0u), "s"(ws) : "memory");
    asm volatile("s_mov_b32 m0, %2\n\ts_nop 0\n\tglobal_load_lds_dwordx4 %0, %1\n\tglobal_load_lds_dwordx4 %0, %1 offset:1024" ::"v"(lane * 16), "s"(x), "s"(L0 + L_XS) : "memory");
    if (lane < 16) lds_st<unsigned>(L0 + L_FLAGS + lane * 4, 0u);
    asm volatile("s_waitcnt lgkmcnt(0)\n\ts_barrier" ::: "memory");
    const u32x4 s4 = {SENT, SENT, SENT, SENT};
    const int OOB = 0x7ffffff0;
    int o1 = OOB;
    if (lane < 2) o1 = WS_ST + (kvh * 32 + idx) * 32 + lane * 16;
    const int o2 = (idx >= 16 && idx < 25) ? WS_YP + (kvh * 9 + idx - 16) * 2048 + lane * 16 : OOB;
    asm volatile("buffer_store_dwordx4 %1, %2, %5, 0 offen sc1\n\t"
                 "buffer_store_dwordx4 %1, %3, %5, 0 offen sc1\n\t"
                 "buffer_store_dwordx4 %1, %4, %5, 0 offen sc1\n\t"
                 "buffer_store_dwordx4 %1, %4, %5, 0 offen offset:1024 sc1\n\ts_waitcnt vmcnt(4)"
                 : "+v"(ep) : "v"(s4), "v"(WS_OP + prod * 1024 + lane * 16), "v"(o1), "v"(o2), "s"(rs) : "memory");
    const u64 tag = (((u64)ep[1] << 32) | ep[0]) + 1;
    tlo = __builtin_amdgcn_readfirstlane((unsigned)tag);
    thi = __builtin_amdgcn_readfirstlane((unsigned)(tag >> 32));
  } else {
    asm volatile("s_barrier" ::: "memory");
  }
  const int nC = 8 - 2 * (kvh >> 2), cc0 = 8 * kvh - 2 * (kvh >> 2) * (kvh - 4), ri = idx - nC;
  const int role = (ri < 0) + 2 * (ri >= 16) + (ri >= 20);
  const int tk = cc0 + idx;
  const int rk = 2 * kvh + (ri >> 3), cq = ri & 7;
  const __amdgpu_buffer_rsrc_t rsb = __builtin_amdgcn_make_buffer_rsrc(ws, 0, WS_END, 0x00020000);
#define TRDEF()                   \
  int lane_ = lane;               \
  asm volatile("" : "+v"(lane_)); \
  const unsigned trq = (lane_ >> 2) & 3, trp = lane_ & 3
  const u32x4 ones = {ONE2, ONE2, ONE2, ONE2};

  if (isk) {
    __builtin_amdgcn_s_setprio(2);
    KVODEF();
    waitvm<4 * K0>();
    flag_add(L0, F_WQ);
    if (role == 1) {
#pragma nounroll
      for (int t = K0; t < 8; ++t) {
        const int tl = (t + 2 * rq) & 7;
        dma4_nt<8>(kb + (tl >> 1) * 32768 + (tl & 1) * 4096, kvo, LK + tl * 4096);
      }
    } else {
#pragma nounroll
      for (int t = K0; t < 8; ++t) {
        const int tl = (t + 2 * rq) & 7;
        dma4_nt<KW_N>(kb + (tl >> 1) * 32768 + (tl & 1) * 4096, kvo, LK + tl * 4096);
      }
    }
    flag_add(L0, F_KISS);
    TS(1);
    flag_wait<1>(L0, F_Q, 1u);
    TS(2);
    u32x4 qf[4];
#pragma unroll
    for (int t = 0; t < 4; ++t) qf[t] = lds_ld<u32x4>(L0 + L_Q + (n16 & 3) * 256 + t * 64 + g4 * 16);
    f32x4 sel[2];
    const unsigned krow = LK + (4 * (n16 & 3) + (n16 >> 2)) * 256;
#pragma unroll
    for (int hf = 0; hf < 2; ++hf) {
      if (hf == 1) waitvm<0>();
      f32x4 S[4];
#pragma unroll
      for (int T = 0; T < 4; ++T) {
        S[T] = f32x4{0.f, 0.f, 0.f, 0.f};
#pragma unroll
        for (int t = 0; t < 4; ++t) S[T] = mfma(lds_ld<u32x4>(krow + ((((2 * hf + (T >> 1) + rq) & 3) * 2 + (T & 1)) * 4096) + (((4 * t + g4) ^ n16) * 16)), qf[t], S[T]);
      }
      const f32x4 lo = (n16 & 4) ? S[1] : S[0], hi = (n16 & 4) ? S[3] : S[2];
      sel[hf] = (n16 & 8) ? hi : lo;
    }
    float m = fmaxf(fmaxf(fmaxf(sel[0][0], sel[0][1]), fmaxf(sel[0][2], sel[0][3])), fmaxf(fmaxf(sel[1][0], sel[1][1]), fmaxf(sel[1][2], sel[1][3])));
    m = fmaxf(m, dpp<ROR4>(m)); m = fmaxf(m, dpp<ROR8>(m));
    m = x32_max(x16_max(m));
    const float ms = m * QSCALE;
    float l = 0.f;
#pragma unroll
    for (int hf = 0; hf < 2; ++hf) {
      u32x4 pp;
#pragma unroll
      for (int r = 0; r < 4; ++r) {
        const float p = __builtin_amdgcn_exp2f(fmaf(sel[hf][r], QSCALE, -ms));
        pp[r] = pk(p, p);
        l += bfhi(pp[r]);
      }
      lds_st<u32x4>(L0 + L_P + (2 * kw + ((n16 >> 2) & 1)) * 1024 + (((n16 & 3) * 4 + g4) * 16 + 4 * (2 * hf + (n16 >> 3))) * 4, pp);
    }
    flag_set(L0, F_P0 + kw, 1u);
    l += dpp<ROR4>(l); l += dpp<ROR8>(l);
    l = x32_sum(x16_sum(l));
    if (lane < 4) {
      lds_st<float>(L0 + L_WM + (kw * 4 + lane) * 4, ms);
      lds_st<float>(L0 + L_WL + (kw * 4 + lane) * 4, l);
    }
    flag_add(L0, F_STAT);
    TS(3);
    __builtin_amdgcn_s_setprio(0);
    if (role == 0) {
      const unsigned char* base = wo + (size_t)(rk * 256 + kw * 64) * 1024 + cq * 128;
      const unsigned v0 = (lane >> 3) * 1024 + (((lane & 7) ^ (g4 << 1)) * 16);
      const unsigned wv[4] = {v0, v0 + 7168, v0 + 2 * 7168, v0 + 3 * 7168};
      flag_wait<1>(L0, F_VISS, 8u);
      dma4_nt<0>(base, wv, LK);
      dma4_nt<0>(base + 32768, wv, LK + 4096);
      TS(4);
      waitvm<4>();
      flag_set(L0, F_P0 + kw, 2u);
      waitvm<0>();
      flag_set(L0, F_P0 + kw, 3u);
      TS(5);
    } else if (role == 1) {
      const unsigned va = (lane >> 2) * 3584u + (lane & 3) * 16u, vbb = ((lane >> 2) ^ 1) * 3584u + (lane & 3) * 16u;
      const unsigned wv[4] = {va, vbb + 56320, va + 2 * 56320, vbb + 3 * 56320};
      const unsigned v2 = (lane >> 3) * 1024 + (lane & 7) * 16;
      const unsigned dv[4] = {v2, (v2 ^ 32) + 7168, v2 + 2 * 7168, (v2 ^ 32) + 3 * 7168};
      const size_t go = (size_t)kw * 128 * 3584 + tk * 64;
      const unsigned char* bd = wd + (size_t)tk * 32 * 1024 + kw * 256;
      flag_wait<1>(L0, F_VISS, 8u);
#pragma nounroll
      for (int j = 0; j < 2; ++j) {
        const int hh = (j ^ tk) & 1;
        dma4_nt<0>(wg + go + hh * 229376, wv, LK + hh * 16384);
        dma4_nt<0>(wu + go + hh * 229376, wv, LK + hh * 16384 + 4096);
      }
      dma4_nt<0>(bd, dv, LK + 8192);
      dma4_nt<0>(bd + 128, dv, LK + 16384 + 8192);
      TS(4);
      waitvm<16>();
      flag_set(L0, F_P0 + kw, 2u);
      waitvm<8>();
      flag_set(L0, F_P0 + kw, 3u);
      TS(5);
      waitvm<0>();
      TS(6);
    } else {
      const int nh = (16 - nC) * 4;
      flag_wait<1>(L0, F_S2, 2u);
#pragma nounroll
      for (int t = (ri - 16) * 4 + kw; t < 4 * nC + 64; t += nh) {
        if (t < 4 * nC) {
          touch(wd + (size_t)cc0 * 32768 + t * 8192, lane * 128, L0 + L_SCR);
        } else {
          const int u = t - 4 * nC, row = ((u >> 2) & 3) * 128 + ((u & 1) * 4 + 3 - (u >> 4)) * 16;
          if (2 * (lane & 3) < nC) touch(((u & 2) ? wu : wg) + (size_t)row * 3584 + cc0 * 64, (lane >> 2) * 3584 + (lane & 3) * 128, L0 + L_SCR);
        }
      }
      TS(4);
      waitvm<0>();
      TS(7);
    }
    asm volatile(".Lfk_ke:" ::: "memory");
    TSEND();
    return;
  }

  if (wave0 == 0) {
    const u32x4 xs0 = lds_ld<u32x4>(L0 + L_XS + lane * 16), xs1 = lds_ld<u32x4>(L0 + L_XS + 1024 + lane * 16);
    u32x4 xa[4];
#pragma unroll
    for (int t = 0; t < 4; ++t) xa[t] = lds_ld<u32x4>(L0 + L_XS + (n16 & 1) * 1024 + rq * 256 + t * 64 + g4 * 16);
    float mu, rr;
    {
      float s0 = 0.f, q0 = 0.f, s1 = 0.f, q1 = 0.f;
#pragma unroll
      for (int j = 0; j < 4; ++j) {
        const float a = bflo(xs0[j]), b = bfhi(xs0[j]), c = bflo(xs1[j]), d = bfhi(xs1[j]);
        s0 += a + b; q0 += a * a + b * b; s1 += c + d; q1 += c * c + d * d;
      }
      s0 = wave_sum(s0); q0 = wave_sum(q0); s1 = wave_sum(s1); q1 = wave_sum(q1);
      const float s = (lane & 1) ? s1 : s0, q = (lane & 1) ? q1 : q0;
      mu = s * (1.f / 512.f);
      rr = 1.f / sqrtf(fmaxf(q * (1.f / 512.f) - mu * mu, 0.f) + 1e-5f);
    }
#pragma unroll
    for (int t = 0; t < 4; ++t) xa[t] = (n16 & 3) == 2 ? ones : xa[t];
    flag_wait<0>(L0, F_WQ, 4u);
    TS(1);
    {
      f32x4 d[4];
      TRDEF();
      const unsigned a0 = L0 + L_WQ + (8 * g4 + trq) * 128 + trp * 8, sw = (g4 & 1) * 32;
#pragma unroll
      for (int nt = 0; nt < 4; ++nt) {
        d[nt] = f32x4{0.f, 0.f, 0.f, 0.f};
#pragma unroll
        for (int t = 0; t < 4; ++t) d[nt] = mfma(trfrag(a0 + t * 4096 + ((nt * 32) ^ sw), a0 + t * 4096 + ((nt * 32) ^ sw) + 512), xa[t], d[nt]);
      }
      const f32x4 lo = (n16 & 4) ? d[1] : d[0], hi = (n16 & 4) ? d[3] : d[2];
      const f32x4 dd = (n16 & 8) ? hi : lo;
      float qv[4];
#pragma unroll
      for (int r = 0; r < 4; ++r) qv[r] = rr * (dd[r] - mu * dpp<0xAA>(dd[r]));
      if ((n16 & 3) < 2)
        __builtin_amdgcn_raw_buffer_store_b128(u32x4{tlo, thi, pk(qv[0], qv[1]), pk(qv[2], qv[3])}, rsb,
                                               WS_QP + (((kvh * 2 + (n16 & 1)) * 4 + rq) * 128 + cbk * 16 + (n16 >> 2) * 4 + g4) * 16, 0, 16);
    }
    waitvm<1>();
    if (lane == 0) __builtin_amdgcn_raw_buffer_store_b64(u32x2{tlo, thi}, rsb, WS_FL + w * 8, 0, 16);
    TS(2);
    {
      u32x4 v[8];
      const unsigned ob = WS_QP + (kvh * 2 + bat) * 8192 + lane * 16;
#pragma unroll
      for (int i = 0; i < 8; ++i) v[i] = u32x4{~tlo, 0u, 0u, 0u};
      if (QDELAY) __builtin_amdgcn_s_sleep(QDELAY);
#pragma nounroll
      for (int spin = 0; spin < SPIN_G; ++spin) {
        u64 m[8];
#pragma unroll
        for (int i = 0; i < 8; ++i) m[i] = __ballot(v[i][0] != tlo || v[i][1] != thi);
        if ((m[0] | m[1] | m[2] | m[3] | m[4] | m[5] | m[6] | m[7]) == 0) break;
        poll_round8(rs, v, ob, m);
      }
#pragma unroll
      for (int h = 0; h < 2; ++h) {
        u32x2 r;
#pragma unroll
        for (int j = 0; j < 2; ++j) {
          const float lo = (bflo(v[h][2 + j]) + bflo(v[2 + h][2 + j])) + (bflo(v[4 + h][2 + j]) + bflo(v[6 + h][2 + j]));
          const float hi = (bfhi(v[h][2 + j]) + bfhi(v[2 + h][2 + j])) + (bfhi(v[4 + h][2 + j]) + bfhi(v[6 + h][2 + j]));
          r[j] = pk(lo, hi);
        }
        lds_st<u32x2>(L0 + L_Q + (64 * h + lane) * 8, r);
      }
      flag_set(L0, F_Q, 1u);
    }
    TS(3);
#pragma nounroll
    for (int spin = 0; spin < SPIN_G; ++spin) {
      asm volatile("" ::: "memory");
      const u32x4 f0 = __builtin_amdgcn_raw_buffer_load_b128(rsb, WS_FL + lane * 16, 0, 16);
      const u32x4 f1 = __builtin_amdgcn_raw_buffer_load_b128(rsb, WS_FL + 1024 + lane * 16, 0, 16);
      const bool ok = f0[0] == tlo && f0[1] == thi && f0[2] == tlo && f0[3] == thi && f1[0] == tlo && f1[1] == thi && f1[2] == tlo && f1[3] == thi;
      if (__all(ok)) break;
    }
    flag_set(L0, F_ALL, 1u);
    if (w == NWG - 1 && lane == 0) __hip_atomic_store((GAS u64*)(ws + WS_EPOCH), ((u64)thi << 32) | tlo, RLX_AGENT);
    TS(4);
    flag_wait<1>(L0, F_STAT, 4u);
    {
      const float m = lds_ld<float>(L0 + L_WM + n16 * 4), ls = lds_ld<float>(L0 + L_WL + n16 * 4);
      float M = fmaxf(m, dpp<ROR4>(m)); M = fmaxf(M, dpp<ROR8>(M));
      const float f = __builtin_amdgcn_exp2f(m - M);
      float L = f * ls;
      L += dpp<ROR4>(L); L += dpp<ROR8>(L);
      if (lane < 16) lds_st<float>(L0 + L_F + lane * 4, f);
      flag_set(L0, F_FAC, 1u);
      if (lane < 4)
        __builtin_amdgcn_raw_buffer_store_b64(u32x2{nosent(__float_as_uint(M)), nosent(__float_as_uint(L))}, rsb, WS_ST + (kvh * 32 + idx) * 32 + lane * 8, 0, 16);
    }
    TS(5);
    if (role == 0) {
      u32x4 st = {SENT, SENT, SENT, SENT};
      flag_wait<1>(L0, F_VISS, ST_AT);
      poll1(rs, st, WS_ST + kvh * 1024 + lane * 16);
      const float m0 = __uint_as_float(st[0]), l0 = __uint_as_float(st[1]), m1 = __uint_as_float(st[2]), l1 = __uint_as_float(st[3]);
      float M0 = m0, M1 = m1;
#pragma unroll
      for (int o2 = 2; o2 <= 16; o2 <<= 1) { M0 = fmaxf(M0, __shfl_xor(M0, o2)); M1 = fmaxf(M1, __shfl_xor(M1, o2)); }
      const float e0 = __builtin_amdgcn_exp2f(m0 - M0), e1 = __builtin_amdgcn_exp2f(m1 - M1);
      float S0 = e0 * l0, S1 = e1 * l1;
#pragma unroll
      for (int o2 = 2; o2 <= 16; o2 <<= 1) { S0 += __shfl_xor(S0, o2); S1 += __shfl_xor(S1, o2); }
      const int bb = lane >> 5, ss = (lane >> 1) & 15, h0 = (lane & 1) * 2;
      lds_st<float>(L0 + L_W + ((bb * 4 + h0) * 16 + ss) * 4, e0 / S0);
      lds_st<float>(L0 + L_W + ((bb * 4 + h0 + 1) * 16 + ss) * 4, e1 / S1);
      flag_set(L0, F_W, 1u);
      TS(6);
    }
    TSEND();
    return;
  }

  const int wave = wave0 - 5;
  const unsigned LW = L0 + wave * 16384;
  const unsigned char* vb = vc + chunk + wave * 4096;
  __builtin_amdgcn_s_setprio(2);
  if (wave == 0 && lane < TPN) touch(vb, lane * (131072 / TPN), L0 + L_SCR);
  if (TPF >= 2 && wave == 2 && lane < TPN) touch(kc + chunk, lane * (131072 / TPN), L0 + L_SCR);
  if (TPF >= 3 && wave == 1 && lane == 0) {
    if (role == 0) touch(wo + (size_t)rk * 262144, 0, L0 + L_SCR);
    if (role == 1) { touch(wg + tk * 64, 0, L0 + L_SCR); touch(wu + tk * 64, 0, L0 + L_SCR); touch(wd + (size_t)tk * 32768, 0, L0 + L_SCR); }
  }
  u32x4 msk;
  {
    const unsigned hm = (lane & 1) ? 0xffff0000u : 0x0000ffffu;
    const int dsel = (lane & 7) >> 1;
    msk = u32x4{dsel == 0 ? hm : 0u, dsel == 1 ? hm : 0u, dsel == 2 ? hm : 0u, dsel == 3 ? hm : 0u};
  }
  u32x4 V[16];
  const unsigned vo = lane * 16;
  flag_wait<1>(L0, F_KISS, 4u);
  TS(1);
#define LDV(i) ldv_nt<((i) & 3) * 1024>(V[i], vb + ((((i) >> 2) + rq) & 3) * 32768, vo)
  LDV(0); LDV(1); LDV(2); LDV(3); LDV(4); LDV(5); LDV(6); LDV(7); LDV(8); LDV(9); LDV(10); LDV(11);
  TS(2);
  flag_wait<1>(L0, F_P0 + (wave >> 1), 1u);
  u32x4 P[2][4];
#pragma unroll
  for (int u = 0; u < 2; ++u)
#pragma unroll
    for (int k = 0; k < 4; ++k) P[u][k] = lds_ld<u32x4>(L0 + L_P + wave * 1024 + ((2 * u + (n16 >> 3)) * 4 + g4) * 64 + k * 16);
  f32x4 acc[2] = {{0.f, 0.f, 0.f, 0.f}, {0.f, 0.f, 0.f, 0.f}};
  TS(3);
#define PV(j, N)                                                                        \
  do {                                                                                  \
    asm volatile("s_waitcnt vmcnt(%1)" : "+v"(V[j]) : "i"(N) : "memory");               \
    const u32x4 b0 = msk & P[0][(j) >> 2][(j) & 3], b1 = msk & P[1][(j) >> 2][(j) & 3]; \
    acc[0] = mfma(V[j], b0, acc[0]);                                                    \
    acc[1] = mfma(V[j], b1, acc[1]);                                                    \
    __builtin_amdgcn_sched_barrier(0);                                                  \
  } while (0)
  LDV(12); PV(0, 12); PV(1, 11);
  LDV(13); PV(2, 11); PV(3, 10);
  LDV(14); PV(4, 10); PV(5, 9);
  LDV(15); flag_add(L0, F_VISS); PV(6, 9); PV(7, 8);
  asm volatile("" : "+v"(acc[0]), "+v"(acc[1]));
  TS(4);
  flag_wait<0>(L0, F_FAC, 1u);
  unsigned oa = L0 + L_RB_O + wave * 1024 + lane * 16, og = WS_OP + prod * 1024 + (2 * wave + (n16 >> 3)) * 256 + g4 * 64 + (lane & 7) * 8;
  asm volatile("" : "+v"(oa), "+v"(og));
  const float f0 = lds_ld<float>(L0 + L_F + ((wave >> 1) * 4 + (n16 >> 3)) * 4), f1 = lds_ld<float>(L0 + L_F + ((wave >> 1) * 4 + 2 + (n16 >> 3)) * 4);
#pragma unroll
  for (int j = 8; j < 16; ++j) PV(j, 15 - j);
  TS(5);
  lds_st<f32x4>(LW + L_RB_O + lane * 16, acc[0] * f0);
  lds_st<f32x4>(LW + L_RB_O + 1024 + lane * 16, acc[1] * f1);
  flag_add(L0, F_S1);
  if (wave < 2) {
    flag_wait<0>(L0, F_S1, 8u);
    f32x4 r[8];
    lds_ld8_b128<16384>(r, oa, oa + 65536);
    const f32x4 s = ((r[0] + r[1]) + (r[2] + r[3])) + ((r[4] + r[5]) + (r[6] + r[7]));
    __builtin_amdgcn_raw_buffer_store_b64(u32x2{nosent(pk(s[0], s[1])), nosent(pk(s[2], s[3]))}, rsb, og, 0, 16);
    flag_add(L0, F_S2);
  }
  TS(6);
  if (role == 3) { TSEND(); return; }
  const u32x4 sel = msk & ones;
  TRDEF();

  if (role == 0) {
    const int kq = wave >> 1, bb = wave & 1;
    const int hd = 2 * (rk & 1) + (kq >> 1);
    const unsigned LB = L0 + kq * 32768;
    u32x4 A0, A1;
    const unsigned o0 = WS_OP + ((bb * 8 + (rk >> 1)) * 16 + (lane >> 3)) * 1024 + hd * 256 + (kq & 1) * 128 + (lane & 7) * 16;
    f32x4 xr0 = {0.f, 0.f, 0.f, 0.f}, xr1 = {0.f, 0.f, 0.f, 0.f};
    if (wave == 0 && rk == 0) {
#pragma unroll
      for (int j = 0; j < 4; ++j) {
        xr0[j] = bflo(lds_ld<unsigned short>(L0 + L_XS + (cq * 64 + j * 16 + n16) * 2));
        xr1[j] = bflo(lds_ld<unsigned short>(L0 + L_XS + (512 + cq * 64 + j * 16 + n16) * 2));
      }
    }
    flag_wait<0>(L0, F_W, 1u);
    const float wa = lds_ld<float>(L0 + L_W + ((bb * 4 + hd) * 16 + 2 * g4 + (n16 >> 3)) * 4), wb = lds_ld<float>(L0 + L_W + ((bb * 4 + hd) * 16 + 8 + 2 * g4 + (n16 >> 3)) * 4);
    u32x4 sa = msk & pk(wa, wa), sb = msk & pk(wb, wb);
    unsigned sw = LW + 8192 + n16 * 16 + (g4 & 1) * 8, sr = LW + 8192 + (n16 & 1) * 128 + g4 * 16, xw = L0 + L_X + wave * 256 + n16 * 16, xa = L0 + L_X + n16 * 16,
             og2 = WS_YP + rk * 2048 + cq * 256 + n16 * 16;
    const u64 vmask = __ballot((g4 >> 1) == (n16 >> 3));
    asm volatile("" : "+v"(sw), "+v"(sr), "+v"(xw), "+v"(xa), "+v"(og2), "+v"(xr0), "+v"(xr1), "+v"(sa), "+v"(sb));
    u32x4 Bw[2][4];
#pragma unroll
    for (int t = 0; t < 2; ++t) {
      if (!EL || t == 1) flag_wait<0>(L0, F_P0 + kq, 2u + t);
      if (t == (EL ? 0 : 1))
        asm volatile("buffer_load_dwordx4 %0, %2, %3, 0 offen sc1\n\tbuffer_load_dwordx4 %1, %2, %3, %4 offen sc1"
                     : "=&v"(A0), "=&v"(A1) : "v"(o0), "s"(rs), "s"(8192u) : "memory");
      if (EL && t == 0) flag_wait<0>(L0, F_P0 + kq, 2u);
#pragma unroll
      for (int nt = 0; nt < 4; ++nt) {
        const unsigned a = LB + (32 * t + 8 * trq + 2 * g4) * 128 + ((nt ^ g4) * 32) + trp * 8;
        Bw[t][nt] = trfrag(a, a + 128);
      }
    }
    TS(7);
    asm volatile("s_waitcnt vmcnt(0)" : "+v"(A0), "+v"(A1)::"memory");
    poll2(rs, A0, A1, o0, 8192u);
    TS(8);
    f32x4 d = mfma(sa, A0, f32x4{0.f, 0.f, 0.f, 0.f});
    d = mfma(sb, A1, d);
    asm volatile("s_mov_b64 exec, %2\n\tds_write_b64 %0, %1\n\ts_mov_b64 exec, -1" ::"v"(sw), "v"(u32x2{pk(d[0], d[1]), pk(d[2], d[3])}), "s"(vmask) : "memory");
    A0 = lds_ld<u32x4>(sr);
    A1 = lds_ld<u32x4>(sr + 64);
    f32x4 val;
#pragma unroll
    for (int nt = 0; nt < 4; ++nt) {
      f32x4 e = mfma(A0, Bw[0][nt], f32x4{0.f, 0.f, 0.f, 0.f});
      e = mfma(A1, Bw[1][nt], e);
      val[nt] = e[0] + e[1];
    }
    asm volatile("s_mov_b64 exec, 0xffff\n\tds_write_b128 %0, %1\n\ts_mov_b64 exec, -1" ::"v"(xw), "v"(val) : "memory");
    wg_barrier();
    if (wave == 0) {
      f32x4 r[8];
      lds_ld8_b128<256>(r, xa, xa + 1024);
      xr0 += (r[0] + r[2]) + (r[4] + r[6]);
      xr1 += (r[1] + r[3]) + (r[5] + r[7]);
      if (lane < 16)
        __builtin_amdgcn_raw_buffer_store_b128(u32x4{nosent(pk(xr0[0], xr0[1])), nosent(pk(xr0[2], xr0[3])), nosent(pk(xr1[0], xr1[1])), nosent(pk(xr1[2], xr1[3]))},
                                               rsb, og2, 0, 16);
      asm volatile(".Lfk_e0:" ::: "memory");
      TS(9);
      FK_END();
    }
  } else if (role == 1) {
    u32x4 H[4];
    const unsigned o = WS_YP + g4 * 2048 + wave * 256 + n16 * 16;
    flag_wait<1>(L0, F_ALL, 1u);
    flag_wait<1>(L0, F_P0 + (wave >> 1), 2u + ((wave ^ tk) & 1));
    issue4(rs, H, o, 8192u);
    u32x4 Bw[2][2][2];
#pragma unroll
    for (int mt = 0; mt < 2; ++mt)
#pragma unroll
      for (int t = 0; t < 2; ++t)
#pragma unroll
        for (int nt = 0; nt < 2; ++nt) {
          const unsigned a = LW + mt * 4096 + (16 * trq + 8 * t + 2 * g4 + (trq & 1)) * 64 + nt * 32 + trp * 8;
          Bw[mt][t][nt] = trfrag(a, a ^ 64);
        }
    unsigned xw = L0 + L_X + wave * 512 + lane * 8, xa = L0 + L_P + lane * 8;
    {
      f32x4 c[2][2];
#pragma unroll
      for (int nt = 0; nt < 2; ++nt)
#pragma unroll
        for (int mt = 0; mt < 2; ++mt) {
          c[nt][mt] = mfma(ones, Bw[mt][0][nt], f32x4{0.f, 0.f, 0.f, 0.f});
          c[nt][mt] = mfma(ones, Bw[mt][1][nt], c[nt][mt]);
        }
      lds_st<f32x2>(L0 + L_P + wave * 1024 + lane * 8, lane < 32 ? f32x2{c[0][0][0], c[0][1][0]} : f32x2{c[1][0][0], c[1][1][0]});
    }
    unsigned hw = LW + lane * 8, hr = LW + ((n16 >> 2) & 1) * 128 + g4 * 16, stw = L0 + L_X + 4096 + wave * 16 + g4 * 8, str = L0 + L_X + 4096 + (n16 & 7) * 16 + (g4 & 1) * 8,
             aw = LW + 256 + (g4 & 1) * 64 + (g4 >> 1) * 32 + n16 * 2, ar = LW + 256 + (n16 & 1) * 64 + g4 * 16, og2 = WS_YP + (16 + tk) * 2048 + wave * 256 + n16 * 16;
    asm volatile("" : "+v"(hw), "+v"(hr), "+v"(stw), "+v"(str), "+v"(aw), "+v"(ar), "+v"(og2), "+v"(xw), "+v"(xa));
    unsigned da = LW + 8192 + (8 * g4 + trq) * 128 + (g4 & 1) * 32 + trp * 8;
    unsigned db = da ^ 32;
    asm volatile("" : "+v"(da), "+v"(db));
    TS(7);
    land4(H);
    poll4(rs, H, o, 8192u);
    TS(8);
    f32x4 h = mfma(sel, H[0], f32x4{0.f, 0.f, 0.f, 0.f}), h2 = mfma(sel, H[1], f32x4{0.f, 0.f, 0.f, 0.f});
    h = mfma(sel, H[2], h);
    h2 = mfma(sel, H[3], h2);
    h += h2;
    asm volatile("s_mov_b32 exec_hi, 0\n\tds_write_b64 %0, %1\n\ts_mov_b32 exec_hi, -1" ::"v"(hw), "v"(u32x2{pk(h[0], h[1]), pk(h[2], h[3])}) : "memory");
    const u32x4 A0 = lds_ld<u32x4>(hr), A1 = lds_ld<u32x4>(hr + 64);
    const float ss = row_sum16((h[0] + h[1]) + (h[2] + h[3])), gs = row_sum16((h[0] * h[0] + h[1] * h[1]) + (h[2] * h[2] + h[3] * h[3]));
    asm volatile("s_mov_b64 exec, 0x10001\n\tds_write_b64 %0, %1\n\ts_mov_b64 exec, -1" ::"v"(stw), "v"(f32x2{ss, gs}) : "memory");
    f32x4 e[2][2];
#pragma unroll
    for (int nt = 0; nt < 2; ++nt)
#pragma unroll
      for (int mt = 0; mt < 2; ++mt) {
        e[nt][mt] = mfma(A0, Bw[mt][0][nt], f32x4{0.f, 0.f, 0.f, 0.f});
        e[nt][mt] = mfma(A1, Bw[mt][1][nt], e[nt][mt]);
      }
    lds_st<f32x2>(xw, lane < 32 ? f32x2{e[0][0][0], e[0][1][0]} : f32x2{e[1][0][0], e[1][1][0]});
    wg_barrier();
    f32x2 st = lds_ld<f32x2>(str);
    f32x2 r[8];
    f32x2 cs[8];
    lds_ld16_b64(r, cs, xa);
    u32x4 Bd[4];
#pragma unroll
    for (int j = 0; j < 4; ++j) Bd[j] = trfrag(((j & 1) ? db : da) + (j >> 1) * 64, ((j & 1) ? db : da) + (j >> 1) * 64 + 512);
    __builtin_amdgcn_sched_barrier(0);
    const f32x2 csum = ((cs[0] + cs[1]) + (cs[2] + cs[3])) + ((cs[4] + cs[5]) + (cs[6] + cs[7]));
    st[0] += dpp<XOR1>(st[0]); st[1] += dpp<XOR1>(st[1]);
    st[0] += dpp<XOR2>(st[0]); st[1] += dpp<XOR2>(st[1]);
    st[0] += dpp<XOR7>(st[0]); st[1] += dpp<XOR7>(st[1]);
    const f32x2 gu = ((r[0] + r[1]) + (r[2] + r[3])) + ((r[4] + r[5]) + (r[6] + r[7]));
    const float mu = st[0] * (1.f / 512.f);
    const float rr = __builtin_amdgcn_rsqf(fmaxf(st[1] * (1.f / 512.f) - mu * mu, 0.f) + 1e-5f);
    const float gg = rr * (gu[0] - mu * csum[0]), uu = rr * (gu[1] - mu * csum[1]);
    const float a = gg * uu * __builtin_amdgcn_rcpf(1.f + __builtin_amdgcn_exp2f(-1.4426950408889634f * gg));
    lds_st<__bf16>(aw, (__bf16)a);
    const u32x4 Aa = lds_ld<u32x4>(ar);
    f32x4 y[4];
#pragma unroll
    for (int j = 0; j < 4; ++j) y[j] = mfma(Aa, Bd[j], f32x4{0.f, 0.f, 0.f, 0.f});
    if (lane < 16)
      __builtin_amdgcn_raw_buffer_store_b128(
          u32x4{nosent(pk(y[0][0], y[1][0])), nosent(pk(y[2][0], y[3][0])), nosent(pk(y[0][1], y[1][1])), nosent(pk(y[2][1], y[3][1]))}, rsb, og2, 0, 16);
    asm volatile(".Lfk_e1:" ::: "memory");
    TS(9);
    FK_END();
  } else if (wave == 2) {
    const int ee = 4 * kvh + ri - 16;
    u32x4 Y0 = {SENT, SENT, SENT, SENT}, Y[4];
    const unsigned ob = WS_YP + (lane >> 2) * 2048 + ee * 64 + (lane & 3) * 16;
#pragma unroll
    for (int i = 0; i < 4; ++i) Y[i] = (i < 3 || lane < 32) ? u32x4{SENT, SENT, SENT, SENT} : u32x4{0u, 0u, 0u, 0u};
    flag_wait<1>(L0, F_ALL, 1u);
#pragma nounroll
    for (int spin = 0; spin < SPIN_G; ++spin) {
      const u64 m = __ballot(pend4(Y0));
      if (m == 0) break;
      __builtin_amdgcn_s_sleep(4);
      poll_round1(rs, Y0, ob, m);
    }
    f32x4 d0 = mfma(sel, Y0, f32x4{0.f, 0.f, 0.f, 0.f});
    TS(7);
    poll4(rs, Y, ob + 32768u, 32768u);
    TS(8);
    f32x4 d1 = mfma(sel, Y[1], f32x4{0.f, 0.f, 0.f, 0.f});
    d0 = mfma(sel, Y[0], d0);
    d1 = mfma(sel, Y[3], d1);
    d0 = mfma(sel, Y[2], d0);
    f32x4 d = d0 + d1;
#pragma unroll
    for (int r = 0; r < 4; ++r) { d[r] += dpp<ROR4>(d[r]); d[r] += dpp<ROR8>(d[r]); }
    const float lo = (n16 & 4) ? d[1] : d[0], hi = (n16 & 4) ? d[3] : d[2];
    const float res = (n16 & 8) ? hi : lo;
    const float nb = dpp<XOR1>(res);
    if (lane < 32 && (lane & 1) == 0)
      *(unsigned*)(out + g4 * 512 + 64 * (ee >> 2) + 16 * (n16 >> 2) + 4 * (ee & 3) + (n16 & 3)) = pk(res, nb);
    asm volatile(".Lfk_e2:" ::: "memory");
    TS(9);
    FK_END();
  }
  asm volatile(".Lfk_te:" ::: "memory");
  TSEND();
}
}

extern "C" void kernel_launch(void* const* d_in, const int* in_sizes, int n_in, void* d_out, int out_size, void* d_ws, size_t ws_size,
                              hipStream_t stream) {
  (void)in_sizes; (void)n_in; (void)out_size; (void)ws_size;
  (void)hipFuncSetAttribute((const void*)fk::fused_kernel, hipFuncAttributeMaxDynamicSharedMemorySize, fk::L_END);
  fk::fused_kernel<<<fk::NWG, fk::NTHR, fk::L_END, stream>>>(
      (const unsigned char*)d_in[3], (const unsigned char*)d_in[1], (unsigned char*)d_ws, (const unsigned char*)d_in[0],
      (const unsigned char*)d_in[2], (const unsigned char*)d_in[4], (const unsigned char*)d_in[6], (const unsigned char*)d_in[7],
      (const unsigned char*)d_in[8], (unsigned short*)d_out);
}
```

```cpp
#include <hip/hip_runtime.h>
#include <hip/hip_bf16.h>
#include <stdint.h>

namespace fk {
typedef unsigned u32x4 __attribute__((ext_vector_type(4)));
typedef unsigned u32x2 __attribute__((ext_vector_type(2)));
typedef float f32x4 __attribute__((ext_vector_type(4)));
typedef float f32x2 __attribute__((ext_vector_type(2)));
typedef short s16x4 __attribute__((ext_vector_type(4)));
typedef __bf16 bf16x8 __attribute__((ext_vector_type(8)));
typedef __bf16 bf16x2 __attribute__((ext_vector_type(2)));
typedef unsigned long long u64;
#define LAS __attribute__((address_space(3)))
#define GAS __attribute__((address_space(1)))
#define RLX_AGENT __ATOMIC_RELAXED, __HIP_MEMORY_SCOPE_AGENT

#define FK_TS 0
constexpr int KW = 6;
constexpr int VW = 4;
constexpr int QDELAY = 40;
constexpr unsigned SENT = 0xFFFFFFFFu;
constexpr int NWG = 256, NWAVES = 13, NTHR = NWAVES * 64;
constexpr int WS_EPOCH = 0;
constexpr int WS_FL = 4096;
constexpr int WS_QP = 8192;
constexpr int WS_ST = WS_QP + 131072;
constexpr int WS_OP = WS_ST + 8192;
constexpr int WS_YP = WS_OP + 262144;
constexpr int WS_END = WS_YP + 72 * 2048;
constexpr int WS_TS = 1 << 20;
constexpr int L_RB_O = 12288;
constexpr int L_WQ = 131072, L_P = L_WQ  , L_X = L_WQ + 8192  , L_Q = L_WQ + 16384, L_WM = L_Q + 1024, L_WL = L_WM + 64, L_F = L_WL + 64,
              L_W = L_F + 64, L_FLAGS = L_W + 512, L_SCR = L_FLAGS + 64, L_XS = L_SCR + 256, L_END = L_XS + 2048;
enum { F_Q = 0, F_STAT = 1, F_FAC = 2, F_VISS = 3, F_WQ = 4, F_KISS = 5, F_S1 = 6, F_S2 = 7, F_ALL = 9, F_W = 10, F_P0 = 12   };
constexpr int SPIN_G = 1 << 18;
constexpr int SPIN_L = 1 << 22;
constexpr float QSCALE = 0.088388347648318440f * 1.4426950408889634f;
constexpr unsigned ONE2 = 0x3F803F80u;

__device__ __forceinline__ f32x4 mfma(u32x4 a, u32x4 b, f32x4 c) {
  return __builtin_amdgcn_mfma_f32_16x16x32_bf16(__builtin_bit_cast(bf16x8, a), __builtin_bit_cast(bf16x8, b), c, 0, 0, 0);
}
__device__ __forceinline__ unsigned pk(float lo, float hi) { bf16x2 v = {(__bf16)lo, (__bf16)hi}; return __builtin_bit_cast(unsigned, v); }
__device__ __forceinline__ unsigned nosent(unsigned x) { return x < 0xFFFFFFFEu ? x : 0xFFFFFFFEu; }
__device__ __forceinline__ float bflo(unsigned u) { return __builtin_bit_cast(float, u << 16); }
__device__ __forceinline__ float bfhi(unsigned u) { return __builtin_bit_cast(float, u & 0xffff0000u); }
__device__ __forceinline__ float x16_sum(float x) {
  auto s = __builtin_amdgcn_permlane16_swap(__float_as_uint(x), __float_as_uint(x), false, false);
  return __uint_as_float(s[0]) + __uint_as_float(s[1]);
}
__device__ __forceinline__ float x32_sum(float x) {
  auto s = __builtin_amdgcn_permlane32_swap(__float_as_uint(x), __float_as_uint(x), false, false);
  return __uint_as_float(s[0]) + __uint_as_float(s[1]);
}
__device__ __forceinline__ float x16_max(float x) {
  auto s = __builtin_amdgcn_permlane16_swap(__float_as_uint(x), __float_as_uint(x), false, false);
  return fmaxf(__uint_as_float(s[0]), __uint_as_float(s[1]));
}
__device__ __forceinline__ float x32_max(float x) {
  auto s = __builtin_amdgcn_permlane32_swap(__float_as_uint(x), __float_as_uint(x), false, false);
  return fmaxf(__uint_as_float(s[0]), __uint_as_float(s[1]));
}
template <int CTRL> __device__ __forceinline__ float dpp(float x) {
  return __builtin_bit_cast(float, __builtin_amdgcn_mov_dpp(__builtin_bit_cast(int, x), CTRL, 0xf, 0xf, true));
}
constexpr int XOR1 = 0xB1, XOR2 = 0x4E, XOR7 = 0x141, ROR4 = 0x124, ROR8 = 0x128;
__device__ __forceinline__ float row_sum16(float x) { x += dpp<XOR1>(x); x += dpp<XOR2>(x); x += dpp<XOR7>(x); x += dpp<ROR8>(x); return x; }
__device__ __forceinline__ float wave_sum(float x) { return x32_sum(x16_sum(row_sum16(x))); }

template <int N> __device__ __forceinline__ void waitvm() { asm volatile("s_waitcnt vmcnt(%0)" ::"i"(N) : "memory"); }
template <int W> __device__ __forceinline__ void dma4_nt(const void* sbase, const unsigned (&v)[4], unsigned ldst) {
  if constexpr (W == 0)
    asm volatile("s_mov_b32 m0, %5\n\ts_nop 0\n\tglobal_load_lds_dwordx4 %0, %4 nt\n\tglobal_load_lds_dwordx4 %1, %4 offset:1024 nt\n\t"
                 "global_load_lds_dwordx4 %2, %4 offset:2048 nt\n\tglobal_load_lds_dwordx4 %3, %4 offset:3072 nt"
                 ::"v"(v[0]), "v"(v[1]), "v"(v[2]), "v"(v[3]), "s"(sbase), "s"(ldst) : "memory");
  else
    asm volatile("s_mov_b32 m0, %5\n\ts_waitcnt vmcnt(%6)\n\tglobal_load_lds_dwordx4 %0, %4 nt\n\ts_waitcnt vmcnt(%6)\n\tglobal_load_lds_dwordx4 %1, %4 offset:1024 nt\n\t"
                 "s_waitcnt vmcnt(%6)\n\tglobal_load_lds_dwordx4 %2, %4 offset:2048 nt\n\ts_waitcnt vmcnt(%6)\n\tglobal_load_lds_dwordx4 %3, %4 offset:3072 nt"
                 ::"v"(v[0]), "v"(v[1]), "v"(v[2]), "v"(v[3]), "s"(sbase), "s"(ldst), "i"(W - 1) : "memory");
}
__device__ __forceinline__ void touch(const void* sbase, unsigned voff, unsigned ldst) {
  asm volatile("s_mov_b32 m0, %2\n\ts_nop 0\n\tglobal_load_lds_dword %0, %1" ::"v"(voff), "s"(sbase), "s"(ldst) : "memory");
}
template <int OFF> __device__ __forceinline__ void ldv_nt(u32x4& dst, const void* sbase, unsigned voff) {
  asm volatile("global_load_dwordx4 %0, %1, %2 offset:%3 nt" : "=v"(dst) : "v"(voff), "s"(sbase), "i"(OFF) : "memory");
}
__device__ __forceinline__ u32x2 trread(unsigned addr) {
  return __builtin_bit_cast(u32x2, __builtin_amdgcn_ds_read_tr16_b64_v4i16((LAS s16x4*)(uintptr_t)addr));
}
__device__ __forceinline__ u32x4 trfrag(unsigned a0, unsigned a1) { u32x2 lo = trread(a0), hi = trread(a1); return u32x4{lo[0], lo[1], hi[0], hi[1]}; }
template <typename T> __device__ __forceinline__ T lds_ld(unsigned addr) { return *(LAS T*)(uintptr_t)addr; }
template <typename T> __device__ __forceinline__ void lds_st(unsigned addr, T v) { *(LAS T*)(uintptr_t)addr = v; }
__device__ __forceinline__ void flag_set(unsigned base, int f, unsigned v) {
  asm volatile("s_mov_b64 exec, 1\n\tds_write_b32 %0, %1\n\ts_mov_b64 exec, -1" ::"v"(base + L_FLAGS + f * 4), "v"(v) : "memory");
}
__device__ __forceinline__ void flag_add(unsigned base, int f) {
  asm volatile("s_mov_b64 exec, 1\n\tds_add_u32 %0, %1\n\ts_mov_b64 exec, -1" ::"v"(base + L_FLAGS + f * 4), "v"(1u) : "memory");
}
__device__ __forceinline__ void wg_barrier() { asm volatile("s_waitcnt lgkmcnt(0)\n\ts_barrier" ::: "memory"); }
template <int ST> __device__ __forceinline__ void lds_ld8_b128(f32x4 (&r)[8], unsigned a, unsigned b) {
  asm volatile("ds_read_b128 %0, %8\n\tds_read_b128 %1, %8 offset:%10\n\tds_read_b128 %2, %8 offset:%11\n\tds_read_b128 %3, %8 offset:%12\n\t"
               "ds_read_b128 %4, %9\n\tds_read_b128 %5, %9 offset:%10\n\tds_read_b128 %6, %9 offset:%11\n\tds_read_b128 %7, %9 offset:%12\n\t"
               "s_waitcnt lgkmcnt(0)"
               : "=&v"(r[0]), "=&v"(r[1]), "=&v"(r[2]), "=&v"(r[3]), "=&v"(r[4]), "=&v"(r[5]), "=&v"(r[6]), "=&v"(r[7])
               : "v"(a), "v"(b), "i"(ST), "i"(2 * ST), "i"(3 * ST) : "memory");
}
template <int SLP> __device__ __forceinline__ void flag_wait(unsigned base, int f, unsigned v) {
  asm volatile("" ::: "memory");
#pragma nounroll
  for (int i = 0; i < SPIN_L; ++i) {
    unsigned x = *(volatile LAS unsigned*)(uintptr_t)(base + L_FLAGS + f * 4);
    if ((unsigned)__builtin_amdgcn_readfirstlane(x) >= v) break;
    if (SLP) __builtin_amdgcn_s_sleep(SLP);
  }
  asm volatile("" ::: "memory");
}
__device__ __forceinline__ bool pend4(u32x4 v) {
  unsigned a = v[0] > v[1] ? v[0] : v[1], b = v[2] > v[3] ? v[2] : v[3];
  return (a > b ? a : b) == SENT;
}
__device__ __forceinline__ void poll_round1(u32x4 rs, u32x4& v0, unsigned o0, u64 m0) {
  asm volatile("s_mov_b64 exec, %3\n\tbuffer_load_dwordx4 %0, %1, %2, 0 offen sc1\n\t"
               "s_mov_b64 exec, -1\n\ts_waitcnt vmcnt(0)"
               : "+v"(v0) : "v"(o0), "s"(rs), "s"(m0) : "memory");
}
__device__ __forceinline__ void poll_round8(u32x4 rs, u32x4 (&v)[8], unsigned o, const u64 (&m)[8]) {
  asm volatile("s_mov_b64 exec, %10\n\tbuffer_load_dwordx4 %0, %8, %9, 0 offen sc1\n\t"
               "s_mov_b64 exec, %11\n\tbuffer_load_dwordx4 %1, %8, %9, 0 offen offset:1024 sc1\n\t"
               "s_mov_b64 exec, %12\n\tbuffer_load_dwordx4 %2, %8, %9, 0 offen offset:2048 sc1\n\t"
               "s_mov_b64 exec, %13\n\tbuffer_load_dwordx4 %3, %8, %9, 0 offen offset:3072 sc1\n\t"
               "s_mov_b64 exec, %14\n\tbuffer_load_dwordx4 %4, %8, %9, %18 offen sc1\n\t"
               "s_mov_b64 exec, %15\n\tbuffer_load_dwordx4 %5, %8, %9, %18 offen offset:1024 sc1\n\t"
               "s_mov_b64 exec, %16\n\tbuffer_load_dwordx4 %6, %8, %9, %18 offen offset:2048 sc1\n\t"
               "s_mov_b64 exec, %17\n\tbuffer_load_dwordx4 %7, %8, %9, %18 offen offset:3072 sc1\n\t"
               "s_mov_b64 exec, -1\n\ts_waitcnt vmcnt(0)"
               : "+v"(v[0]), "+v"(v[1]), "+v"(v[2]), "+v"(v[3]), "+v"(v[4]), "+v"(v[5]), "+v"(v[6]), "+v"(v[7])
               : "v"(o), "s"(rs), "s"(m[0]), "s"(m[1]), "s"(m[2]), "s"(m[3]), "s"(m[4]), "s"(m[5]), "s"(m[6]), "s"(m[7]), "s"(4096u) : "memory");
}
__device__ __forceinline__ void issue4(u32x4 rs, u32x4 (&v)[4], unsigned o, unsigned st) {
  asm volatile("buffer_load_dwordx4 %0, %4, %5, 0 offen sc1\n\tbuffer_load_dwordx4 %1, %4, %5, %6 offen sc1\n\t"
               "buffer_load_dwordx4 %2, %4, %5, %7 offen sc1\n\tbuffer_load_dwordx4 %3, %4, %5, %8 offen sc1"
               : "=&v"(v[0]), "=&v"(v[1]), "=&v"(v[2]), "=&v"(v[3]) : "v"(o), "s"(rs), "s"(st), "s"(2 * st), "s"(3 * st) : "memory");
}
__device__ __forceinline__ void land4(u32x4 (&v)[4]) {
  asm volatile("s_waitcnt vmcnt(0)" : "+v"(v[0]), "+v"(v[1]), "+v"(v[2]), "+v"(v[3])::"memory");
}
__device__ __forceinline__ void poll2(u32x4 rs, u32x4& A0, u32x4& A1, unsigned o, unsigned st) {
#pragma nounroll
  for (int spin = 0; spin < SPIN_G; ++spin) {
    const u64 m0 = __ballot(pend4(A0)), m1 = __ballot(pend4(A1));
    if ((m0 | m1) == 0) break;
    if (__builtin_popcountll(m0) + __builtin_popcountll(m1) > 24) __builtin_amdgcn_s_sleep(16);
    asm volatile("s_mov_b64 exec, %5\n\tbuffer_load_dwordx4 %0, %2, %3, 0 offen sc1\n\t"
                 "s_mov_b64 exec, %6\n\tbuffer_load_dwordx4 %1, %2, %3, %4 offen sc1\n\t"
                 "s_mov_b64 exec, -1\n\ts_waitcnt vmcnt(0)"
                 : "+v"(A0), "+v"(A1) : "v"(o), "s"(rs), "s"(st), "s"(m0), "s"(m1) : "memory");
  }
}
__device__ __forceinline__ void poll4(u32x4 rs, u32x4 (&A)[4], unsigned o, unsigned st) {
#pragma nounroll
  for (int spin = 0; spin < SPIN_G; ++spin) {
    u64 m[4];
#pragma unroll
    for (int i = 0; i < 4; ++i) m[i] = __ballot(pend4(A[i]));
    if ((m[0] | m[1] | m[2] | m[3]) == 0) break;
    asm volatile("s_mov_b64 exec, %9\n\tbuffer_load_dwordx4 %0, %4, %5, 0 offen sc1\n\t"
                 "s_mov_b64 exec, %10\n\tbuffer_load_dwordx4 %1, %4, %5, %6 offen sc1\n\t"
                 "s_mov_b64 exec, %11\n\tbuffer_load_dwordx4 %2, %4, %5, %7 offen sc1\n\t"
                 "s_mov_b64 exec, %12\n\tbuffer_load_dwordx4 %3, %4, %5, %8 offen sc1\n\t"
                 "s_mov_b64 exec, -1\n\ts_waitcnt vmcnt(0)"
                 : "+v"(A[0]), "+v"(A[1]), "+v"(A[2]), "+v"(A[3])
                 : "v"(o), "s"(rs), "s"(st), "s"(2 * st), "s"(3 * st), "s"(m[0]), "s"(m[1]), "s"(m[2]), "s"(m[3]) : "memory");
  }
}
__device__ __forceinline__ void poll1(u32x4 rs, u32x4& A, unsigned o) {
#pragma nounroll
  for (int spin = 0; spin < SPIN_G; ++spin) {
    const u64 m = __ballot(pend4(A));
    if (m == 0) break;
    poll_round1(rs, A, o, m);
  }
}

#define FK_LAST ".Lfk_ke"
#define FK_END() asm volatile("s_endpgm" ::: "memory")
#define TS(k) do {} while (0)
#define TSEND() do {} while (0)

__global__ void __launch_bounds__(NTHR, 4)
fused_kernel(const unsigned char* wq_, const unsigned char* kc_, unsigned char* ws_, const unsigned char* x_,
             const unsigned char* __restrict__ vc, const unsigned char* __restrict__ wo, const unsigned char* __restrict__ wg,
             const unsigned char* __restrict__ wu, const unsigned char* __restrict__ wd, unsigned short* __restrict__ out) {
  typedef unsigned long long u64x4 __attribute__((ext_vector_type(4)));
  u64x4 ka;
  asm volatile(".Lfk_start:\n\ts_load_dwordx8 %0, %1, 0x0" : "=s"(ka) : "s"(__builtin_amdgcn_kernarg_segment_ptr()) : "memory");
  extern __shared__ __attribute__((aligned(16))) unsigned char smem[];
  const unsigned L0 = (unsigned)(uintptr_t)(LAS unsigned char*)smem;
  const int tid = threadIdx.x, lane = tid & 63, wave0 = __builtin_amdgcn_readfirstlane(tid >> 6);
  const int w = blockIdx.x, kvh = w & 7, idx = w >> 3, bat = idx >> 4, spl = idx & 15;
  const int n16 = lane & 15, g4 = lane >> 4;
  const int rq = idx & 3, cbk = idx >> 2;
  const size_t chunk = ((size_t)(bat * 8 + kvh) * 8192 + spl * 512) * 256;
  const int kw = wave0 - 1;
  const bool isk = (unsigned)kw < 4u;
  if ((w & 56) == 0) {
    u64 pc;
    unsigned o;
    asm volatile("s_getpc_b64 %0\n\t.Lfk_h2:\n\tv_lshlrev_b32 %1, 5, %2\n\tv_min_u32 %1, " FK_LAST "-.Lfk_h2-4, %1\n\ts_mov_b32 m0, %3\n\ts_nop 0\n\t"
                 "global_load_lds_dword %1, %0" : "=&s"(pc), "=&v"(o) : "v"(tid), "s"(L0 + L_SCR) : "memory");
  }
  asm volatile("s_waitcnt lgkmcnt(0)" : "+s"(ka)::"memory");
  const unsigned char* wq = (const unsigned char*)ka[0];
  const unsigned char* kc = (const unsigned char*)ka[1];
  unsigned char* ws = (unsigned char*)ka[2];
  const unsigned char* x = (const unsigned char*)ka[3];
  const unsigned LK = L0 + kw * 32768;
  const unsigned char* kb = kc + chunk + kw * 8192;
#define KVODEF()                  \
  int lane_k = lane;              \
  asm volatile("" : "+v"(lane_k)); \
  unsigned kvo[4];                \
  _Pragma("unroll") for (int a = 0; a < 4; ++a) kvo[a] = (lane_k >> 4) * 256 + (((lane_k & 15) ^ (4 * (lane_k >> 4) + a)) * 16)
  if (isk) {
    const unsigned v0 = (lane >> 3) * 8192 + (lane & 7) * 16;
    const unsigned wv[4] = {v0, (v0 ^ 32) + 64512, v0 + 2 * 64512, (v0 ^ 32) + 3 * 64512};
    dma4_nt<0>(wq + (size_t)(rq * 128 + kw * 32) * 8192 + (kvh * 512 + cbk * 64) * 2, wv, L0 + L_WQ + kw * 4096);
    KVODEF();
    dma4_nt<0>(kb + rq * 32768, kvo, LK + rq * 8192);
  }
  const int prod = (bat * 8 + kvh) * 16 + spl;
  const u32x4 rs = {(unsigned)(uintptr_t)ws, (unsigned)((uintptr_t)ws >> 32) & 0xffffu, (unsigned)WS_END, 0x00020000u};
  unsigned tlo = 0, thi = 0;
  if (wave0 == 0) {
    __builtin_amdgcn_s_setprio(3);
    u32x2 ep;
    asm volatile("global_load_dwordx2 %0, %1, %2 sc1" : "=v"(ep) : "v"(0u), "s"(ws) : "memory");
    asm volatile("s_mov_b32 m0, %2\n\ts_nop 0\n\tglobal_load_lds_dwordx4 %0, %1\n\tglobal_load_lds_dwordx4 %0, %1 offset:1024" ::"v"(lane * 16), "s"(x), "s"(L0 + L_XS) : "memory");
    if (lane < 16) lds_st<unsigned>(L0 + L_FLAGS + lane * 4, 0u);
    asm volatile("s_waitcnt lgkmcnt(0)\n\ts_barrier" ::: "memory");
    const u32x4 s4 = {SENT, SENT, SENT, SENT};
    const int OOB = 0x7ffffff0;
    int o1 = OOB;
    if (lane < 2) o1 = WS_ST + (kvh * 32 + idx) * 32 + lane * 16;
    const int o2 = (idx >= 16 && idx < 25) ? WS_YP + (kvh * 9 + idx - 16) * 2048 + lane * 16 : OOB;
    asm volatile("buffer_store_dwordx4 %1, %2, %5, 0 offen sc1\n\t"
                 "buffer_store_dwordx4 %1, %3, %5, 0 offen sc1\n\t"
                 "buffer_store_dwordx4 %1, %4, %5, 0 offen sc1\n\t"
                 "buffer_store_dwordx4 %1, %4, %5, 0 offen offset:1024 sc1\n\ts_waitcnt vmcnt(4)"
                 : "+v"(ep) : "v"(s4), "v"(WS_OP + prod * 1024 + lane * 16), "v"(o1), "v"(o2), "s"(rs) : "memory");
    const u64 tag = (((u64)ep[1] << 32) | ep[0]) + 1;
    tlo = __builtin_amdgcn_readfirstlane((unsigned)tag);
    thi = __builtin_amdgcn_readfirstlane((unsigned)(tag >> 32));
  } else {
    asm volatile("s_barrier" ::: "memory");
  }
  const int nC = 8 - 2 * (kvh >> 2), cc0 = 8 * kvh - 2 * (kvh >> 2) * (kvh - 4), ri = idx - nC;
  const int role = (ri < 0) + 2 * (ri >= 16) + (ri >= 20);
  const int tk = cc0 + idx;
  const int rk = 2 * kvh + (ri >> 3), cq = ri & 7;
  const __amdgpu_buffer_rsrc_t rsb = __builtin_amdgcn_make_buffer_rsrc(ws, 0, WS_END, 0x00020000);
#define TRDEF()                   \
  int lane_ = lane;               \
  asm volatile("" : "+v"(lane_)); \
  const unsigned trq = (lane_ >> 2) & 3, trp = lane_ & 3
  const u32x4 ones = {ONE2, ONE2, ONE2, ONE2};

  if (isk) {
    __builtin_amdgcn_s_setprio(2);
    KVODEF();
    waitvm<4>();
    flag_add(L0, F_WQ);
#pragma nounroll
    for (int t = 1; t < 8; ++t) {
      const int tl = (t + 2 * rq) & 7;
      dma4_nt<KW>(kb + (tl >> 1) * 32768 + (tl & 1) * 4096, kvo, LK + tl * 4096);
    }
    flag_add(L0, F_KISS);
    TS(1);
    flag_wait<1>(L0, F_Q, 1u);
    TS(2);
    u32x4 qf[4];
#pragma unroll
    for (int t = 0; t < 4; ++t) qf[t] = lds_ld<u32x4>(L0 + L_Q + (n16 & 3) * 256 + t * 64 + g4 * 16);
    f32x4 sel[2];
    const unsigned krow = LK + (4 * (n16 & 3) + (n16 >> 2)) * 256;
#pragma unroll
    for (int hf = 0; hf < 2; ++hf) {
      if (hf == 1) waitvm<0>();
      f32x4 S[4];
#pragma unroll
      for (int T = 0; T < 4; ++T) {
        S[T] = f32x4{0.f, 0.f, 0.f, 0.f};
#pragma unroll
        for (int t = 0; t < 4; ++t) S[T] = mfma(lds_ld<u32x4>(krow + ((((2 * hf + (T >> 1) + rq) & 3) * 2 + (T & 1)) * 4096) + (((4 * t + g4) ^ n16) * 16)), qf[t], S[T]);
      }
      const f32x4 lo = (n16 & 4) ? S[1] : S[0], hi = (n16 & 4) ? S[3] : S[2];
      sel[hf] = (n16 & 8) ? hi : lo;
    }
    float m = fmaxf(fmaxf(fmaxf(sel[0][0], sel[0][1]), fmaxf(sel[0][2], sel[0][3])), fmaxf(fmaxf(sel[1][0], sel[1][1]), fmaxf(sel[1][2], sel[1][3])));
    m = fmaxf(m, dpp<ROR4>(m)); m = fmaxf(m, dpp<ROR8>(m));
    m = x32_max(x16_max(m));
    const float ms = m * QSCALE;
    float l = 0.f;
#pragma unroll
    for (int hf = 0; hf < 2; ++hf) {
      u32x4 pp;
#pragma unroll
      for (int r = 0; r < 4; ++r) {
        const float p = __builtin_amdgcn_exp2f(fmaf(sel[hf][r], QSCALE, -ms));
        pp[r] = pk(p, p);
        l += bfhi(pp[r]);
      }
      lds_st<u32x4>(L0 + L_P + (2 * kw + ((n16 >> 2) & 1)) * 1024 + (((n16 & 3) * 4 + g4) * 16 + 4 * (2 * hf + (n16 >> 3))) * 4, pp);
    }
    flag_set(L0, F_P0 + kw, 1u);
    l += dpp<ROR4>(l); l += dpp<ROR8>(l);
    l = x32_sum(x16_sum(l));
    if (lane < 4) {
      lds_st<float>(L0 + L_WM + (kw * 4 + lane) * 4, ms);
      lds_st<float>(L0 + L_WL + (kw * 4 + lane) * 4, l);
    }
    flag_add(L0, F_STAT);
    TS(3);
    __builtin_amdgcn_s_setprio(0);
    if (role == 0) {
      const unsigned char* base = wo + (size_t)(rk * 256 + kw * 64) * 1024 + cq * 128;
      const unsigned v0 = (lane >> 3) * 1024 + (((lane & 7) ^ (g4 << 1)) * 16);
      const unsigned wv[4] = {v0, v0 + 7168, v0 + 2 * 7168, v0 + 3 * 7168};
      flag_wait<1>(L0, F_VISS, 8u);
      dma4_nt<0>(base, wv, LK);
      dma4_nt<0>(base + 32768, wv, LK + 4096);
      TS(4);
      waitvm<4>();
      flag_set(L0, F_P0 + kw, 2u);
      waitvm<0>();
      flag_set(L0, F_P0 + kw, 3u);
      TS(5);
    } else if (role == 1) {
      const unsigned va = (lane >> 2) * 3584u + (lane & 3) * 16u, vbb = ((lane >> 2) ^ 1) * 3584u + (lane & 3) * 16u;
      const unsigned wv[4] = {va, vbb + 56320, va + 2 * 56320, vbb + 3 * 56320};
      const unsigned v2 = (lane >> 3) * 1024 + (lane & 7) * 16;
      const unsigned dv[4] = {v2, (v2 ^ 32) + 7168, v2 + 2 * 7168, (v2 ^ 32) + 3 * 7168};
      const size_t go = (size_t)kw * 128 * 3584 + tk * 64;
      const unsigned char* bd = wd + (size_t)tk * 32 * 1024 + kw * 256;
      const int h0 = tk & 1, h1 = h0 ^ 1;
      flag_wait<1>(L0, F_S2, 2u);
      dma4_nt<0>(bd, dv, LK + 8192);
      dma4_nt<0>(bd + 128, dv, LK + 16384 + 8192);
      dma4_nt<0>(wg + go + h0 * 229376, wv, LK + h0 * 16384);
      dma4_nt<0>(wu + go + h0 * 229376, wv, LK + h0 * 16384 + 4096);
      dma4_nt<0>(wg + go + h1 * 229376, wv, LK + h1 * 16384);
      dma4_nt<0>(wu + go + h1 * 229376, wv, LK + h1 * 16384 + 4096);
      TS(4);
      waitvm<8>();
      flag_set(L0, F_P0 + kw, 2u);
      waitvm<0>();
      flag_set(L0, F_P0 + kw, 3u);
      TS(5);
      TS(6);
    } else {
      flag_wait<1>(L0, F_S2, 2u);
#pragma nounroll
      for (int u = (ri - 16) * 4 + kw; u < 64; u += (16 - nC) * 4) {
        const int row = ((u >> 2) & 3) * 128 + ((u & 1) * 4 + 3 - (u >> 4)) * 16;
        if (2 * (lane & 3) < nC) touch(((u & 2) ? wu : wg) + (size_t)row * 3584 + cc0 * 64, (lane >> 2) * 3584 + (lane & 3) * 128, L0 + L_SCR);
      }
      TS(4);
      waitvm<0>();
      TS(7);
    }
    asm volatile(".Lfk_ke:" ::: "memory");
    TSEND();
    return;
  }

  if (wave0 == 0) {
    const u32x4 xs0 = lds_ld<u32x4>(L0 + L_XS + lane * 16), xs1 = lds_ld<u32x4>(L0 + L_XS + 1024 + lane * 16);
    u32x4 xa[4];
#pragma unroll
    for (int t = 0; t < 4; ++t) xa[t] = lds_ld<u32x4>(L0 + L_XS + (n16 & 1) * 1024 + rq * 256 + t * 64 + g4 * 16);
    float mu, rr;
    {
      float s0 = 0.f, q0 = 0.f, s1 = 0.f, q1 = 0.f;
#pragma unroll
      for (int j = 0; j < 4; ++j) {
        const float a = bflo(xs0[j]), b = bfhi(xs0[j]), c = bflo(xs1[j]), d = bfhi(xs1[j]);
        s0 += a + b; q0 += a * a + b * b; s1 += c + d; q1 += c * c + d * d;
      }
      s0 = wave_sum(s0); q0 = wave_sum(q0); s1 = wave_sum(s1); q1 = wave_sum(q1);
      const float s = (lane & 1) ? s1 : s0, q = (lane & 1) ? q1 : q0;
      mu = s * (1.f / 512.f);
      rr = 1.f / sqrtf(fmaxf(q * (1.f / 512.f) - mu * mu, 0.f) + 1e-5f);
    }
#pragma unroll
    for (int t = 0; t < 4; ++t) xa[t] = (n16 & 3) == 2 ? ones : xa[t];
    flag_wait<0>(L0, F_WQ, 4u);
    TS(1);
    {
      f32x4 d[4];
      TRDEF();
      const unsigned a0 = L0 + L_WQ + (8 * g4 + trq) * 128 + trp * 8, sw = (g4 & 1) * 32;
#pragma unroll
      for (int nt = 0; nt < 4; ++nt) {
        d[nt] = f32x4{0.f, 0.f, 0.f, 0.f};
#pragma unroll
        for (int t = 0; t < 4; ++t) d[nt] = mfma(trfrag(a0 + t * 4096 + ((nt * 32) ^ sw), a0 + t * 4096 + ((nt * 32) ^ sw) + 512), xa[t], d[nt]);
      }
      const f32x4 lo = (n16 & 4) ? d[1] : d[0], hi = (n16 & 4) ? d[3] : d[2];
      const f32x4 dd = (n16 & 8) ? hi : lo;
      float qv[4];
#pragma unroll
      for (int r = 0; r < 4; ++r) qv[r] = rr * (dd[r] - mu * dpp<0xAA>(dd[r]));
      if ((n16 & 3) < 2)
        __builtin_amdgcn_raw_buffer_store_b128(u32x4{tlo, thi, pk(qv[0], qv[1]), pk(qv[2], qv[3])}, rsb,
                                               WS_QP + (((kvh * 2 + (n16 & 1)) * 4 + rq) * 128 + cbk * 16 + (n16 >> 2) * 4 + g4) * 16, 0, 16);
    }
    waitvm<1>();
    if (lane == 0) __builtin_amdgcn_raw_buffer_store_b64(u32x2{tlo, thi}, rsb, WS_FL + w * 8, 0, 16);
    TS(2);
    {
      u32x4 v[8];
      const unsigned ob = WS_QP + (kvh * 2 + bat) * 8192 + lane * 16;
#pragma unroll
      for (int i = 0; i < 8; ++i) v[i] = u32x4{~tlo, 0u, 0u, 0u};
      __builtin_amdgcn_s_sleep(QDELAY);
#pragma nounroll
      for (int spin = 0; spin < SPIN_G; ++spin) {
        u64 m[8];
#pragma unroll
        for (int i = 0; i < 8; ++i) m[i] = __ballot(v[i][0] != tlo || v[i][1] != thi);
        if ((m[0] | m[1] | m[2] | m[3] | m[4] | m[5] | m[6] | m[7]) == 0) break;
        poll_round8(rs, v, ob, m);
      }
#pragma unroll
      for (int h = 0; h < 2; ++h) {
        u32x2 r;
#pragma unroll
        for (int j = 0; j < 2; ++j) {
          const float lo = (bflo(v[h][2 + j]) + bflo(v[2 + h][2 + j])) + (bflo(v[4 + h][2 + j]) + bflo(v[6 + h][2 + j]));
          const float hi = (bfhi(v[h][2 + j]) + bfhi(v[2 + h][2 + j])) + (bfhi(v[4 + h][2 + j]) + bfhi(v[6 + h][2 + j]));
          r[j] = pk(lo, hi);
        }
        lds_st<u32x2>(L0 + L_Q + (64 * h + lane) * 8, r);
      }
      flag_set(L0, F_Q, 1u);
    }
    TS(3);
#pragma nounroll
    for (int spin = 0; spin < SPIN_G; ++spin) {
      asm volatile("" ::: "memory");
      const u32x4 f0 = __builtin_amdgcn_raw_buffer_load_b128(rsb, WS_FL + lane * 16, 0, 16);
      const u32x4 f1 = __builtin_amdgcn_raw_buffer_load_b128(rsb, WS_FL + 1024 + lane * 16, 0, 16);
      const bool ok = f0[0] == tlo && f0[1] == thi && f0[2] == tlo && f0[3] == thi && f1[0] == tlo && f1[1] == thi && f1[2] == tlo && f1[3] == thi;
      if (__all(ok)) break;
    }
    flag_set(L0, F_ALL, 1u);
    if (w == NWG - 1 && lane == 0) __hip_atomic_store((GAS u64*)(ws + WS_EPOCH), ((u64)thi << 32) | tlo, RLX_AGENT);
    TS(4);
    flag_wait<1>(L0, F_STAT, 4u);
    {
      const float m = lds_ld<float>(L0 + L_WM + n16 * 4), ls = lds_ld<float>(L0 + L_WL + n16 * 4);
      float M = fmaxf(m, dpp<ROR4>(m)); M = fmaxf(M, dpp<ROR8>(M));
      const float f = __builtin_amdgcn_exp2f(m - M);
      float L = f * ls;
      L += dpp<ROR4>(L); L += dpp<ROR8>(L);
      if (lane < 16) lds_st<float>(L0 + L_F + lane * 4, f);
      flag_set(L0, F_FAC, 1u);
      if (lane < 4)
        __builtin_amdgcn_raw_buffer_store_b64(u32x2{nosent(__float_as_uint(M)), nosent(__float_as_uint(L))}, rsb, WS_ST + (kvh * 32 + idx) * 32 + lane * 8, 0, 16);
    }
    TS(5);
    if (role == 0) {
      u32x4 st = {SENT, SENT, SENT, SENT};
      flag_wait<1>(L0, F_VISS, 4u);
      poll1(rs, st, WS_ST + kvh * 1024 + lane * 16);
      const float m0 = __uint_as_float(st[0]), l0 = __uint_as_float(st[1]), m1 = __uint_as_float(st[2]), l1 = __uint_as_float(st[3]);
      float M0 = m0, M1 = m1;
#pragma unroll
      for (int o2 = 2; o2 <= 16; o2 <<= 1) { M0 = fmaxf(M0, __shfl_xor(M0, o2)); M1 = fmaxf(M1, __shfl_xor(M1, o2)); }
      const float e0 = __builtin_amdgcn_exp2f(m0 - M0), e1 = __builtin_amdgcn_exp2f(m1 - M1);
      float S0 = e0 * l0, S1 = e1 * l1;
#pragma unroll
      for (int o2 = 2; o2 <= 16; o2 <<= 1) { S0 += __shfl_xor(S0, o2); S1 += __shfl_xor(S1, o2); }
      const int bb = lane >> 5, ss = (lane >> 1) & 15, h0 = (lane & 1) * 2;
      lds_st<float>(L0 + L_W + ((bb * 4 + h0) * 16 + ss) * 4, e0 / S0);
      lds_st<float>(L0 + L_W + ((bb * 4 + h0 + 1) * 16 + ss) * 4, e1 / S1);
      flag_set(L0, F_W, 1u);
      TS(6);
    }
    TSEND();
    return;
  }

  const int wave = wave0 - 5;
  const unsigned LW = L0 + wave * 16384;
  const unsigned char* vb = vc + chunk + wave * 4096;
  __builtin_amdgcn_s_setprio(2);
  if (wave == 0 && lane < 2) touch(vb, lane * 65536, L0 + L_SCR);
  u32x4 msk;
  {
    const unsigned hm = (lane & 1) ? 0xffff0000u : 0x0000ffffu;
    const int dsel = (lane & 7) >> 1;
    msk = u32x4{dsel == 0 ? hm : 0u, dsel == 1 ? hm : 0u, dsel == 2 ? hm : 0u, dsel == 3 ? hm : 0u};
  }
  u32x4 V[16];
  const unsigned vo = lane * 16;
  flag_wait<1>(L0, F_KISS, 4u);
  TS(1);
#define LDV(i) do { if ((i) >= VW) waitvm<VW - 1>(); ldv_nt<((i) & 3) * 1024>(V[i], vb + ((((i) >> 2) + rq) & 3) * 32768, vo); } while (0)
  LDV(0); LDV(1); LDV(2); LDV(3); LDV(4); LDV(5); LDV(6); LDV(7); LDV(8); LDV(9); LDV(10); LDV(11);
  TS(2);
  flag_wait<1>(L0, F_P0 + (wave >> 1), 1u);
  u32x4 P[2][4];
#pragma unroll
  for (int u = 0; u < 2; ++u)
#pragma unroll
    for (int k = 0; k < 4; ++k) P[u][k] = lds_ld<u32x4>(L0 + L_P + wave * 1024 + ((2 * u + (n16 >> 3)) * 4 + g4) * 64 + k * 16);
  f32x4 acc[2] = {{0.f, 0.f, 0.f, 0.f}, {0.f, 0.f, 0.f, 0.f}};
  TS(3);
#define PV(j, N)                                                                        \
  do {                                                                                  \
    asm volatile("s_waitcnt vmcnt(%1)" : "+v"(V[j]) : "i"(N) : "memory");               \
    const u32x4 b0 = msk & P[0][(j) >> 2][(j) & 3], b1 = msk & P[1][(j) >> 2][(j) & 3]; \
    acc[0] = mfma(V[j], b0, acc[0]);                                                    \
    acc[1] = mfma(V[j], b1, acc[1]);                                                    \
    __builtin_amdgcn_sched_barrier(0);                                                  \
  } while (0)
  LDV(12); PV(0, 12); PV(1, 11);
  LDV(13); PV(2, 11); PV(3, 10);
  LDV(14); PV(4, 10); PV(5, 9);
  LDV(15); flag_add(L0, F_VISS); PV(6, 9); PV(7, 8);
  asm volatile("" : "+v"(acc[0]), "+v"(acc[1]));
  TS(4);
  flag_wait<0>(L0, F_FAC, 1u);
  unsigned oa = L0 + L_RB_O + wave * 1024 + lane * 16, og = WS_OP + prod * 1024 + (2 * wave + (n16 >> 3)) * 256 + g4 * 64 + (lane & 7) * 8;
  asm volatile("" : "+v"(oa), "+v"(og));
  const float f0 = lds_ld<float>(L0 + L_F + ((wave >> 1) * 4 + (n16 >> 3)) * 4), f1 = lds_ld<float>(L0 + L_F + ((wave >> 1) * 4 + 2 + (n16 >> 3)) * 4);
#pragma unroll
  for (int j = 8; j < 16; ++j) PV(j, 15 - j);
  TS(5);
  lds_st<f32x4>(LW + L_RB_O + lane * 16, acc[0] * f0);
  lds_st<f32x4>(LW + L_RB_O + 1024 + lane * 16, acc[1] * f1);
  flag_add(L0, F_S1);
  if (wave < 2) {
    flag_wait<0>(L0, F_S1, 8u);
    f32x4 r[8];
    lds_ld8_b128<16384>(r, oa, oa + 65536);
    const f32x4 s = ((r[0] + r[1]) + (r[2] + r[3])) + ((r[4] + r[5]) + (r[6] + r[7]));
    const unsigned s0 = nosent(pk(s[0], s[1])), s1 = nosent(pk(s[2], s[3]));
    const u32x4 sv = {s0, s1, (unsigned)__builtin_amdgcn_mov_dpp((int)s0, XOR1, 0xf, 0xf, true), (unsigned)__builtin_amdgcn_mov_dpp((int)s1, XOR1, 0xf, 0xf, true)};
    asm volatile("s_mov_b64 exec, %3\n\tbuffer_store_dwordx4 %0, %1, %2, 0 offen sc1\n\ts_mov_b64 exec, -1" ::"v"(sv), "v"(og), "s"(rs), "s"(0x5555555555555555ull) : "memory");
    flag_add(L0, F_S2);
  }
  TS(6);
  if (role == 3) { TSEND(); return; }
  const u32x4 sel = msk & ones;
  TRDEF();

  if (role == 0) {
    const int kq = wave >> 1, bb = wave & 1;
    const int hd = 2 * (rk & 1) + (kq >> 1);
    const unsigned LB = L0 + kq * 32768;
    u32x4 A0, A1;
    const unsigned o0 = WS_OP + ((bb * 8 + (rk >> 1)) * 16 + (lane >> 3)) * 1024 + hd * 256 + (kq & 1) * 128 + (lane & 7) * 16;
    f32x4 xr0 = {0.f, 0.f, 0.f, 0.f}, xr1 = {0.f, 0.f, 0.f, 0.f};
    if (wave == 0 && rk == 0) {
#pragma unroll
      for (int j = 0; j < 4; ++j) {
        xr0[j] = bflo(lds_ld<unsigned short>(L0 + L_XS + (cq * 64 + j * 16 + n16) * 2));
        xr1[j] = bflo(lds_ld<unsigned short>(L0 + L_XS + (512 + cq * 64 + j * 16 + n16) * 2));
      }
    }
    flag_wait<0>(L0, F_W, 1u);
    const float wa = lds_ld<float>(L0 + L_W + ((bb * 4 + hd) * 16 + 2 * g4 + (n16 >> 3)) * 4), wb = lds_ld<float>(L0 + L_W + ((bb * 4 + hd) * 16 + 8 + 2 * g4 + (n16 >> 3)) * 4);
    u32x4 sa = msk & pk(wa, wa), sb = msk & pk(wb, wb);
    unsigned sw = LW + 8192 + n16 * 16 + (g4 & 1) * 8, sr = LW + 8192 + (n16 & 1) * 128 + g4 * 16, xw = L0 + L_X + wave * 256 + n16 * 16, xa = L0 + L_X + n16 * 16,
             og2 = WS_YP + rk * 2048 + cq * 256 + n16 * 16;
    const u64 vmask = __ballot((g4 >> 1) == (n16 >> 3));
    asm volatile("" : "+v"(sw), "+v"(sr), "+v"(xw), "+v"(xa), "+v"(og2), "+v"(xr0), "+v"(xr1), "+v"(sa), "+v"(sb));
    u32x4 Bw[2][4];
#pragma unroll
    for (int t = 0; t < 2; ++t) {
      flag_wait<0>(L0, F_P0 + kq, 2u + t);
      if (t == 1)
        asm volatile("buffer_load_dwordx4 %0, %2, %3, 0 offen sc1\n\tbuffer_load_dwordx4 %1, %2, %3, %4 offen sc1"
                     : "=&v"(A0), "=&v"(A1) : "v"(o0), "s"(rs), "s"(8192u) : "memory");
#pragma unroll
      for (int nt = 0; nt < 4; ++nt) {
        const unsigned a = LB + (32 * t + 8 * trq + 2 * g4) * 128 + ((nt ^ g4) * 32) + trp * 8;
        Bw[t][nt] = trfrag(a, a + 128);
      }
    }
    TS(7);
    asm volatile("s_waitcnt vmcnt(0)" : "+v"(A0), "+v"(A1)::"memory");
    poll2(rs, A0, A1, o0, 8192u);
    TS(8);
    f32x4 d = mfma(sa, A0, f32x4{0.f, 0.f, 0.f, 0.f});
    d = mfma(sb, A1, d);
    asm volatile("s_mov_b64 exec, %2\n\tds_write_b64 %0, %1\n\ts_mov_b64 exec, -1" ::"v"(sw), "v"(u32x2{pk(d[0], d[1]), pk(d[2], d[3])}), "s"(vmask) : "memory");
    A0 = lds_ld<u32x4>(sr);
    A1 = lds_ld<u32x4>(sr + 64);
    f32x4 val;
#pragma unroll
    for (int nt = 0; nt < 4; ++nt) {
      f32x4 e = mfma(A0, Bw[0][nt], f32x4{0.f, 0.f, 0.f, 0.f});
      e = mfma(A1, Bw[1][nt], e);
      val[nt] = e[0] + e[1];
    }
    asm volatile("s_mov_b64 exec, 0xffff\n\tds_write_b128 %0, %1\n\ts_mov_b64 exec, -1" ::"v"(xw), "v"(val) : "memory");
    wg_barrier();
    if (wave == 0) {
      f32x4 r[8];
      lds_ld8_b128<256>(r, xa, xa + 1024);
      xr0 += (r[0] + r[2]) + (r[4] + r[6]);
      xr1 += (r[1] + r[3]) + (r[5] + r[7]);
      if (lane < 16)
        __builtin_amdgcn_raw_buffer_store_b128(u32x4{nosent(pk(xr0[0], xr0[1])), nosent(pk(xr0[2], xr0[3])), nosent(pk(xr1[0], xr1[1])), nosent(pk(xr1[2], xr1[3]))},
                                               rsb, og2, 0, 16);
      asm volatile(".Lfk_e0:" ::: "memory");
      TS(9);
      FK_END();
    }
  } else if (role == 1) {
    u32x4 H[4];
    const unsigned o = WS_YP + g4 * 2048 + wave * 256 + n16 * 16;
    const unsigned LS = LW + 14336;
    lds_st<unsigned short>(LS + 256 + lane * 2, (unsigned short)0x3F80);
    unsigned xw = L0 + L_X + wave * 1024 + lane * 16, xa = L0 + L_X + lane * 16;
    unsigned hw = LS + lane * 8, hr = LS + ((n16 & 3) == 1 ? 256 : ((n16 >> 2) & 1) * 128) + g4 * 16, stw = L0 + L_P + wave * 1040 + g4 * 8,
             str = L0 + L_P + (n16 & 7) * 1040 + (g4 & 1) * 8, aw = LS + 384 + (g4 & 1) * 64 + (g4 >> 1) * 32 + n16 * 2, ar = LS + 384 + (n16 & 1) * 64 + g4 * 16,
             og2 = WS_YP + (16 + tk) * 2048 + wave * 256 + n16 * 16;
    asm volatile("" : "+v"(hw), "+v"(hr), "+v"(stw), "+v"(str), "+v"(aw), "+v"(ar), "+v"(og2), "+v"(xw), "+v"(xa));
    unsigned da = LW + 8192 + (8 * g4 + trq) * 128 + (g4 & 1) * 32 + trp * 8, db = da ^ 32;
    unsigned ga = LW + (16 * trq + 2 * g4 + (trq & 1)) * 64 + trp * 8, gb = ga ^ 64;
    asm volatile("" : "+v"(da), "+v"(db), "+v"(ga), "+v"(gb));
    flag_wait<1>(L0, F_ALL, 1u);
    flag_wait<1>(L0, F_P0 + (wave >> 1), 2u + ((wave ^ tk) & 1));
    issue4(rs, H, o, 8192u);
    u32x4 Bw[2][2][2], Bd[4];
#pragma unroll
    for (int mt = 0; mt < 2; ++mt)
#pragma unroll
      for (int t = 0; t < 2; ++t)
#pragma unroll
        for (int nt = 0; nt < 2; ++nt) Bw[mt][t][nt] = trfrag(ga + mt * 4096 + t * 512 + nt * 32, gb + mt * 4096 + t * 512 + nt * 32);
#pragma unroll
    for (int j = 0; j < 4; ++j) Bd[j] = trfrag(((j & 1) ? db : da) + (j >> 1) * 64, ((j & 1) ? db : da) + (j >> 1) * 64 + 512);
    TS(7);
    land4(H);
    poll4(rs, H, o, 8192u);
    TS(8);
    f32x4 h = mfma(sel, H[0], f32x4{0.f, 0.f, 0.f, 0.f}), h2 = mfma(sel, H[1], f32x4{0.f, 0.f, 0.f, 0.f});
    h = mfma(sel, H[2], h);
    h2 = mfma(sel, H[3], h2);
    h += h2;
    asm volatile("s_mov_b32 exec_hi, 0\n\tds_write_b64 %0, %1\n\ts_mov_b32 exec_hi, -1" ::"v"(hw), "v"(u32x2{pk(h[0], h[1]), pk(h[2], h[3])}) : "memory");
    const u32x4 A0 = lds_ld<u32x4>(hr), A1 = lds_ld<u32x4>(hr + 64);
    __builtin_amdgcn_sched_barrier(0);
    const float ss = row_sum16((h[0] + h[1]) + (h[2] + h[3])), gs = row_sum16((h[0] * h[0] + h[1] * h[1]) + (h[2] * h[2] + h[3] * h[3]));
    asm volatile("s_mov_b64 exec, 0x10001\n\tds_write_b64 %0, %1\n\ts_mov_b64 exec, -1" ::"v"(stw), "v"(f32x2{ss, gs}) : "memory");
    __builtin_amdgcn_sched_barrier(0);
    f32x4 e[2][2];
#pragma unroll
    for (int nt = 0; nt < 2; ++nt)
#pragma unroll
      for (int mt = 0; mt < 2; ++mt) {
        e[nt][mt] = mfma(A0, Bw[mt][0][nt], f32x4{0.f, 0.f, 0.f, 0.f});
        e[nt][mt] = mfma(A1, Bw[mt][1][nt], e[nt][mt]);
      }
    lds_st<f32x4>(xw, lane < 32 ? f32x4{e[0][0][0], e[0][1][0], e[0][0][1], e[0][1][1]} : f32x4{e[1][0][0], e[1][1][0], e[1][0][1], e[1][1][1]});
    wg_barrier();
    f32x2 st;
    f32x4 r[8];
    asm volatile("ds_read_b64 %8, %10\n\tds_read_b128 %0, %9\n\tds_read_b128 %1, %9 offset:1024\n\tds_read_b128 %2, %9 offset:2048\n\tds_read_b128 %3, %9 offset:3072\n\t"
                 "ds_read_b128 %4, %9 offset:4096\n\tds_read_b128 %5, %9 offset:5120\n\tds_read_b128 %6, %9 offset:6144\n\tds_read_b128 %7, %9 offset:7168\n\t"
                 "s_waitcnt lgkmcnt(8)"
                 : "=&v"(r[0]), "=&v"(r[1]), "=&v"(r[2]), "=&v"(r[3]), "=&v"(r[4]), "=&v"(r[5]), "=&v"(r[6]), "=&v"(r[7]), "=&v"(st)
                 : "v"(xa), "v"(str) : "memory");
    st[0] += dpp<XOR1>(st[0]); st[1] += dpp<XOR1>(st[1]);
    st[0] += dpp<XOR2>(st[0]); st[1] += dpp<XOR2>(st[1]);
    st[0] += dpp<XOR7>(st[0]); st[1] += dpp<XOR7>(st[1]);
    float mu = st[0] * (1.f / 512.f);
    float rr = __builtin_amdgcn_rsqf(fmaxf(st[1] * (1.f / 512.f) - mu * mu, 0.f) + 1e-5f);
    asm volatile("s_waitcnt lgkmcnt(0)"
                 : "+v"(r[0]), "+v"(r[1]), "+v"(r[2]), "+v"(r[3]), "+v"(r[4]), "+v"(r[5]), "+v"(r[6]), "+v"(r[7]), "+v"(mu), "+v"(rr)::"memory");
    const f32x4 gu = ((r[0] + r[1]) + (r[2] + r[3])) + ((r[4] + r[5]) + (r[6] + r[7]));
    const float gg = rr * (gu[0] - mu * gu[2]), uu = rr * (gu[1] - mu * gu[3]);
    const float a = gg * uu * __builtin_amdgcn_rcpf(1.f + __builtin_amdgcn_exp2f(-1.4426950408889634f * gg));
    lds_st<__bf16>(aw, (__bf16)a);
    const u32x4 Aa = lds_ld<u32x4>(ar);
    f32x4 y[4];
#pragma unroll
    for (int j = 0; j < 4; ++j) y[j] = mfma(Aa, Bd[j], f32x4{0.f, 0.f, 0.f, 0.f});
    if (lane < 16)
      __builtin_amdgcn_raw_buffer_store_b128(
          u32x4{nosent(pk(y[0][0], y[1][0])), nosent(pk(y[2][0], y[3][0])), nosent(pk(y[0][1], y[1][1])), nosent(pk(y[2][1], y[3][1]))}, rsb, og2, 0, 16);
    asm volatile(".Lfk_e1:" ::: "memory");
    TS(9);
    FK_END();
  } else if (wave == 2) {
    const int ee = 4 * kvh + ri - 16;
    u32x4 Y0 = {SENT, SENT, SENT, SENT}, Y[4];
    const unsigned ob = WS_YP + (lane >> 2) * 2048 + ee * 64 + (lane & 3) * 16;
#pragma unroll
    for (int i = 0; i < 4; ++i) Y[i] = (i < 3 || lane < 32) ? u32x4{SENT, SENT, SENT, SENT} : u32x4{0u, 0u, 0u, 0u};
    flag_wait<1>(L0, F_ALL, 1u);
#pragma nounroll
    for (int spin = 0; spin < SPIN_G; ++spin) {
      const u64 m = __ballot(pend4(Y0));
      if (m == 0) break;
      __builtin_amdgcn_s_sleep(4);
      poll_round1(rs, Y0, ob, m);
    }
    f32x4 d0 = mfma(sel, Y0, f32x4{0.f, 0.f, 0.f, 0.f});
    TS(7);
    poll4(rs, Y, ob + 32768u, 32768u);
    TS(8);
    f32x4 d1 = mfma(sel, Y[1], f32x4{0.f, 0.f, 0.f, 0.f});
    d0 = mfma(sel, Y[0], d0);
    d1 = mfma(sel, Y[3], d1);
    d0 = mfma(sel, Y[2], d0);
    f32x4 d = d0 + d1;
#pragma unroll
    for (int r = 0; r < 4; ++r) { d[r] += dpp<ROR4>(d[r]); d[r] += dpp<ROR8>(d[r]); }
    const float lo = (n16 & 4) ? d[1] : d[0], hi = (n16 & 4) ? d[3] : d[2];
    const float res = (n16 & 8) ? hi : lo;
    const float nb = dpp<XOR1>(res);
    if (lane < 32 && (lane & 1) == 0)
      *(unsigned*)(out + g4 * 512 + 64 * (ee >> 2) + 16 * (n16 >> 2) + 4 * (ee & 3) + (n16 & 3)) = pk(res, nb);
    asm volatile(".Lfk_e2:" ::: "memory");
    TS(9);
    FK_END();
  }
  asm volatile(".Lfk_te:" ::: "memory");
  TSEND();
}
}

extern "C" void kernel_launch(void* const* d_in, const int* in_sizes, int n_in, void* d_out, int out_size, void* d_ws, size_t ws_size,
                              hipStream_t stream) {
  (void)in_sizes; (void)n_in; (void)out_size; (void)ws_size;
  (void)hipFuncSetAttribute((const void*)fk::fused_kernel, hipFuncAttributeMaxDynamicSharedMemorySize, fk::L_END);
  fk::fused_kernel<<<fk::NWG, fk::NTHR, fk::L_END, stream>>>(
      (const unsigned char*)d_in[3], (const unsigned char*)d_in[1], (unsigned char*)d_ws, (const unsigned char*)d_in[0],
      (const unsigned char*)d_in[2], (const unsigned char*)d_in[4], (const unsigned char*)d_in[6], (const unsigned char*)d_in[7],
      (const unsigned char*)d_in[8], (unsigned short*)d_out);
}
```

```cpp
#include <hip/hip_runtime.h>
#include <hip/hip_bf16.h>
#include <stdint.h>

namespace fk {
typedef unsigned u32x4 __attribute__((ext_vector_type(4)));
typedef unsigned u32x2 __attribute__((ext_vector_type(2)));
typedef float f32x4 __attribute__((ext_vector_type(4)));
typedef float f32x2 __attribute__((ext_vector_type(2)));
typedef short s16x4 __attribute__((ext_vector_type(4)));
typedef __bf16 bf16x8 __attribute__((ext_vector_type(8)));
typedef __bf16 bf16x2 __attribute__((ext_vector_type(2)));
typedef unsigned long long u64;
#define LAS __attribute__((address_space(3)))
#define GAS __attribute__((address_space(1)))
#define RLX_AGENT __ATOMIC_RELAXED, __HIP_MEMORY_SCOPE_AGENT

constexpr int KW = 6;
constexpr int VW = 4;
constexpr int QDELAY = 40;
constexpr unsigned SENT = 0xFFFFFFFFu;
constexpr int NWG = 256, NWAVES = 13, NTHR = NWAVES * 64;
constexpr int WS_EPOCH = 0;
constexpr int WS_FL = 4096;
constexpr int WS_QP = 8192;
constexpr int WS_ST = WS_QP + 131072;
constexpr int WS_OP = WS_ST + 8192;
constexpr int WS_YP = WS_OP + 262144;
constexpr int WS_END = WS_YP + 72 * 2048;
constexpr int L_RB_O = 12288;
constexpr int L_WQ = 131072, L_P = L_WQ  , L_X = L_WQ + 8192  , L_Q = L_WQ + 16384, L_WM = L_Q + 1024, L_WL = L_WM + 64, L_F = L_WL + 64,
              L_W = L_F + 64, L_FLAGS = L_W + 512, L_SCR = L_FLAGS + 64, L_XS = L_SCR + 256, L_END = L_XS + 2048;
enum { F_Q = 0, F_STAT = 1, F_FAC = 2, F_VISS = 3, F_WQ = 4, F_KISS = 5, F_S1 = 6, F_S2 = 7, F_ALL = 9, F_W = 10, F_P0 = 12   };
constexpr int SPIN_G = 1 << 18;
constexpr int SPIN_L = 1 << 22;
constexpr float QSCALE = 0.088388347648318440f * 1.4426950408889634f;
constexpr unsigned ONE2 = 0x3F803F80u;

__device__ __forceinline__ f32x4 mfma(u32x4 a, u32x4 b, f32x4 c) {
  return __builtin_amdgcn_mfma_f32_16x16x32_bf16(__builtin_bit_cast(bf16x8, a), __builtin_bit_cast(bf16x8, b), c, 0, 0, 0);
}
__device__ __forceinline__ unsigned pk(float lo, float hi) { bf16x2 v = {(__bf16)lo, (__bf16)hi}; return __builtin_bit_cast(unsigned, v); }
__device__ __forceinline__ unsigned nosent(unsigned x) { return x < 0xFFFFFFFEu ? x : 0xFFFFFFFEu; }
__device__ __forceinline__ float bflo(unsigned u) { return __builtin_bit_cast(float, u << 16); }
__device__ __forceinline__ float bfhi(unsigned u) { return __builtin_bit_cast(float, u & 0xffff0000u); }
__device__ __forceinline__ float x16_sum(float x) {
  auto s = __builtin_amdgcn_permlane16_swap(__float_as_uint(x), __float_as_uint(x), false, false);
  return __uint_as_float(s[0]) + __uint_as_float(s[1]);
}
__device__ __forceinline__ float x32_sum(float x) {
  auto s = __builtin_amdgcn_permlane32_swap(__float_as_uint(x), __float_as_uint(x), false, false);
  return __uint_as_float(s[0]) + __uint_as_float(s[1]);
}
__device__ __forceinline__ float x16_max(float x) {
  auto s = __builtin_amdgcn_permlane16_swap(__float_as_uint(x), __float_as_uint(x), false, false);
  return fmaxf(__uint_as_float(s[0]), __uint_as_float(s[1]));
}
__device__ __forceinline__ float x32_max(float x) {
  auto s = __builtin_amdgcn_permlane32_swap(__float_as_uint(x), __float_as_uint(x), false, false);
  return fmaxf(__uint_as_float(s[0]), __uint_as_float(s[1]));
}
template <int CTRL> __device__ __forceinline__ float dpp(float x) {
  return __builtin_bit_cast(float, __builtin_amdgcn_mov_dpp(__builtin_bit_cast(int, x), CTRL, 0xf, 0xf, true));
}
constexpr int XOR1 = 0xB1, XOR2 = 0x4E, XOR7 = 0x141, ROR4 = 0x124, ROR8 = 0x128;
__device__ __forceinline__ float row_sum16(float x) { x += dpp<XOR1>(x); x += dpp<XOR2>(x); x += dpp<XOR7>(x); x += dpp<ROR8>(x); return x; }
__device__ __forceinline__ float wave_sum(float x) { return x32_sum(x16_sum(row_sum16(x))); }

template <int N> __device__ __forceinline__ void waitvm() { asm volatile("s_waitcnt vmcnt(%0)" ::"i"(N) : "memory"); }
template <int W> __device__ __forceinline__ void dma4_nt(const void* sbase, const unsigned (&v)[4], unsigned ldst) {
  if constexpr (W == 0)
    asm volatile("s_mov_b32 m0, %5\n\ts_nop 0\n\tglobal_load_lds_dwordx4 %0, %4 nt\n\tglobal_load_lds_dwordx4 %1, %4 offset:1024 nt\n\t"
                 "global_load_lds_dwordx4 %2, %4 offset:2048 nt\n\tglobal_load_lds_dwordx4 %3, %4 offset:3072 nt"
                 ::"v"(v[0]), "v"(v[1]), "v"(v[2]), "v"(v[3]), "s"(sbase), "s"(ldst) : "memory");
  else
    asm volatile("s_mov_b32 m0, %5\n\ts_waitcnt vmcnt(%6)\n\tglobal_load_lds_dwordx4 %0, %4 nt\n\ts_waitcnt vmcnt(%6)\n\tglobal_load_lds_dwordx4 %1, %4 offset:1024 nt\n\t"
                 "s_waitcnt vmcnt(%6)\n\tglobal_load_lds_dwordx4 %2, %4 offset:2048 nt\n\ts_waitcnt vmcnt(%6)\n\tglobal_load_lds_dwordx4 %3, %4 offset:3072 nt"
                 ::"v"(v[0]), "v"(v[1]), "v"(v[2]), "v"(v[3]), "s"(sbase), "s"(ldst), "i"(W - 1) : "memory");
}
__device__ __forceinline__ void touch(const void* sbase, unsigned voff, unsigned ldst) {
  asm volatile("s_mov_b32 m0, %2\n\ts_nop 0\n\tglobal_load_lds_dword %0, %1" ::"v"(voff), "s"(sbase), "s"(ldst) : "memory");
}
template <int OFF> __device__ __forceinline__ void ldv_nt(u32x4& dst, const void* sbase, unsigned voff) {
  asm volatile("global_load_dwordx4 %0, %1, %2 offset:%3 nt" : "=v"(dst) : "v"(voff), "s"(sbase), "i"(OFF) : "memory");
}
__device__ __forceinline__ u32x2 trread(unsigned addr) {
  return __builtin_bit_cast(u32x2, __builtin_amdgcn_ds_read_tr16_b64_v4i16((LAS s16x4*)(uintptr_t)addr));
}
__device__ __forceinline__ u32x4 trfrag(unsigned a0, unsigned a1) { u32x2 lo = trread(a0), hi = trread(a1); return u32x4{lo[0], lo[1], hi[0], hi[1]}; }
template <typename T> __device__ __forceinline__ T lds_ld(unsigned addr) { return *(LAS T*)(uintptr_t)addr; }
template <typename T> __device__ __forceinline__ void lds_st(unsigned addr, T v) { *(LAS T*)(uintptr_t)addr = v; }
__device__ __forceinline__ void flag_set(unsigned base, int f, unsigned v) {
  asm volatile("s_mov_b64 exec, 1\n\tds_write_b32 %0, %1\n\ts_mov_b64 exec, -1" ::"v"(base + L_FLAGS + f * 4), "v"(v) : "memory");
}
__device__ __forceinline__ void flag_add(unsigned base, int f) {
  asm volatile("s_mov_b64 exec, 1\n\tds_add_u32 %0, %1\n\ts_mov_b64 exec, -1" ::"v"(base + L_FLAGS + f * 4), "v"(1u) : "memory");
}
__device__ __forceinline__ void wg_barrier() { asm volatile("s_waitcnt lgkmcnt(0)\n\ts_barrier" ::: "memory"); }
template <int ST> __device__ __forceinline__ void lds_ld8_b128(f32x4 (&r)[8], unsigned a, unsigned b) {
  asm volatile("ds_read_b128 %0, %8\n\tds_read_b128 %1, %8 offset:%10\n\tds_read_b128 %2, %8 offset:%11\n\tds_read_b128 %3, %8 offset:%12\n\t"
               "ds_read_b128 %4, %9\n\tds_read_b128 %5, %9 offset:%10\n\tds_read_b128 %6, %9 offset:%11\n\tds_read_b128 %7, %9 offset:%12\n\t"
               "s_waitcnt lgkmcnt(0)"
               : "=&v"(r[0]), "=&v"(r[1]), "=&v"(r[2]), "=&v"(r[3]), "=&v"(r[4]), "=&v"(r[5]), "=&v"(r[6]), "=&v"(r[7])
               : "v"(a), "v"(b), "i"(ST), "i"(2 * ST), "i"(3 * ST) : "memory");
}
template <int SLP> __device__ __forceinline__ void flag_wait(unsigned base, int f, unsigned v) {
  asm volatile("" ::: "memory");
#pragma nounroll
  for (int i = 0; i < SPIN_L; ++i) {
    unsigned x = *(volatile LAS unsigned*)(uintptr_t)(base + L_FLAGS + f * 4);
    if ((unsigned)__builtin_amdgcn_readfirstlane(x) >= v) break;
    if (SLP) __builtin_amdgcn_s_sleep(SLP);
  }
  asm volatile("" ::: "memory");
}
__device__ __forceinline__ bool pend4(u32x4 v) {
  unsigned a = v[0] > v[1] ? v[0] : v[1], b = v[2] > v[3] ? v[2] : v[3];
  return (a > b ? a : b) == SENT;
}
__device__ __forceinline__ void poll_round1(u32x4 rs, u32x4& v0, unsigned o0, u64 m0) {
  asm volatile("s_mov_b64 exec, %3\n\tbuffer_load_dwordx4 %0, %1, %2, 0 offen sc1\n\t"
               "s_mov_b64 exec, -1\n\ts_waitcnt vmcnt(0)"
               : "+v"(v0) : "v"(o0), "s"(rs), "s"(m0) : "memory");
}
__device__ __forceinline__ void poll_round8(u32x4 rs, u32x4 (&v)[8], unsigned o, const u64 (&m)[8]) {
  asm volatile("s_mov_b64 exec, %10\n\tbuffer_load_dwordx4 %0, %8, %9, 0 offen sc1\n\t"
               "s_mov_b64 exec, %11\n\tbuffer_load_dwordx4 %1, %8, %9, 0 offen offset:1024 sc1\n\t"
               "s_mov_b64 exec, %12\n\tbuffer_load_dwordx4 %2, %8, %9, 0 offen offset:2048 sc1\n\t"
               "s_mov_b64 exec, %13\n\tbuffer_load_dwordx4 %3, %8, %9, 0 offen offset:3072 sc1\n\t"
               "s_mov_b64 exec, %14\n\tbuffer_load_dwordx4 %4, %8, %9, %18 offen sc1\n\t"
               "s_mov_b64 exec, %15\n\tbuffer_load_dwordx4 %5, %8, %9, %18 offen offset:1024 sc1\n\t"
               "s_mov_b64 exec, %16\n\tbuffer_load_dwordx4 %6, %8, %9, %18 offen offset:2048 sc1\n\t"
               "s_mov_b64 exec, %17\n\tbuffer_load_dwordx4 %7, %8, %9, %18 offen offset:3072 sc1\n\t"
               "s_mov_b64 exec, -1\n\ts_waitcnt vmcnt(0)"
               : "+v"(v[0]), "+v"(v[1]), "+v"(v[2]), "+v"(v[3]), "+v"(v[4]), "+v"(v[5]), "+v"(v[6]), "+v"(v[7])
               : "v"(o), "s"(rs), "s"(m[0]), "s"(m[1]), "s"(m[2]), "s"(m[3]), "s"(m[4]), "s"(m[5]), "s"(m[6]), "s"(m[7]), "s"(4096u) : "memory");
}
__device__ __forceinline__ void issue4(u32x4 rs, u32x4 (&v)[4], unsigned o, unsigned st) {
  asm volatile("buffer_load_dwordx4 %0, %4, %5, 0 offen sc1\n\tbuffer_load_dwordx4 %1, %4, %5, %6 offen sc1\n\t"
               "buffer_load_dwordx4 %2, %4, %5, %7 offen sc1\n\tbuffer_load_dwordx4 %3, %4, %5, %8 offen sc1"
               : "=&v"(v[0]), "=&v"(v[1]), "=&v"(v[2]), "=&v"(v[3]) : "v"(o), "s"(rs), "s"(st), "s"(2 * st), "s"(3 * st) : "memory");
}
__device__ __forceinline__ void land4(u32x4 (&v)[4]) {
  asm volatile("s_waitcnt vmcnt(0)" : "+v"(v[0]), "+v"(v[1]), "+v"(v[2]), "+v"(v[3])::"memory");
}
__device__ __forceinline__ void poll2(u32x4 rs, u32x4& A0, u32x4& A1, unsigned o, unsigned st) {
#pragma nounroll
  for (int spin = 0; spin < SPIN_G; ++spin) {
    const u64 m0 = __ballot(pend4(A0)), m1 = __ballot(pend4(A1));
    if ((m0 | m1) == 0) break;
    if (__builtin_popcountll(m0) + __builtin_popcountll(m1) > 24) __builtin_amdgcn_s_sleep(16);
    asm volatile("s_mov_b64 exec, %5\n\tbuffer_load_dwordx4 %0, %2, %3, 0 offen sc1\n\t"
                 "s_mov_b64 exec, %6\n\tbuffer_load_dwordx4 %1, %2, %3, %4 offen sc1\n\t"
                 "s_mov_b64 exec, -1\n\ts_waitcnt vmcnt(0)"
                 : "+v"(A0), "+v"(A1) : "v"(o), "s"(rs), "s"(st), "s"(m0), "s"(m1) : "memory");
  }
}
__device__ __forceinline__ void poll4(u32x4 rs, u32x4 (&A)[4], unsigned o, unsigned st) {
#pragma nounroll
  for (int spin = 0; spin < SPIN_G; ++spin) {
    u64 m[4];
#pragma unroll
    for (int i = 0; i < 4; ++i) m[i] = __ballot(pend4(A[i]));
    if ((m[0] | m[1] | m[2] | m[3]) == 0) break;
    asm volatile("s_mov_b64 exec, %9\n\tbuffer_load_dwordx4 %0, %4, %5, 0 offen sc1\n\t"
                 "s_mov_b64 exec, %10\n\tbuffer_load_dwordx4 %1, %4, %5, %6 offen sc1\n\t"
                 "s_mov_b64 exec, %11\n\tbuffer_load_dwordx4 %2, %4, %5, %7 offen sc1\n\t"
                 "s_mov_b64 exec, %12\n\tbuffer_load_dwordx4 %3, %4, %5, %8 offen sc1\n\t"
                 "s_mov_b64 exec, -1\n\ts_waitcnt vmcnt(0)"
                 : "+v"(A[0]), "+v"(A[1]), "+v"(A[2]), "+v"(A[3])
                 : "v"(o), "s"(rs), "s"(st), "s"(2 * st), "s"(3 * st), "s"(m[0]), "s"(m[1]), "s"(m[2]), "s"(m[3]) : "memory");
  }
}
__device__ __forceinline__ void poll1(u32x4 rs, u32x4& A, unsigned o) {
#pragma nounroll
  for (int spin = 0; spin < SPIN_G; ++spin) {
    const u64 m = __ballot(pend4(A));
    if (m == 0) break;
    poll_round1(rs, A, o, m);
  }
}

#define FK_END() asm volatile("s_endpgm" ::: "memory")

__global__ void __launch_bounds__(NTHR, 4)
fused_kernel(const unsigned char* wq_, const unsigned char* kc_, unsigned char* ws_, const unsigned char* x_,
             const unsigned char* __restrict__ vc, const unsigned char* __restrict__ wo, const unsigned char* __restrict__ wg,
             const unsigned char* __restrict__ wu, const unsigned char* __restrict__ wd, unsigned short* __restrict__ out) {
  typedef unsigned long long u64x4 __attribute__((ext_vector_type(4)));
  u64x4 ka;
  asm volatile(".Lfk_start:\n\ts_load_dwordx8 %0, %1, 0x0" : "=s"(ka) : "s"(__builtin_amdgcn_kernarg_segment_ptr()) : "memory");
  extern __shared__ __attribute__((aligned(16))) unsigned char smem[];
  const unsigned L0 = (unsigned)(uintptr_t)(LAS unsigned char*)smem;
  const int tid = threadIdx.x, lane = tid & 63, wave0 = __builtin_amdgcn_readfirstlane(tid >> 6);
  const int w = blockIdx.x, kvh = w & 7, idx = w >> 3, bat = idx >> 4, spl = idx & 15;
  const int n16 = lane & 15, g4 = lane >> 4;
  const int rq = idx & 3, cbk = idx >> 2;
  const size_t chunk = ((size_t)(bat * 8 + kvh) * 8192 + spl * 512) * 256;
  const int kw = wave0 - 1;
  const bool isk = (unsigned)kw < 4u;
  if ((w & 56) == 0) {
    u64 pc;
    unsigned o;
    asm volatile("s_getpc_b64 %0\n\t.Lfk_h2:\n\tv_lshlrev_b32 %1, 5, %2\n\tv_min_u32 %1, .Lfk_ke-.Lfk_h2-4, %1\n\ts_mov_b32 m0, %3\n\ts_nop 0\n\t"
                 "global_load_lds_dword %1, %0" : "=&s"(pc), "=&v"(o) : "v"(tid), "s"(L0 + L_SCR) : "memory");
  }
  asm volatile("s_waitcnt lgkmcnt(0)" : "+s"(ka)::"memory");
  const unsigned char* wq = (const unsigned char*)ka[0];
  const unsigned char* kc = (const unsigned char*)ka[1];
  unsigned char* ws = (unsigned char*)ka[2];
  const unsigned char* x = (const unsigned char*)ka[3];
  const unsigned LK = L0 + kw * 32768;
  const unsigned char* kb = kc + chunk + kw * 8192;
#define KVODEF()                  \
  int lane_k = lane;              \
  asm volatile("" : "+v"(lane_k)); \
  unsigned kvo[4];                \
  _Pragma("unroll") for (int a = 0; a < 4; ++a) kvo[a] = (lane_k >> 4) * 256 + (((lane_k & 15) ^ (4 * (lane_k >> 4) + a)) * 16)
  if (isk) {
    const unsigned v0 = (lane >> 3) * 8192 + (lane & 7) * 16;
    const unsigned wv[4] = {v0, (v0 ^ 32) + 64512, v0 + 2 * 64512, (v0 ^ 32) + 3 * 64512};
    dma4_nt<0>(wq + (size_t)(rq * 128 + kw * 32) * 8192 + (kvh * 512 + cbk * 64) * 2, wv, L0 + L_WQ + kw * 4096);
    KVODEF();
    dma4_nt<0>(kb + rq * 32768, kvo, LK + rq * 8192);
  }
  const int prod = (bat * 8 + kvh) * 16 + spl;
  const u32x4 rs = {(unsigned)(uintptr_t)ws, (unsigned)((uintptr_t)ws >> 32) & 0xffffu, (unsigned)WS_END, 0x00020000u};
  unsigned tlo = 0, thi = 0;
  if (wave0 == 0) {
    __builtin_amdgcn_s_setprio(3);
    u32x2 ep;
    asm volatile("global_load_dwordx2 %0, %1, %2 sc1" : "=v"(ep) : "v"(0u), "s"(ws) : "memory");
    asm volatile("s_mov_b32 m0, %2\n\ts_nop 0\n\tglobal_load_lds_dwordx4 %0, %1\n\tglobal_load_lds_dwordx4 %0, %1 offset:1024" ::"v"(lane * 16), "s"(x), "s"(L0 + L_XS) : "memory");
    if (lane < 16) lds_st<unsigned>(L0 + L_FLAGS + lane * 4, 0u);
    asm volatile("s_waitcnt lgkmcnt(0)\n\ts_barrier" ::: "memory");
    const u32x4 s4 = {SENT, SENT, SENT, SENT};
    const int OOB = 0x7ffffff0;
    int o1 = OOB;
    if (lane < 2) o1 = WS_ST + (kvh * 32 + idx) * 32 + lane * 16;
    const int o2 = (idx >= 16 && idx < 25) ? WS_YP + (kvh * 9 + idx - 16) * 2048 + lane * 16 : OOB;
    asm volatile("buffer_store_dwordx4 %1, %2, %5, 0 offen sc1\n\t"
                 "buffer_store_dwordx4 %1, %3, %5, 0 offen sc1\n\t"
                 "buffer_store_dwordx4 %1, %4, %5, 0 offen sc1\n\t"
                 "buffer_store_dwordx4 %1, %4, %5, 0 offen offset:1024 sc1\n\ts_waitcnt vmcnt(4)"
                 : "+v"(ep) : "v"(s4), "v"(WS_OP + prod * 1024 + lane * 16), "v"(o1), "v"(o2), "s"(rs) : "memory");
    const u64 tag = (((u64)ep[1] << 32) | ep[0]) + 1;
    tlo = __builtin_amdgcn_readfirstlane((unsigned)tag);
    thi = __builtin_amdgcn_readfirstlane((unsigned)(tag >> 32));
  } else {
    asm volatile("s_barrier" ::: "memory");
  }
  const int nC = 8 - 2 * (kvh >> 2), cc0 = 8 * kvh - 2 * (kvh >> 2) * (kvh - 4), ri = idx - nC;
  const int role = (ri < 0) + 2 * (ri >= 16) + (ri >= 20);
  const int tk = cc0 + idx;
  const int rk = 2 * kvh + (ri >> 3), cq = ri & 7;
  const __amdgpu_buffer_rsrc_t rsb = __builtin_amdgcn_make_buffer_rsrc(ws, 0, WS_END, 0x00020000);
#define TRDEF()                   \
  int lane_ = lane;               \
  asm volatile("" : "+v"(lane_)); \
  const unsigned trq = (lane_ >> 2) & 3, trp = lane_ & 3
  const u32x4 ones = {ONE2, ONE2, ONE2, ONE2};

  if (isk) {
    __builtin_amdgcn_s_setprio(2);
    KVODEF();
    waitvm<4>();
    flag_add(L0, F_WQ);
#pragma nounroll
    for (int t = 1; t < 8; ++t) {
      const int tl = (t + 2 * rq) & 7;
      dma4_nt<KW>(kb + (tl >> 1) * 32768 + (tl & 1) * 4096, kvo, LK + tl * 4096);
    }
    flag_add(L0, F_KISS);
    flag_wait<1>(L0, F_Q, 1u);
    u32x4 qf[4];
#pragma unroll
    for (int t = 0; t < 4; ++t) qf[t] = lds_ld<u32x4>(L0 + L_Q + (n16 & 3) * 256 + t * 64 + g4 * 16);
    f32x4 sel[2];
    const unsigned krow = LK + (4 * (n16 & 3) + (n16 >> 2)) * 256;
#pragma unroll
    for (int hf = 0; hf < 2; ++hf) {
      if (hf == 1) waitvm<0>();
      f32x4 S[4];
#pragma unroll
      for (int T = 0; T < 4; ++T) {
        S[T] = f32x4{0.f, 0.f, 0.f, 0.f};
#pragma unroll
        for (int t = 0; t < 4; ++t) S[T] = mfma(lds_ld<u32x4>(krow + ((((2 * hf + (T >> 1) + rq) & 3) * 2 + (T & 1)) * 4096) + (((4 * t + g4) ^ n16) * 16)), qf[t], S[T]);
      }
      const f32x4 lo = (n16 & 4) ? S[1] : S[0], hi = (n16 & 4) ? S[3] : S[2];
      sel[hf] = (n16 & 8) ? hi : lo;
    }
    float m = fmaxf(fmaxf(fmaxf(sel[0][0], sel[0][1]), fmaxf(sel[0][2], sel[0][3])), fmaxf(fmaxf(sel[1][0], sel[1][1]), fmaxf(sel[1][2], sel[1][3])));
    m = fmaxf(m, dpp<ROR4>(m)); m = fmaxf(m, dpp<ROR8>(m));
    m = x32_max(x16_max(m));
    const float ms = m * QSCALE;
    float l = 0.f;
#pragma unroll
    for (int hf = 0; hf < 2; ++hf) {
      u32x4 pp;
#pragma unroll
      for (int r = 0; r < 4; ++r) {
        const float p = __builtin_amdgcn_exp2f(fmaf(sel[hf][r], QSCALE, -ms));
        pp[r] = pk(p, p);
        l += bfhi(pp[r]);
      }
      lds_st<u32x4>(L0 + L_P + (2 * kw + ((n16 >> 2) & 1)) * 1024 + (((n16 & 3) * 4 + g4) * 16 + 4 * (2 * hf + (n16 >> 3))) * 4, pp);
    }
    flag_set(L0, F_P0 + kw, 1u);
    l += dpp<ROR4>(l); l += dpp<ROR8>(l);
    l = x32_sum(x16_sum(l));
    if (lane < 4) {
      lds_st<float>(L0 + L_WM + (kw * 4 + lane) * 4, ms);
      lds_st<float>(L0 + L_WL + (kw * 4 + lane) * 4, l);
    }
    flag_add(L0, F_STAT);
    __builtin_amdgcn_s_setprio(0);
    if (role == 0) {
      const unsigned char* base = wo + (size_t)(rk * 256 + kw * 64) * 1024 + cq * 128;
      const unsigned v0 = (lane >> 3) * 1024 + (((lane & 7) ^ (g4 << 1)) * 16);
      const unsigned wv[4] = {v0, v0 + 7168, v0 + 2 * 7168, v0 + 3 * 7168};
      flag_wait<1>(L0, F_VISS, 8u);
      dma4_nt<0>(base, wv, LK);
      dma4_nt<0>(base + 32768, wv, LK + 4096);
      waitvm<4>();
      flag_set(L0, F_P0 + kw, 2u);
      waitvm<0>();
      flag_set(L0, F_P0 + kw, 3u);
    } else if (role == 1) {
      const unsigned va = (lane >> 2) * 3584u + (lane & 3) * 16u, vbb = ((lane >> 2) ^ 1) * 3584u + (lane & 3) * 16u;
      const unsigned wv[4] = {va, vbb + 56320, va + 2 * 56320, vbb + 3 * 56320};
      const unsigned v2 = (lane >> 3) * 1024 + (lane & 7) * 16;
      const unsigned dv[4] = {v2, (v2 ^ 32) + 7168, v2 + 2 * 7168, (v2 ^ 32) + 3 * 7168};
      const size_t go = (size_t)kw * 128 * 3584 + tk * 64;
      const unsigned char* bd = wd + (size_t)tk * 32 * 1024 + kw * 256;
      const int h0 = tk & 1, h1 = h0 ^ 1;
      flag_wait<1>(L0, F_S2, 2u);
      dma4_nt<0>(bd, dv, LK + 8192);
      dma4_nt<0>(bd + 128, dv, LK + 16384 + 8192);
      dma4_nt<0>(wg + go + h0 * 229376, wv, LK + h0 * 16384);
      dma4_nt<0>(wu + go + h0 * 229376, wv, LK + h0 * 16384 + 4096);
      dma4_nt<0>(wg + go + h1 * 229376, wv, LK + h1 * 16384);
      dma4_nt<0>(wu + go + h1 * 229376, wv, LK + h1 * 16384 + 4096);
      waitvm<8>();
      flag_set(L0, F_P0 + kw, 2u);
      waitvm<0>();
      flag_set(L0, F_P0 + kw, 3u);
    } else {
      flag_wait<1>(L0, F_S2, 2u);
#pragma nounroll
      for (int u = (ri - 16) * 4 + kw; u < 64; u += (16 - nC) * 4) {
        const int row = ((u >> 2) & 3) * 128 + ((u & 1) * 4 + 3 - (u >> 4)) * 16;
        if (2 * (lane & 3) < nC) touch(((u & 2) ? wu : wg) + (size_t)row * 3584 + cc0 * 64, (lane >> 2) * 3584 + (lane & 3) * 128, L0 + L_SCR);
      }
      waitvm<0>();
    }
    asm volatile(".Lfk_ke:" ::: "memory");
    return;
  }

  if (wave0 == 0) {
    const u32x4 xs0 = lds_ld<u32x4>(L0 + L_XS + lane * 16), xs1 = lds_ld<u32x4>(L0 + L_XS + 1024 + lane * 16);
    u32x4 xa[4];
#pragma unroll
    for (int t = 0; t < 4; ++t) xa[t] = lds_ld<u32x4>(L0 + L_XS + (n16 & 1) * 1024 + rq * 256 + t * 64 + g4 * 16);
    float mu, rr;
    {
      float s0 = 0.f, q0 = 0.f, s1 = 0.f, q1 = 0.f;
#pragma unroll
      for (int j = 0; j < 4; ++j) {
        const float a = bflo(xs0[j]), b = bfhi(xs0[j]), c = bflo(xs1[j]), d = bfhi(xs1[j]);
        s0 += a + b; q0 += a * a + b * b; s1 += c + d; q1 += c * c + d * d;
      }
      s0 = wave_sum(s0); q0 = wave_sum(q0); s1 = wave_sum(s1); q1 = wave_sum(q1);
      const float s = (lane & 1) ? s1 : s0, q = (lane & 1) ? q1 : q0;
      mu = s * (1.f / 512.f);
      rr = 1.f / sqrtf(fmaxf(q * (1.f / 512.f) - mu * mu, 0.f) + 1e-5f);
    }
#pragma unroll
    for (int t = 0; t < 4; ++t) xa[t] = (n16 & 3) == 2 ? ones : xa[t];
    flag_wait<0>(L0, F_WQ, 4u);
    {
      f32x4 d[4];
      TRDEF();
      const unsigned a0 = L0 + L_WQ + (8 * g4 + trq) * 128 + trp * 8, sw = (g4 & 1) * 32;
#pragma unroll
      for (int nt = 0; nt < 4; ++nt) {
        d[nt] = f32x4{0.f, 0.f, 0.f, 0.f};
#pragma unroll
        for (int t = 0; t < 4; ++t) d[nt] = mfma(trfrag(a0 + t * 4096 + ((nt * 32) ^ sw), a0 + t * 4096 + ((nt * 32) ^ sw) + 512), xa[t], d[nt]);
      }
      const f32x4 lo = (n16 & 4) ? d[1] : d[0], hi = (n16 & 4) ? d[3] : d[2];
      const f32x4 dd = (n16 & 8) ? hi : lo;
      float qv[4];
#pragma unroll
      for (int r = 0; r < 4; ++r) qv[r] = rr * (dd[r] - mu * dpp<0xAA>(dd[r]));
      if ((n16 & 3) < 2)
        __builtin_amdgcn_raw_buffer_store_b128(u32x4{tlo, thi, pk(qv[0], qv[1]), pk(qv[2], qv[3])}, rsb,
                                               WS_QP + (((kvh * 2 + (n16 & 1)) * 4 + rq) * 128 + cbk * 16 + (n16 >> 2) * 4 + g4) * 16, 0, 16);
    }
    waitvm<1>();
    if (lane == 0) __builtin_amdgcn_raw_buffer_store_b64(u32x2{tlo, thi}, rsb, WS_FL + w * 8, 0, 16);
    {
      u32x4 v[8];
      const unsigned ob = WS_QP + (kvh * 2 + bat) * 8192 + lane * 16;
#pragma unroll
      for (int i = 0; i < 8; ++i) v[i] = u32x4{~tlo, 0u, 0u, 0u};
      __builtin_amdgcn_s_sleep(QDELAY);
#pragma nounroll
      for (int spin = 0; spin < SPIN_G; ++spin) {
        u64 m[8];
#pragma unroll
        for (int i = 0; i < 8; ++i) m[i] = __ballot(v[i][0] != tlo || v[i][1] != thi);
        if ((m[0] | m[1] | m[2] | m[3] | m[4] | m[5] | m[6] | m[7]) == 0) break;
        poll_round8(rs, v, ob, m);
      }
#pragma unroll
      for (int h = 0; h < 2; ++h) {
        u32x2 r;
#pragma unroll
        for (int j = 0; j < 2; ++j) {
          const float lo = (bflo(v[h][2 + j]) + bflo(v[2 + h][2 + j])) + (bflo(v[4 + h][2 + j]) + bflo(v[6 + h][2 + j]));
          const float hi = (bfhi(v[h][2 + j]) + bfhi(v[2 + h][2 + j])) + (bfhi(v[4 + h][2 + j]) + bfhi(v[6 + h][2 + j]));
          r[j] = pk(lo, hi);
        }
        lds_st<u32x2>(L0 + L_Q + (64 * h + lane) * 8, r);
      }
      flag_set(L0, F_Q, 1u);
    }
#pragma nounroll
    for (int spin = 0; spin < SPIN_G; ++spin) {
      asm volatile("" ::: "memory");
      const u32x4 f0 = __builtin_amdgcn_raw_buffer_load_b128(rsb, WS_FL + lane * 16, 0, 16);
      const u32x4 f1 = __builtin_amdgcn_raw_buffer_load_b128(rsb, WS_FL + 1024 + lane * 16, 0, 16);
      const bool ok = f0[0] == tlo && f0[1] == thi && f0[2] == tlo && f0[3] == thi && f1[0] == tlo && f1[1] == thi && f1[2] == tlo && f1[3] == thi;
      if (__all(ok)) break;
    }
    flag_set(L0, F_ALL, 1u);
    if (w == NWG - 1 && lane == 0) __hip_atomic_store((GAS u64*)(ws + WS_EPOCH), ((u64)thi << 32) | tlo, RLX_AGENT);
    flag_wait<1>(L0, F_STAT, 4u);
    {
      const float m = lds_ld<float>(L0 + L_WM + n16 * 4), ls = lds_ld<float>(L0 + L_WL + n16 * 4);
      float M = fmaxf(m, dpp<ROR4>(m)); M = fmaxf(M, dpp<ROR8>(M));
      const float f = __builtin_amdgcn_exp2f(m - M);
      float L = f * ls;
      L += dpp<ROR4>(L); L += dpp<ROR8>(L);
      if (lane < 16) lds_st<float>(L0 + L_F + lane * 4, f);
      flag_set(L0, F_FAC, 1u);
      if (lane < 4)
        __builtin_amdgcn_raw_buffer_store_b64(u32x2{nosent(__float_as_uint(M)), nosent(__float_as_uint(L))}, rsb, WS_ST + (kvh * 32 + idx) * 32 + lane * 8, 0, 16);
    }
    if (role == 0) {
      u32x4 st = {SENT, SENT, SENT, SENT};
      flag_wait<1>(L0, F_VISS, 4u);
      poll1(rs, st, WS_ST + kvh * 1024 + lane * 16);
      const float m0 = __uint_as_float(st[0]), l0 = __uint_as_float(st[1]), m1 = __uint_as_float(st[2]), l1 = __uint_as_float(st[3]);
      float M0 = m0, M1 = m1;
#pragma unroll
      for (int o2 = 2; o2 <= 16; o2 <<= 1) { M0 = fmaxf(M0, __shfl_xor(M0, o2)); M1 = fmaxf(M1, __shfl_xor(M1, o2)); }
      const float e0 = __builtin_amdgcn_exp2f(m0 - M0), e1 = __builtin_amdgcn_exp2f(m1 - M1);
      float S0 = e0 * l0, S1 = e1 * l1;
#pragma unroll
      for (int o2 = 2; o2 <= 16; o2 <<= 1) { S0 += __shfl_xor(S0, o2); S1 += __shfl_xor(S1, o2); }
      const int bb = lane >> 5, ss = (lane >> 1) & 15, h0 = (lane & 1) * 2;
      lds_st<float>(L0 + L_W + ((bb * 4 + h0) * 16 + ss) * 4, e0 / S0);
      lds_st<float>(L0 + L_W + ((bb * 4 + h0 + 1) * 16 + ss) * 4, e1 / S1);
      flag_set(L0, F_W, 1u);
    }
    return;
  }

  const int wave = wave0 - 5;
  const unsigned LW = L0 + wave * 16384;
  const unsigned char* vb = vc + chunk + wave * 4096;
  __builtin_amdgcn_s_setprio(2);
  if (wave == 0 && lane < 2) touch(vb, lane * 65536, L0 + L_SCR);
  u32x4 msk;
  {
    const unsigned hm = (lane & 1) ? 0xffff0000u : 0x0000ffffu;
    const int dsel = (lane & 7) >> 1;
    msk = u32x4{dsel == 0 ? hm : 0u, dsel == 1 ? hm : 0u, dsel == 2 ? hm : 0u, dsel == 3 ? hm : 0u};
  }
  u32x4 V[16];
  const unsigned vo = lane * 16;
  flag_wait<1>(L0, F_KISS, 4u);
#define LDV(i) do { if ((i) >= VW) waitvm<VW - 1>(); ldv_nt<((i) & 3) * 1024>(V[i], vb + ((((i) >> 2) + rq) & 3) * 32768, vo); } while (0)
  LDV(0); LDV(1); LDV(2); LDV(3); LDV(4); LDV(5); LDV(6); LDV(7); LDV(8); LDV(9); LDV(10); LDV(11);
  flag_wait<1>(L0, F_P0 + (wave >> 1), 1u);
  u32x4 P[2][4];
#pragma unroll
  for (int u = 0; u < 2; ++u)
#pragma unroll
    for (int k = 0; k < 4; ++k) P[u][k] = lds_ld<u32x4>(L0 + L_P + wave * 1024 + ((2 * u + (n16 >> 3)) * 4 + g4) * 64 + k * 16);
  f32x4 acc[2] = {{0.f, 0.f, 0.f, 0.f}, {0.f, 0.f, 0.f, 0.f}};
#define PV(j, N)                                                                        \
  do {                                                                                  \
    asm volatile("s_waitcnt vmcnt(%1)" : "+v"(V[j]) : "i"(N) : "memory");               \
    const u32x4 b0 = msk & P[0][(j) >> 2][(j) & 3], b1 = msk & P[1][(j) >> 2][(j) & 3]; \
    acc[0] = mfma(V[j], b0, acc[0]);                                                    \
    acc[1] = mfma(V[j], b1, acc[1]);                                                    \
    __builtin_amdgcn_sched_barrier(0);                                                  \
  } while (0)
  LDV(12); PV(0, 12); PV(1, 11);
  LDV(13); PV(2, 11); PV(3, 10);
  LDV(14); PV(4, 10); PV(5, 9);
  LDV(15); flag_add(L0, F_VISS); PV(6, 9); PV(7, 8);
  asm volatile("" : "+v"(acc[0]), "+v"(acc[1]));
  flag_wait<0>(L0, F_FAC, 1u);
  unsigned oa = L0 + L_RB_O + wave * 1024 + lane * 16, og = WS_OP + prod * 1024 + (2 * wave + (n16 >> 3)) * 256 + g4 * 64 + (lane & 7) * 8;
  asm volatile("" : "+v"(oa), "+v"(og));
  const float f0 = lds_ld<float>(L0 + L_F + ((wave >> 1) * 4 + (n16 >> 3)) * 4), f1 = lds_ld<float>(L0 + L_F + ((wave >> 1) * 4 + 2 + (n16 >> 3)) * 4);
#pragma unroll
  for (int j = 8; j < 16; ++j) PV(j, 15 - j);
  lds_st<f32x4>(LW + L_RB_O + lane * 16, acc[0] * f0);
  lds_st<f32x4>(LW + L_RB_O + 1024 + lane * 16, acc[1] * f1);
  flag_add(L0, F_S1);
  if (wave < 2) {
    flag_wait<0>(L0, F_S1, 8u);
    f32x4 r[8];
    lds_ld8_b128<16384>(r, oa, oa + 65536);
    const f32x4 s = ((r[0] + r[1]) + (r[2] + r[3])) + ((r[4] + r[5]) + (r[6] + r[7]));
    const unsigned s0 = nosent(pk(s[0], s[1])), s1 = nosent(pk(s[2], s[3]));
    const u32x4 sv = {s0, s1, (unsigned)__builtin_amdgcn_mov_dpp((int)s0, XOR1, 0xf, 0xf, true), (unsigned)__builtin_amdgcn_mov_dpp((int)s1, XOR1, 0xf, 0xf, true)};
    asm volatile("s_mov_b64 exec, %3\n\tbuffer_store_dwordx4 %0, %1, %2, 0 offen sc1\n\ts_mov_b64 exec, -1" ::"v"(sv), "v"(og), "s"(rs), "s"(0x5555555555555555ull) : "memory");
    flag_add(L0, F_S2);
  }
  if (role == 3) return;
  const u32x4 sel = msk & ones;
  TRDEF();

  if (role == 0) {
    const int kq = wave >> 1, bb = wave & 1;
    const int hd = 2 * (rk & 1) + (kq >> 1);
    const unsigned LB = L0 + kq * 32768;
    u32x4 A0, A1;
    const unsigned o0 = WS_OP + ((bb * 8 + (rk >> 1)) * 16 + (lane >> 3)) * 1024 + hd * 256 + (kq & 1) * 128 + (lane & 7) * 16;
    f32x4 xr0 = {0.f, 0.f, 0.f, 0.f}, xr1 = {0.f, 0.f, 0.f, 0.f};
    if (wave == 0 && rk == 0) {
#pragma unroll
      for (int j = 0; j < 4; ++j) {
        xr0[j] = bflo(lds_ld<unsigned short>(L0 + L_XS + (cq * 64 + j * 16 + n16) * 2));
        xr1[j] = bflo(lds_ld<unsigned short>(L0 + L_XS + (512 + cq * 64 + j * 16 + n16) * 2));
      }
    }
    flag_wait<0>(L0, F_W, 1u);
    const float wa = lds_ld<float>(L0 + L_W + ((bb * 4 + hd) * 16 + 2 * g4 + (n16 >> 3)) * 4), wb = lds_ld<float>(L0 + L_W + ((bb * 4 + hd) * 16 + 8 + 2 * g4 + (n16 >> 3)) * 4);
    u32x4 sa = msk & pk(wa, wa), sb = msk & pk(wb, wb);
    unsigned sw = LW + 8192 + n16 * 16 + (g4 & 1) * 8, sr = LW + 8192 + (n16 & 1) * 128 + g4 * 16, xw = L0 + L_X + wave * 256 + n16 * 16, xa = L0 + L_X + n16 * 16,
             og2 = WS_YP + rk * 2048 + cq * 256 + n16 * 16;
    const u64 vmask = __ballot((g4 >> 1) == (n16 >> 3));
    asm volatile("" : "+v"(sw), "+v"(sr), "+v"(xw), "+v"(xa), "+v"(og2), "+v"(xr0), "+v"(xr1), "+v"(sa), "+v"(sb));
    u32x4 Bw[2][4];
#pragma unroll
    for (int t = 0; t < 2; ++t) {
      flag_wait<0>(L0, F_P0 + kq, 2u + t);
      if (t == 1)
        asm volatile("buffer_load_dwordx4 %0, %2, %3, 0 offen sc1\n\tbuffer_load_dwordx4 %1, %2, %3, %4 offen sc1"
                     : "=&v"(A0), "=&v"(A1) : "v"(o0), "s"(rs), "s"(8192u) : "memory");
#pragma unroll
      for (int nt = 0; nt < 4; ++nt) {
        const unsigned a = LB + (32 * t + 8 * trq + 2 * g4) * 128 + ((nt ^ g4) * 32) + trp * 8;
        Bw[t][nt] = trfrag(a, a + 128);
      }
    }
    asm volatile("s_waitcnt vmcnt(0)" : "+v"(A0), "+v"(A1)::"memory");
    poll2(rs, A0, A1, o0, 8192u);
    f32x4 d = mfma(sa, A0, f32x4{0.f, 0.f, 0.f, 0.f});
    d = mfma(sb, A1, d);
    asm volatile("s_mov_b64 exec, %2\n\tds_write_b64 %0, %1\n\ts_mov_b64 exec, -1" ::"v"(sw), "v"(u32x2{pk(d[0], d[1]), pk(d[2], d[3])}), "s"(vmask) : "memory");
    A0 = lds_ld<u32x4>(sr);
    A1 = lds_ld<u32x4>(sr + 64);
    f32x4 val;
#pragma unroll
    for (int nt = 0; nt < 4; ++nt) {
      f32x4 e = mfma(A0, Bw[0][nt], f32x4{0.f, 0.f, 0.f, 0.f});
      e = mfma(A1, Bw[1][nt], e);
      val[nt] = e[0] + e[1];
    }
    asm volatile("s_mov_b64 exec, 0xffff\n\tds_write_b128 %0, %1\n\ts_mov_b64 exec, -1" ::"v"(xw), "v"(val) : "memory");
    wg_barrier();
    if (wave == 0) {
      f32x4 r[8];
      lds_ld8_b128<256>(r, xa, xa + 1024);
      xr0 += (r[0] + r[2]) + (r[4] + r[6]);
      xr1 += (r[1] + r[3]) + (r[5] + r[7]);
      if (lane < 16)
        __builtin_amdgcn_raw_buffer_store_b128(u32x4{nosent(pk(xr0[0], xr0[1])), nosent(pk(xr0[2], xr0[3])), nosent(pk(xr1[0], xr1[1])), nosent(pk(xr1[2], xr1[3]))},
                                               rsb, og2, 0, 16);
      asm volatile(".Lfk_e0:" ::: "memory");
      FK_END();
    }
  } else if (role == 1) {
    u32x4 H[4];
    const unsigned o = WS_YP + g4 * 2048 + wave * 256 + n16 * 16;
    const unsigned LS = LW + 14336;
    lds_st<unsigned short>(LS + 256 + lane * 2, (unsigned short)0x3F80);
    unsigned xw = L0 + L_X + wave * 1024 + lane * 16, xa = L0 + L_X + lane * 16;
    unsigned hw = LS + lane * 8, hr = LS + ((n16 & 3) == 1 ? 256 : ((n16 >> 2) & 1) * 128) + g4 * 16, stw = L0 + L_P + wave * 1040 + g4 * 8,
             str = L0 + L_P + (n16 & 7) * 1040 + (g4 & 1) * 8, aw = LS + 384 + (g4 & 1) * 64 + (g4 >> 1) * 32 + n16 * 2, ar = LS + 384 + (n16 & 1) * 64 + g4 * 16,
             og2 = WS_YP + (16 + tk) * 2048 + wave * 256 + n16 * 16;
    asm volatile("" : "+v"(hw), "+v"(hr), "+v"(stw), "+v"(str), "+v"(aw), "+v"(ar), "+v"(og2), "+v"(xw), "+v"(xa));
    unsigned da = LW + 8192 + (8 * g4 + trq) * 128 + (g4 & 1) * 32 + trp * 8, db = da ^ 32;
    unsigned ga = LW + (16 * trq + 2 * g4 + (trq & 1)) * 64 + trp * 8, gb = ga ^ 64;
    asm volatile("" : "+v"(da), "+v"(db), "+v"(ga), "+v"(gb));
    flag_wait<1>(L0, F_ALL, 1u);
    flag_wait<1>(L0, F_P0 + (wave >> 1), 2u + ((wave ^ tk) & 1));
    issue4(rs, H, o, 8192u);
    u32x4 Bw[2][2][2], Bd[4];
#pragma unroll
    for (int mt = 0; mt < 2; ++mt)
#pragma unroll
      for (int t = 0; t < 2; ++t)
#pragma unroll
        for (int nt = 0; nt < 2; ++nt) Bw[mt][t][nt] = trfrag(ga + mt * 4096 + t * 512 + nt * 32, gb + mt * 4096 + t * 512 + nt * 32);
#pragma unroll
    for (int j = 0; j < 4; ++j) Bd[j] = trfrag(((j & 1) ? db : da) + (j >> 1) * 64, ((j & 1) ? db : da) + (j >> 1) * 64 + 512);
    land4(H);
    poll4(rs, H, o, 8192u);
    f32x4 h = mfma(sel, H[0], f32x4{0.f, 0.f, 0.f, 0.f}), h2 = mfma(sel, H[1], f32x4{0.f, 0.f, 0.f, 0.f});
    h = mfma(sel, H[2], h);
    h2 = mfma(sel, H[3], h2);
    h += h2;
    asm volatile("s_mov_b32 exec_hi, 0\n\tds_write_b64 %0, %1\n\ts_mov_b32 exec_hi, -1" ::"v"(hw), "v"(u32x2{pk(h[0], h[1]), pk(h[2], h[3])}) : "memory");
    const u32x4 A0 = lds_ld<u32x4>(hr), A1 = lds_ld<u32x4>(hr + 64);
    __builtin_amdgcn_sched_barrier(0);
    const float ss = row_sum16((h[0] + h[1]) + (h[2] + h[3])), gs = row_sum16((h[0] * h[0] + h[1] * h[1]) + (h[2] * h[2] + h[3] * h[3]));
    asm volatile("s_mov_b64 exec, 0x10001\n\tds_write_b64 %0, %1\n\ts_mov_b64 exec, -1" ::"v"(stw), "v"(f32x2{ss, gs}) : "memory");
    __builtin_amdgcn_sched_barrier(0);
    f32x4 e[2][2];
#pragma unroll
    for (int nt = 0; nt < 2; ++nt)
#pragma unroll
      for (int mt = 0; mt < 2; ++mt) {
        e[nt][mt] = mfma(A0, Bw[mt][0][nt], f32x4{0.f, 0.f, 0.f, 0.f});
        e[nt][mt] = mfma(A1, Bw[mt][1][nt], e[nt][mt]);
      }
    lds_st<f32x4>(xw, lane < 32 ? f32x4{e[0][0][0], e[0][1][0], e[0][0][1], e[0][1][1]} : f32x4{e[1][0][0], e[1][1][0], e[1][0][1], e[1][1][1]});
    wg_barrier();
    f32x2 st;
    f32x4 r[8];
    asm volatile("ds_read_b64 %8, %10\n\tds_read_b128 %0, %9\n\tds_read_b128 %1, %9 offset:1024\n\tds_read_b128 %2, %9 offset:2048\n\tds_read_b128 %3, %9 offset:3072\n\t"
                 "ds_read_b128 %4, %9 offset:4096\n\tds_read_b128 %5, %9 offset:5120\n\tds_read_b128 %6, %9 offset:6144\n\tds_read_b128 %7, %9 offset:7168\n\t"
                 "s_waitcnt lgkmcnt(8)"
                 : "=&v"(r[0]), "=&v"(r[1]), "=&v"(r[2]), "=&v"(r[3]), "=&v"(r[4]), "=&v"(r[5]), "=&v"(r[6]), "=&v"(r[7]), "=&v"(st)
                 : "v"(xa), "v"(str) : "memory");
    st[0] += dpp<XOR1>(st[0]); st[1] += dpp<XOR1>(st[1]);
    st[0] += dpp<XOR2>(st[0]); st[1] += dpp<XOR2>(st[1]);
    st[0] += dpp<XOR7>(st[0]); st[1] += dpp<XOR7>(st[1]);
    float mu = st[0] * (1.f / 512.f);
    float rr = __builtin_amdgcn_rsqf(fmaxf(st[1] * (1.f / 512.f) - mu * mu, 0.f) + 1e-5f);
    asm volatile("s_waitcnt lgkmcnt(0)"
                 : "+v"(r[0]), "+v"(r[1]), "+v"(r[2]), "+v"(r[3]), "+v"(r[4]), "+v"(r[5]), "+v"(r[6]), "+v"(r[7]), "+v"(mu), "+v"(rr)::"memory");
    const f32x4 gu = ((r[0] + r[1]) + (r[2] + r[3])) + ((r[4] + r[5]) + (r[6] + r[7]));
    const float gg = rr * (gu[0] - mu * gu[2]), uu = rr * (gu[1] - mu * gu[3]);
    const float a = gg * uu * __builtin_amdgcn_rcpf(1.f + __builtin_amdgcn_exp2f(-1.4426950408889634f * gg));
    lds_st<__bf16>(aw, (__bf16)a);
    const u32x4 Aa = lds_ld<u32x4>(ar);
    f32x4 y[4];
#pragma unroll
    for (int j = 0; j < 4; ++j) y[j] = mfma(Aa, Bd[j], f32x4{0.f, 0.f, 0.f, 0.f});
    if (lane < 16)
      __builtin_amdgcn_raw_buffer_store_b128(
          u32x4{nosent(pk(y[0][0], y[1][0])), nosent(pk(y[2][0], y[3][0])), nosent(pk(y[0][1], y[1][1])), nosent(pk(y[2][1], y[3][1]))}, rsb, og2, 0, 16);
    asm volatile(".Lfk_e1:" ::: "memory");
    FK_END();
  } else if (wave == 2) {
    const int ee = 4 * kvh + ri - 16;
    u32x4 Y0 = {SENT, SENT, SENT, SENT}, Y[4];
    const unsigned ob = WS_YP + (lane >> 2) * 2048 + ee * 64 + (lane & 3) * 16;
#pragma unroll
    for (int i = 0; i < 4; ++i) Y[i] = (i < 3 || lane < 32) ? u32x4{SENT, SENT, SENT, SENT} : u32x4{0u, 0u, 0u, 0u};
    flag_wait<1>(L0, F_ALL, 1u);
#pragma nounroll
    for (int spin = 0; spin < SPIN_G; ++spin) {
      const u64 m = __ballot(pend4(Y0));
      if (m == 0) break;
      __builtin_amdgcn_s_sleep(4);
      poll_round1(rs, Y0, ob, m);
    }
    f32x4 d0 = mfma(sel, Y0, f32x4{0.f, 0.f, 0.f, 0.f});
    poll4(rs, Y, ob + 32768u, 32768u);
    f32x4 d1 = mfma(sel, Y[1], f32x4{0.f, 0.f, 0.f, 0.f});
    d0 = mfma(sel, Y[0], d0);
    d1 = mfma(sel, Y[3], d1);
    d0 = mfma(sel, Y[2], d0);
    f32x4 d = d0 + d1;
#pragma unroll
    for (int r = 0; r < 4; ++r) { d[r] += dpp<ROR4>(d[r]); d[r] += dpp<ROR8>(d[r]); }
    const float lo = (n16 & 4) ? d[1] : d[0], hi = (n16 & 4) ? d[3] : d[2];
    const float res = (n16 & 8) ? hi : lo;
    const float nb = dpp<XOR1>(res);
    if (lane < 32 && (lane & 1) == 0)
      *(unsigned*)(out + g4 * 512 + 64 * (ee >> 2) + 16 * (n16 >> 2) + 4 * (ee & 3) + (n16 & 3)) = pk(res, nb);
    asm volatile(".Lfk_e2:" ::: "memory");
    FK_END();
  }
  asm volatile(".Lfk_te:" ::: "memory");
}
}

extern "C" void kernel_launch(void* const* d_in, const int* in_sizes, int n_in, void* d_out, int out_size, void* d_ws, size_t ws_size,
                              hipStream_t stream) {
  (void)in_sizes; (void)n_in; (void)out_size; (void)ws_size;
  (void)hipFuncSetAttribute((const void*)fk::fused_kernel, hipFuncAttributeMaxDynamicSharedMemorySize, fk::L_END);
  fk::fused_kernel<<<fk::NWG, fk::NTHR, fk::L_END, stream>>>(
      (const unsigned char*)d_in[3], (const unsigned char*)d_in[1], (unsigned char*)d_ws, (const unsigned char*)d_in[0],
      (const unsigned char*)d_in[2], (const unsigned char*)d_in[4], (const unsigned char*)d_in[6], (const unsigned char*)d_in[7],
      (const unsigned char*)d_in[8], (unsigned short*)d_out);
}
```
